```python
import torch

def _softmax(x, axis=-1):
    return torch.softmax(x, dim=axis)
def _gelu(x):
    return torch.nn.functional.gelu(x, approximate="tanh")
def _silu(x):
    return torch.nn.functional.silu(x)
def _sigmoid(x):
    return torch.sigmoid(x)
def _layernorm(x, axis=-1, eps=1e-5):
    m = x.mean(dim=axis, keepdim=True)
    v = x.var(dim=axis, keepdim=True, unbiased=False)
    return (x - m) / torch.sqrt(v + eps)
def _scatter_add(tgt, idx, src, axis=0):
    return tgt.clone().index_add_(axis, idx, src)
def _segment_sum(src, seg, n):
    out = torch.zeros((n,) + src.shape[1:], dtype=src.dtype)
    return out.index_add_(0, seg, src)
def _pad(x, pw):
    flat = tuple(v for pair in reversed(pw) for v in pair)
    return torch.nn.functional.pad(x, flat)

kernel_name = 'parallel_sum__spectral_gfnet__glu_gated_geglu__prompt'

def setup_inputs():

    _w = (lambda a: (lambda t: t.to(torch.bfloat16) if t.is_floating_point() else t)(torch.as_tensor(a)))
    def _A_setup():
        B, S, D = 1, 2048, 448
        Df = D // 2 + 1
        return {
            "x": _w((lambda *s: torch.randn(*s, dtype=torch.bfloat16) * 0.05)(B, S, D)),
            "w_r": _w((lambda *s: torch.randn(*s, dtype=torch.bfloat16) * 0.8)(S, Df) * 0.02),
            "w_i": _w((lambda *s: torch.randn(*s, dtype=torch.bfloat16) * 0.8)(S, Df) * 0.02),
        }
    _A_in = _A_setup()

    def _B_setup():
        B, S, Dm, Df = 1, 2048, 448, 1792
        return {
            "x": _w((lambda *s: torch.randn(*s, dtype=torch.bfloat16) * 0.05)(B, S, Dm)),
            "w_gate": _w((lambda *s: torch.randn(*s, dtype=torch.bfloat16) * 0.4)(Dm, Df) * (Dm ** -0.5)),
            "w_up": _w((lambda *s: torch.randn(*s, dtype=torch.bfloat16) * 0.4)(Dm, Df) * (Dm ** -0.5)),
            "w_down": _w((lambda *s: torch.randn(*s, dtype=torch.bfloat16) * 0.4)(Df, Dm) * (Df ** -0.5)),
        }
    _B_in = _B_setup()

    return {
        "A_x": _A_in["x"],
        "A_w_r": _A_in["w_r"],
        "A_w_i": _A_in["w_i"],
        "B_x": _B_in["x"],
        "B_w_gate": _B_in["w_gate"],
        "B_w_up": _B_in["w_up"],
        "B_w_down": _B_in["w_down"],
    }

def reference(A_x, A_w_r, A_w_i, B_x, B_w_gate, B_w_up, B_w_down):
    def _A_ref(x, w_r, w_i):
        B, S, D = x.shape
        X = (lambda x, axes=(-2,-1): torch.fft.rfft2(x.float(), dim=axes))(x, axes=(-2, -1))
        W = w_r + 1j * w_i
        X = X * W
        y = (lambda x, s, axes=(-2,-1): torch.fft.irfft2(x, s=s, dim=axes).to(torch.bfloat16))(X, (S, D), axes=(-2, -1))
        return y

    def _B_ref(x, w_gate, w_up, w_down):
        g = torch.einsum("bsd,df->bsf", x, w_gate)
        u = torch.einsum("bsd,df->bsf", x, w_up)
        h = _gelu(g) * u
        return torch.einsum("bsf,fd->bsd", h, w_down)

    x = A_x
    xn = _layernorm(x, axis=-1)
    return x + _A_ref(xn, A_w_r, A_w_i) + _B_ref(xn, B_w_gate, B_w_up, B_w_down)
```

```cpp
#include <hip/hip_runtime.h>
#include <hip/hip_bf16.h>
#include <stdint.h>

struct __attribute__((aligned(8))) cf { float x, y; };
__device__ const cf T448A[384] = {
 {1.0f,0.f},{0.999901652f,-0.0140245073f},{0.999606609f,-0.0280462559f},{0.99911499f,-0.0420624875f},
 {0.998426795f,-0.056070447f},{0.997542262f,-0.0700673759f},{0.996461511f,-0.0840505213f},{0.99518472f,-0.0980171412f},
 {0.993712187f,-0.111964479f},{0.99204421f,-0.125889793f},{0.990181148f,-0.139790341f},{0.988123238f,-0.153663397f},
 {0.985871017f,-0.167506218f},{0.983424842f,-0.181316108f},{0.980785251f,-0.195090324f},{0.977952778f,-0.208826169f},
 {0.974927902f,-0.222520933f},{0.971711278f,-0.236171931f},{0.968303502f,-0.249776483f},{0.964705288f,-0.26333189f},
 {0.960917294f,-0.276835501f},{0.956940353f,-0.290284663f},{0.952775121f,-0.303676754f},{0.948422492f,-0.317009091f},
 {0.943883359f,-0.330279052f},{0.939158499f,-0.343484074f},{0.934248924f,-0.356621534f},{0.929155588f,-0.369688839f},
 {0.923879504f,-0.382683426f},{0.918421745f,-0.395602763f},{0.912783265f,-0.408444256f},{0.906965256f,-0.421205431f},
 {0.90096885f,-0.433883727f},{0.894795239f,-0.446476698f},{0.888445616f,-0.458981872f},{0.881921291f,-0.471396744f},
 {0.875223398f,-0.483718902f},{0.868353426f,-0.495945901f},{0.861312628f,-0.508075356f},{0.854102433f,-0.520104885f},
 {0.846724212f,-0.532032073f},{0.839179456f,-0.543854654f},{0.831469595f,-0.555570245f},{0.823596239f,-0.567176521f},
 {0.815560877f,-0.578671277f},{0.80736506f,-0.590052247f},{0.799010456f,-0.601317108f},{0.790498734f,-0.612463653f},
 {0.781831503f,-0.623489797f},{0.773010433f,-0.634393275f},{0.764037371f,-0.645172f},{0.754913986f,-0.655823767f},
 {0.745642185f,-0.66634655f},{0.736223638f,-0.676738322f},{0.726660311f,-0.686996937f},{0.716954052f,-0.697120428f},
 {0.707106769f,-0.707106769f},{0.697120428f,-0.716954052f},{0.686996937f,-0.726660311f},{0.676738322f,-0.736223638f},
 {0.66634655f,-0.745642185f},{0.655823767f,-0.754913986f},{0.645172f,-0.764037371f},{0.634393275f,-0.773010433f},
 {1.0f,0.f},{0.999606609f,-0.0280462559f},{0.998426795f,-0.056070447f},{0.996461511f,-0.0840505213f},
 {0.993712187f,-0.111964479f},{0.990181148f,-0.139790341f},{0.985871017f,-0.167506218f},{0.980785251f,-0.195090324f},
 {0.974927902f,-0.222520933f},{0.968303502f,-0.249776483f},{0.960917294f,-0.276835501f},{0.952775121f,-0.303676754f},
 {0.943883359f,-0.330279052f},{0.934248924f,-0.356621534f},{0.923879504f,-0.382683426f},{0.912783265f,-0.408444256f},
 {0.90096885f,-0.433883727f},{0.888445616f,-0.458981872f},{0.875223398f,-0.483718902f},{0.861312628f,-0.508075356f},
 {0.846724212f,-0.532032073f},{0.831469595f,-0.555570245f},{0.815560877f,-0.578671277f},{0.799010456f,-0.601317108f},
 {0.781831503f,-0.623489797f},{0.764037371f,-0.645172f},{0.745642185f,-0.66634655f},{0.726660311f,-0.686996937f},
 {0.707106769f,-0.707106769f},{0.686996937f,-0.726660311f},{0.66634655f,-0.745642185f},{0.645172f,-0.764037371f},
 {0.623489797f,-0.781831503f},{0.601317108f,-0.799010456f},{0.578671277f,-0.815560877f},{0.555570245f,-0.831469595f},
 {0.532032073f,-0.846724212f},{0.508075356f,-0.861312628f},{0.483718902f,-0.875223398f},{0.458981872f,-0.888445616f},
 {0.433883727f,-0.90096885f},{0.408444256f,-0.912783265f},{0.382683426f,-0.923879504f},{0.356621534f,-0.934248924f},
 {0.330279052f,-0.943883359f},{0.303676754f,-0.952775121f},{0.276835501f,-0.960917294f},{0.249776483f,-0.968303502f},
 {0.222520933f,-0.974927902f},{0.195090324f,-0.980785251f},{0.167506218f,-0.985871017f},{0.139790341f,-0.990181148f},
 {0.111964479f,-0.993712187f},{0.0840505213f,-0.996461511f},{0.056070447f,-0.998426795f},{0.0280462559f,-0.999606609f},
 {6.12323426e-17f,-1.0f},{-0.0280462559f,-0.999606609f},{-0.056070447f,-0.998426795f},{-0.0840505213f,-0.996461511f},
 {-0.111964479f,-0.993712187f},{-0.139790341f,-0.990181148f},{-0.167506218f,-0.985871017f},{-0.195090324f,-0.980785251f},
 {1.0f,0.f},{0.99911499f,-0.0420624875f},{0.996461511f,-0.0840505213f},{0.99204421f,-0.125889793f},
 {0.985871017f,-0.167506218f},{0.977952778f,-0.208826169f},{0.968303502f,-0.249776483f},{0.956940353f,-0.290284663f},
 {0.943883359f,-0.330279052f},{0.929155588f,-0.369688839f},{0.912783265f,-0.408444256f},{0.894795239f,-0.446476698f},
 {0.875223398f,-0.483718902f},{0.854102433f,-0.520104885f},{0.831469595f,-0.555570245f},{0.80736506f,-0.590052247f},
 {0.781831503f,-0.623489797f},{0.754913986f,-0.655823767f},{0.726660311f,-0.686996937f},{0.697120428f,-0.716954052f},
 {0.66634655f,-0.745642185f},{0.634393275f,-0.773010433f},{0.601317108f,-0.799010456f},{0.567176521f,-0.823596239f},
 {0.532032073f,-0.846724212f},{0.495945901f,-0.868353426f},{0.458981872f,-0.888445616f},{0.421205431f,-0.906965256f},
 {0.382683426f,-0.923879504f},{0.343484074f,-0.939158499f},{0.303676754f,-0.952775121f},{0.26333189f,-0.964705288f},
 {0.222520933f,-0.974927902f},{0.181316108f,-0.983424842f},{0.139790341f,-0.990181148f},{0.0980171412f,-0.99518472f},
 {0.056070447f,-0.998426795f},{0.0140245073f,-0.999901652f},{-0.0280462559f,-0.999606609f},{-0.0700673759f,-0.997542262f},
 {-0.111964479f,-0.993712187f},{-0.153663397f,-0.988123238f},{-0.195090324f,-0.980785251f},{-0.236171931f,-0.971711278f},
 {-0.276835501f,-0.960917294f},{-0.317009091f,-0.948422492f},{-0.356621534f,-0.934248924f},{-0.395602763f,-0.918421745f},
 {-0.433883727f,-0.90096885f},{-0.471396744f,-0.881921291f},{-0.508075356f,-0.861312628f},{-0.543854654f,-0.839179456f},
 {-0.578671277f,-0.815560877f},{-0.612463653f,-0.790498734f},{-0.645172f,-0.764037371f},{-0.676738322f,-0.736223638f},
 {-0.707106769f,-0.707106769f},{-0.736223638f,-0.676738322f},{-0.764037371f,-0.645172f},{-0.790498734f,-0.612463653f},
 {-0.815560877f,-0.578671277f},{-0.839179456f,-0.543854654f},{-0.861312628f,-0.508075356f},{-0.881921291f,-0.471396744f},
 {1.0f,0.f},{0.998426795f,-0.056070447f},{0.993712187f,-0.111964479f},{0.985871017f,-0.167506218f},
 {0.974927902f,-0.222520933f},{0.960917294f,-0.276835501f},{0.943883359f,-0.330279052f},{0.923879504f,-0.382683426f},
 {0.90096885f,-0.433883727f},{0.875223398f,-0.483718902f},{0.846724212f,-0.532032073f},{0.815560877f,-0.578671277f},
 {0.781831503f,-0.623489797f},{0.745642185f,-0.66634655f},{0.707106769f,-0.707106769f},{0.66634655f,-0.745642185f},
 {0.623489797f,-0.781831503f},{0.578671277f,-0.815560877f},{0.532032073f,-0.846724212f},{0.483718902f,-0.875223398f},
 {0.433883727f,-0.90096885f},{0.382683426f,-0.923879504f},{0.330279052f,-0.943883359f},{0.276835501f,-0.960917294f},
 {0.222520933f,-0.974927902f},{0.167506218f,-0.985871017f},{0.111964479f,-0.993712187f},{0.056070447f,-0.998426795f},
 {6.12323426e-17f,-1.0f},{-0.056070447f,-0.998426795f},{-0.111964479f,-0.993712187f},{-0.167506218f,-0.985871017f},
 {-0.222520933f,-0.974927902f},{-0.276835501f,-0.960917294f},{-0.330279052f,-0.943883359f},{-0.382683426f,-0.923879504f},
 {-0.433883727f,-0.90096885f},{-0.483718902f,-0.875223398f},{-0.532032073f,-0.846724212f},{-0.578671277f,-0.815560877f},
 {-0.623489797f,-0.781831503f},{-0.66634655f,-0.745642185f},{-0.707106769f,-0.707106769f},{-0.745642185f,-0.66634655f},
 {-0.781831503f,-0.623489797f},{-0.815560877f,-0.578671277f},{-0.846724212f,-0.532032073f},{-0.875223398f,-0.483718902f},
 {-0.90096885f,-0.433883727f},{-0.923879504f,-0.382683426f},{-0.943883359f,-0.330279052f},{-0.960917294f,-0.276835501f},
 {-0.974927902f,-0.222520933f},{-0.985871017f,-0.167506218f},{-0.993712187f,-0.111964479f},{-0.998426795f,-0.056070447f},
 {-1.0f,-1.22464685e-16f},{-0.998426795f,0.056070447f},{-0.993712187f,0.111964479f},{-0.985871017f,0.167506218f},
 {-0.974927902f,0.222520933f},{-0.960917294f,0.276835501f},{-0.943883359f,0.330279052f},{-0.923879504f,0.382683426f},
 {1.0f,0.f},{0.997542262f,-0.0700673759f},{0.990181148f,-0.139790341f},{0.977952778f,-0.208826169f},
 {0.960917294f,-0.276835501f},{0.939158499f,-0.343484074f},{0.912783265f,-0.408444256f},{0.881921291f,-0.471396744f},
 {0.846724212f,-0.532032073f},{0.80736506f,-0.590052247f},{0.764037371f,-0.645172f},{0.716954052f,-0.697120428f},
 {0.66634655f,-0.745642185f},{0.612463653f,-0.790498734f},{0.555570245f,-0.831469595f},{0.495945901f,-0.868353426f},
 {0.433883727f,-0.90096885f},{0.369688839f,-0.929155588f},{0.303676754f,-0.952775121f},{0.236171931f,-0.971711278f},
 {0.167506218f,-0.985871017f},{0.0980171412f,-0.99518472f},{0.0280462559f,-0.999606609f},{-0.0420624875f,-0.99911499f},
 {-0.111964479f,-0.993712187f},{-0.181316108f,-0.983424842f},{-0.249776483f,-0.968303502f},{-0.317009091f,-0.948422492f},
 {-0.382683426f,-0.923879504f},{-0.446476698f,-0.894795239f},{-0.508075356f,-0.861312628f},{-0.567176521f,-0.823596239f},
 {-0.623489797f,-0.781831503f},{-0.676738322f,-0.736223638f},{-0.726660311f,-0.686996937f},{-0.773010433f,-0.634393275f},
 {-0.815560877f,-0.578671277f},{-0.854102433f,-0.520104885f},{-0.888445616f,-0.458981872f},{-0.918421745f,-0.395602763f},
 {-0.943883359f,-0.330279052f},{-0.964705288f,-0.26333189f},{-0.980785251f,-0.195090324f},{-0.99204421f,-0.125889793f},
 {-0.998426795f,-0.056070447f},{-0.999901652f,0.0140245073f},{-0.996461511f,0.0840505213f},{-0.988123238f,0.153663397f},
 {-0.974927902f,0.222520933f},{-0.956940353f,0.290284663f},{-0.934248924f,0.356621534f},{-0.906965256f,0.421205431f},
 {-0.875223398f,0.483718902f},{-0.839179456f,0.543854654f},{-0.799010456f,0.601317108f},{-0.754913986f,0.655823767f},
 {-0.707106769f,0.707106769f},{-0.655823767f,0.754913986f},{-0.601317108f,0.799010456f},{-0.543854654f,0.839179456f},
 {-0.483718902f,0.875223398f},{-0.421205431f,0.906965256f},{-0.356621534f,0.934248924f},{-0.290284663f,0.956940353f},
 {1.0f,0.f},{0.996461511f,-0.0840505213f},{0.985871017f,-0.167506218f},{0.968303502f,-0.249776483f},
 {0.943883359f,-0.330279052f},{0.912783265f,-0.408444256f},{0.875223398f,-0.483718902f},{0.831469595f,-0.555570245f},
 {0.781831503f,-0.623489797f},{0.726660311f,-0.686996937f},{0.66634655f,-0.745642185f},{0.601317108f,-0.799010456f},
 {0.532032073f,-0.846724212f},{0.458981872f,-0.888445616f},{0.382683426f,-0.923879504f},{0.303676754f,-0.952775121f},
 {0.222520933f,-0.974927902f},{0.139790341f,-0.990181148f},{0.056070447f,-0.998426795f},{-0.0280462559f,-0.999606609f},
 {-0.111964479f,-0.993712187f},{-0.195090324f,-0.980785251f},{-0.276835501f,-0.960917294f},{-0.356621534f,-0.934248924f},
 {-0.433883727f,-0.90096885f},{-0.508075356f,-0.861312628f},{-0.578671277f,-0.815560877f},{-0.645172f,-0.764037371f},
 {-0.707106769f,-0.707106769f},{-0.764037371f,-0.645172f},{-0.815560877f,-0.578671277f},{-0.861312628f,-0.508075356f},
 {-0.90096885f,-0.433883727f},{-0.934248924f,-0.356621534f},{-0.960917294f,-0.276835501f},{-0.980785251f,-0.195090324f},
 {-0.993712187f,-0.111964479f},{-0.999606609f,-0.0280462559f},{-0.998426795f,0.056070447f},{-0.990181148f,0.139790341f},
 {-0.974927902f,0.222520933f},{-0.952775121f,0.303676754f},{-0.923879504f,0.382683426f},{-0.888445616f,0.458981872f},
 {-0.846724212f,0.532032073f},{-0.799010456f,0.601317108f},{-0.745642185f,0.66634655f},{-0.686996937f,0.726660311f},
 {-0.623489797f,0.781831503f},{-0.555570245f,0.831469595f},{-0.483718902f,0.875223398f},{-0.408444256f,0.912783265f},
 {-0.330279052f,0.943883359f},{-0.249776483f,0.968303502f},{-0.167506218f,0.985871017f},{-0.0840505213f,0.996461511f},
 {-1.83697015e-16f,1.0f},{0.0840505213f,0.996461511f},{0.167506218f,0.985871017f},{0.249776483f,0.968303502f},
 {0.330279052f,0.943883359f},{0.408444256f,0.912783265f},{0.483718902f,0.875223398f},{0.555570245f,0.831469595f},
};
__device__ const cf T448B[56] = {
 {1.0f,0.f},{0.99518472f,-0.0980171412f},{0.980785251f,-0.195090324f},{0.956940353f,-0.290284663f},
 {0.923879504f,-0.382683426f},{0.881921291f,-0.471396744f},{0.831469595f,-0.555570245f},{0.773010433f,-0.634393275f},
 {1.0f,0.f},{0.980785251f,-0.195090324f},{0.923879504f,-0.382683426f},{0.831469595f,-0.555570245f},
 {0.707106769f,-0.707106769f},{0.555570245f,-0.831469595f},{0.382683426f,-0.923879504f},{0.195090324f,-0.980785251f},
 {1.0f,0.f},{0.956940353f,-0.290284663f},{0.831469595f,-0.555570245f},{0.634393275f,-0.773010433f},
 {0.382683426f,-0.923879504f},{0.0980171412f,-0.99518472f},{-0.195090324f,-0.980785251f},{-0.471396744f,-0.881921291f},
 {1.0f,0.f},{0.923879504f,-0.382683426f},{0.707106769f,-0.707106769f},{0.382683426f,-0.923879504f},
 {6.12323426e-17f,-1.0f},{-0.382683426f,-0.923879504f},{-0.707106769f,-0.707106769f},{-0.923879504f,-0.382683426f},
 {1.0f,0.f},{0.881921291f,-0.471396744f},{0.555570245f,-0.831469595f},{0.0980171412f,-0.99518472f},
 {-0.382683426f,-0.923879504f},{-0.773010433f,-0.634393275f},{-0.980785251f,-0.195090324f},{-0.956940353f,0.290284663f},
 {1.0f,0.f},{0.831469595f,-0.555570245f},{0.382683426f,-0.923879504f},{-0.195090324f,-0.980785251f},
 {-0.707106769f,-0.707106769f},{-0.980785251f,-0.195090324f},{-0.923879504f,0.382683426f},{-0.555570245f,0.831469595f},
 {1.0f,0.f},{0.773010433f,-0.634393275f},{0.195090324f,-0.980785251f},{-0.471396744f,-0.881921291f},
 {-0.923879504f,-0.382683426f},{-0.956940353f,0.290284663f},{-0.555570245f,0.831469595f},{0.0980171412f,0.99518472f},
};
__device__ const cf T2A[1792] = {
 {1.0f,0.f},{0.999995291f,-0.00306795677f},{0.999981165f,-0.00613588467f},{0.999957621f,-0.00920375437f},
 {0.999924719f,-0.0122715384f},{0.99988234f,-0.015339206f},{0.999830604f,-0.0184067301f},{0.99976939f,-0.0214740802f},
 {0.999698818f,-0.024541229f},{0.999618828f,-0.027608145f},{0.999529421f,-0.030674804f},{0.999430597f,-0.0337411724f},
 {0.999322355f,-0.0368072242f},{0.999204755f,-0.0398729257f},{0.999077737f,-0.0429382585f},{0.998941302f,-0.0460031815f},
 {0.99879545f,-0.0490676761f},{0.998640239f,-0.052131705f},{0.998475552f,-0.0551952459f},{0.998301566f,-0.0582582653f},
 {0.998118103f,-0.061320737f},{0.997925282f,-0.0643826276f},{0.997723043f,-0.0674439222f},{0.997511446f,-0.070504576f},
 {0.997290432f,-0.0735645667f},{0.997060061f,-0.0766238645f},{0.996820271f,-0.0796824396f},{0.996571124f,-0.0827402622f},
 {0.996312618f,-0.0857973099f},{0.996044695f,-0.0888535529f},{0.995767415f,-0.0919089541f},{0.995480776f,-0.0949634984f},
 {0.99518472f,-0.0980171412f},{0.994879305f,-0.10106986f},{0.994564593f,-0.104121633f},{0.994240463f,-0.107172422f},
 {0.993906975f,-0.110222206f},{0.993564129f,-0.113270953f},{0.993211925f,-0.116318628f},{0.992850423f,-0.119365215f},
 {0.992479563f,-0.122410677f},{0.992099285f,-0.125454977f},{0.991709769f,-0.128498107f},{0.991310835f,-0.13154003f},
 {0.990902662f,-0.134580702f},{0.990485072f,-0.137620121f},{0.990058184f,-0.140658244f},{0.989621997f,-0.143695027f},
 {0.989176512f,-0.146730468f},{0.988721669f,-0.149764538f},{0.988257587f,-0.152797192f},{0.987784147f,-0.155828401f},
 {0.987301409f,-0.15885815f},{0.986809373f,-0.161886394f},{0.986308098f,-0.164913118f},{0.985797524f,-0.167938292f},
 {0.985277653f,-0.170961887f},{0.984748483f,-0.173983872f},{0.984210074f,-0.177004218f},{0.983662426f,-0.180022895f},
 {0.983105481f,-0.183039889f},{0.982539296f,-0.186055154f},{0.981963873f,-0.18906866f},{0.981379211f,-0.192080393f},
 {0.980785251f,-0.195090324f},{0.980182111f,-0.198098406f},{0.979569793f,-0.201104641f},{0.978948176f,-0.204108968f},
 {0.97831738f,-0.207111374f},{0.977677345f,-0.210111842f},{0.977028131f,-0.213110313f},{0.976369739f,-0.216106802f},
 {0.975702107f,-0.219101235f},{0.975025356f,-0.222093627f},{0.974339366f,-0.225083917f},{0.973644257f,-0.228072077f},
 {0.972939968f,-0.231058106f},{0.972226501f,-0.234041959f},{0.971503913f,-0.237023607f},{0.970772147f,-0.24000302f},
 {0.970031261f,-0.242980182f},{0.969281256f,-0.24595505f},{0.968522072f,-0.248927608f},{0.967753828f,-0.251897812f},
 {0.966976464f,-0.254865646f},{0.966189981f,-0.257831097f},{0.965394437f,-0.260794103f},{0.964589775f,-0.263754666f},
 {0.963776052f,-0.266712755f},{0.962953269f,-0.269668311f},{0.962121427f,-0.272621363f},{0.961280465f,-0.275571823f},
 {0.960430503f,-0.27851969f},{0.95957154f,-0.281464934f},{0.958703458f,-0.284407526f},{0.957826436f,-0.287347466f},
 {0.956940353f,-0.290284663f},{0.95604527f,-0.293219149f},{0.955141187f,-0.296150893f},{0.954228103f,-0.299079835f},
 {0.953306019f,-0.302005947f},{0.952374995f,-0.304929227f},{0.95143503f,-0.307849646f},{0.950486064f,-0.310767144f},
 {0.949528158f,-0.313681751f},{0.94856137f,-0.316593379f},{0.947585583f,-0.319502026f},{0.946600914f,-0.322407693f},
 {0.945607305f,-0.32531029f},{0.944604814f,-0.328209847f},{0.943593442f,-0.331106305f},{0.94257319f,-0.333999664f},
 {0.941544056f,-0.336889863f},{0.940506041f,-0.339776874f},{0.939459205f,-0.342660725f},{0.938403547f,-0.345541328f},
 {0.937339008f,-0.348418683f},{0.936265647f,-0.351292759f},{0.935183525f,-0.354163527f},{0.934092522f,-0.357030958f},
 {0.932992816f,-0.359895051f},{0.931884289f,-0.362755716f},{0.93076694f,-0.365612984f},{0.929640889f,-0.368466824f},
 {0.928506076f,-0.371317208f},{0.927362502f,-0.374164075f},{0.926210225f,-0.377007425f},{0.925049245f,-0.379847199f},
 {0.923879504f,-0.382683426f},{0.92270112f,-0.385516047f},{0.921514034f,-0.388345033f},{0.920318305f,-0.391170382f},
 {0.919113874f,-0.393992037f},{0.917900801f,-0.396809995f},{0.916679084f,-0.399624199f},{0.915448725f,-0.402434647f},
 {0.914209783f,-0.405241311f},{0.912962198f,-0.408044159f},{0.91170603f,-0.410843164f},{0.910441279f,-0.413638324f},
 {0.909168005f,-0.416429549f},{0.907886088f,-0.419216901f},{0.906595707f,-0.422000259f},{0.905296743f,-0.424779683f},
 {0.903989315f,-0.427555084f},{0.902673304f,-0.430326492f},{0.901348829f,-0.433093816f},{0.900015891f,-0.435857087f},
 {0.898674488f,-0.438616246f},{0.897324562f,-0.441371262f},{0.895966232f,-0.444122136f},{0.894599497f,-0.446868837f},
 {0.893224299f,-0.449611336f},{0.891840696f,-0.452349573f},{0.890448749f,-0.455083579f},{0.889048338f,-0.457813293f},
 {0.887639642f,-0.460538715f},{0.886222541f,-0.463259786f},{0.884797096f,-0.465976506f},{0.883363366f,-0.468688816f},
 {0.881921291f,-0.471396744f},{0.880470872f,-0.474100202f},{0.879012227f,-0.47679922f},{0.877545297f,-0.479493767f},
 {0.876070082f,-0.482183784f},{0.874586642f,-0.484869242f},{0.873094976f,-0.487550169f},{0.871595085f,-0.490226477f},
 {0.870086968f,-0.492898196f},{0.868570685f,-0.495565265f},{0.867046237f,-0.498227656f},{0.865513623f,-0.500885367f},
 {0.863972843f,-0.50353837f},{0.862423956f,-0.506186664f},{0.860866964f,-0.50883013f},{0.859301805f,-0.511468828f},
 {0.857728601f,-0.514102757f},{0.856147349f,-0.516731799f},{0.854557991f,-0.519356012f},{0.852960587f,-0.521975279f},
 {0.851355195f,-0.524589658f},{0.849741757f,-0.527199149f},{0.848120332f,-0.529803634f},{0.84649092f,-0.532403111f},
 {0.84485358f,-0.534997642f},{0.843208253f,-0.537587047f},{0.841554999f,-0.540171444f},{0.839893818f,-0.542750776f},
 {0.838224709f,-0.545324981f},{0.836547732f,-0.547894061f},{0.834862888f,-0.550457954f},{0.833170176f,-0.553016722f},
 {0.831469595f,-0.555570245f},{0.829761207f,-0.558118522f},{0.82804507f,-0.560661554f},{0.826321065f,-0.563199341f},
 {0.824589312f,-0.565731823f},{0.82284981f,-0.568258941f},{0.8211025f,-0.570780754f},{0.819347501f,-0.573297143f},
 {0.817584813f,-0.575808167f},{0.815814435f,-0.578313768f},{0.81403631f,-0.580813944f},{0.812250614f,-0.583308637f},
 {0.81045717f,-0.585797846f},{0.808656156f,-0.588281572f},{0.806847572f,-0.590759695f},{0.805031359f,-0.593232274f},
 {0.803207517f,-0.59569931f},{0.801376164f,-0.598160684f},{0.799537241f,-0.600616455f},{0.797690868f,-0.603066623f},
 {0.795836926f,-0.605511069f},{0.793975472f,-0.607949793f},{0.792106569f,-0.610382795f},{0.790230215f,-0.612810075f},
 {0.78834641f,-0.615231574f},{0.786455214f,-0.61764729f},{0.784556568f,-0.620057225f},{0.78265059f,-0.622461259f},
 {0.780737221f,-0.624859512f},{0.778816521f,-0.627251804f},{0.77688849f,-0.629638255f},{0.774953127f,-0.632018745f},
 {0.773010433f,-0.634393275f},{0.771060526f,-0.636761844f},{0.769103348f,-0.639124453f},{0.767138898f,-0.641481042f},
 {0.765167236f,-0.643831551f},{0.763188422f,-0.64617604f},{0.761202395f,-0.64851439f},{0.759209216f,-0.65084666f},
 {0.757208824f,-0.653172851f},{0.755201399f,-0.655492842f},{0.753186822f,-0.657806695f},{0.751165152f,-0.660114348f},
 {0.749136388f,-0.662415802f},{0.747100592f,-0.664710999f},{0.745057762f,-0.666999936f},{0.743007958f,-0.669282615f},
 {0.740951121f,-0.671558976f},{0.73888731f,-0.673829019f},{0.736816585f,-0.676092684f},{0.734738886f,-0.678350031f},
 {0.732654274f,-0.680601001f},{0.730562747f,-0.682845533f},{0.728464365f,-0.685083687f},{0.726359129f,-0.687315345f},
 {0.724247098f,-0.689540565f},{0.722128212f,-0.691759229f},{0.720002532f,-0.693971455f},{0.717870057f,-0.696177125f},
 {0.715730846f,-0.698376238f},{0.71358484f,-0.700568795f},{0.711432219f,-0.702754736f},{0.709272802f,-0.704934061f},
 {1.0f,0.f},{0.999981165f,-0.00613588467f},{0.999924719f,-0.0122715384f},{0.999830604f,-0.0184067301f},
 {0.999698818f,-0.024541229f},{0.999529421f,-0.030674804f},{0.999322355f,-0.0368072242f},{0.999077737f,-0.0429382585f},
 {0.99879545f,-0.0490676761f},{0.998475552f,-0.0551952459f},{0.998118103f,-0.061320737f},{0.997723043f,-0.0674439222f},
 {0.997290432f,-0.0735645667f},{0.996820271f,-0.0796824396f},{0.996312618f,-0.0857973099f},{0.995767415f,-0.0919089541f},
 {0.99518472f,-0.0980171412f},{0.994564593f,-0.104121633f},{0.993906975f,-0.110222206f},{0.993211925f,-0.116318628f},
 {0.992479563f,-0.122410677f},{0.991709769f,-0.128498107f},{0.990902662f,-0.134580702f},{0.990058184f,-0.140658244f},
 {0.989176512f,-0.146730468f},{0.988257587f,-0.152797192f},{0.987301409f,-0.15885815f},{0.986308098f,-0.164913118f},
 {0.985277653f,-0.170961887f},{0.984210074f,-0.177004218f},{0.983105481f,-0.183039889f},{0.981963873f,-0.18906866f},
 {0.980785251f,-0.195090324f},{0.979569793f,-0.201104641f},{0.97831738f,-0.207111374f},{0.977028131f,-0.213110313f},
 {0.975702107f,-0.219101235f},{0.974339366f,-0.225083917f},{0.972939968f,-0.231058106f},{0.971503913f,-0.237023607f},
 {0.970031261f,-0.242980182f},{0.968522072f,-0.248927608f},{0.966976464f,-0.254865646f},{0.965394437f,-0.260794103f},
 {0.963776052f,-0.266712755f},{0.962121427f,-0.272621363f},{0.960430503f,-0.27851969f},{0.958703458f,-0.284407526f},
 {0.956940353f,-0.290284663f},{0.955141187f,-0.296150893f},{0.953306019f,-0.302005947f},{0.95143503f,-0.307849646f},
 {0.949528158f,-0.313681751f},{0.947585583f,-0.319502026f},{0.945607305f,-0.32531029f},{0.943593442f,-0.331106305f},
 {0.941544056f,-0.336889863f},{0.939459205f,-0.342660725f},{0.937339008f,-0.348418683f},{0.935183525f,-0.354163527f},
 {0.932992816f,-0.359895051f},{0.93076694f,-0.365612984f},{0.928506076f,-0.371317208f},{0.926210225f,-0.377007425f},
 {0.923879504f,-0.382683426f},{0.921514034f,-0.388345033f},{0.919113874f,-0.393992037f},{0.916679084f,-0.399624199f},
 {0.914209783f,-0.405241311f},{0.91170603f,-0.410843164f},{0.909168005f,-0.416429549f},{0.906595707f,-0.422000259f},
 {0.903989315f,-0.427555084f},{0.901348829f,-0.433093816f},{0.898674488f,-0.438616246f},{0.895966232f,-0.444122136f},
 {0.893224299f,-0.449611336f},{0.890448749f,-0.455083579f},{0.887639642f,-0.460538715f},{0.884797096f,-0.465976506f},
 {0.881921291f,-0.471396744f},{0.879012227f,-0.47679922f},{0.876070082f,-0.482183784f},{0.873094976f,-0.487550169f},
 {0.870086968f,-0.492898196f},{0.867046237f,-0.498227656f},{0.863972843f,-0.50353837f},{0.860866964f,-0.50883013f},
 {0.857728601f,-0.514102757f},{0.854557991f,-0.519356012f},{0.851355195f,-0.524589658f},{0.848120332f,-0.529803634f},
 {0.84485358f,-0.534997642f},{0.841554999f,-0.540171444f},{0.838224709f,-0.545324981f},{0.834862888f,-0.550457954f},
 {0.831469595f,-0.555570245f},{0.82804507f,-0.560661554f},{0.824589312f,-0.565731823f},{0.8211025f,-0.570780754f},
 {0.817584813f,-0.575808167f},{0.81403631f,-0.580813944f},{0.81045717f,-0.585797846f},{0.806847572f,-0.590759695f},
 {0.803207517f,-0.59569931f},{0.799537241f,-0.600616455f},{0.795836926f,-0.605511069f},{0.792106569f,-0.610382795f},
 {0.78834641f,-0.615231574f},{0.784556568f,-0.620057225f},{0.780737221f,-0.624859512f},{0.77688849f,-0.629638255f},
 {0.773010433f,-0.634393275f},{0.769103348f,-0.639124453f},{0.765167236f,-0.643831551f},{0.761202395f,-0.64851439f},
 {0.757208824f,-0.653172851f},{0.753186822f,-0.657806695f},{0.749136388f,-0.662415802f},{0.745057762f,-0.666999936f},
 {0.740951121f,-0.671558976f},{0.736816585f,-0.676092684f},{0.732654274f,-0.680601001f},{0.728464365f,-0.685083687f},
 {0.724247098f,-0.689540565f},{0.720002532f,-0.693971455f},{0.715730846f,-0.698376238f},{0.711432219f,-0.702754736f},
 {0.707106769f,-0.707106769f},{0.702754736f,-0.711432219f},{0.698376238f,-0.715730846f},{0.693971455f,-0.720002532f},
 {0.689540565f,-0.724247098f},{0.685083687f,-0.728464365f},{0.680601001f,-0.732654274f},{0.676092684f,-0.736816585f},
 {0.671558976f,-0.740951121f},{0.666999936f,-0.745057762f},{0.662415802f,-0.749136388f},{0.657806695f,-0.753186822f},
 {0.653172851f,-0.757208824f},{0.64851439f,-0.761202395f},{0.643831551f,-0.765167236f},{0.639124453f,-0.769103348f},
 {0.634393275f,-0.773010433f},{0.629638255f,-0.77688849f},{0.624859512f,-0.780737221f},{0.620057225f,-0.784556568f},
 {0.615231574f,-0.78834641f},{0.610382795f,-0.792106569f},{0.605511069f,-0.795836926f},{0.600616455f,-0.799537241f},
 {0.59569931f,-0.803207517f},{0.590759695f,-0.806847572f},{0.585797846f,-0.81045717f},{0.580813944f,-0.81403631f},
 {0.575808167f,-0.817584813f},{0.570780754f,-0.8211025f},{0.565731823f,-0.824589312f},{0.560661554f,-0.82804507f},
 {0.555570245f,-0.831469595f},{0.550457954f,-0.834862888f},{0.545324981f,-0.838224709f},{0.540171444f,-0.841554999f},
 {0.534997642f,-0.84485358f},{0.529803634f,-0.848120332f},{0.524589658f,-0.851355195f},{0.519356012f,-0.854557991f},
 {0.514102757f,-0.857728601f},{0.50883013f,-0.860866964f},{0.50353837f,-0.863972843f},{0.498227656f,-0.867046237f},
 {0.492898196f,-0.870086968f},{0.487550169f,-0.873094976f},{0.482183784f,-0.876070082f},{0.47679922f,-0.879012227f},
 {0.471396744f,-0.881921291f},{0.465976506f,-0.884797096f},{0.460538715f,-0.887639642f},{0.455083579f,-0.890448749f},
 {0.449611336f,-0.893224299f},{0.444122136f,-0.895966232f},{0.438616246f,-0.898674488f},{0.433093816f,-0.901348829f},
 {0.427555084f,-0.903989315f},{0.422000259f,-0.906595707f},{0.416429549f,-0.909168005f},{0.410843164f,-0.91170603f},
 {0.405241311f,-0.914209783f},{0.399624199f,-0.916679084f},{0.393992037f,-0.919113874f},{0.388345033f,-0.921514034f},
 {0.382683426f,-0.923879504f},{0.377007425f,-0.926210225f},{0.371317208f,-0.928506076f},{0.365612984f,-0.93076694f},
 {0.359895051f,-0.932992816f},{0.354163527f,-0.935183525f},{0.348418683f,-0.937339008f},{0.342660725f,-0.939459205f},
 {0.336889863f,-0.941544056f},{0.331106305f,-0.943593442f},{0.32531029f,-0.945607305f},{0.319502026f,-0.947585583f},
 {0.313681751f,-0.949528158f},{0.307849646f,-0.95143503f},{0.302005947f,-0.953306019f},{0.296150893f,-0.955141187f},
 {0.290284663f,-0.956940353f},{0.284407526f,-0.958703458f},{0.27851969f,-0.960430503f},{0.272621363f,-0.962121427f},
 {0.266712755f,-0.963776052f},{0.260794103f,-0.965394437f},{0.254865646f,-0.966976464f},{0.248927608f,-0.968522072f},
 {0.242980182f,-0.970031261f},{0.237023607f,-0.971503913f},{0.231058106f,-0.972939968f},{0.225083917f,-0.974339366f},
 {0.219101235f,-0.975702107f},{0.213110313f,-0.977028131f},{0.207111374f,-0.97831738f},{0.201104641f,-0.979569793f},
 {0.195090324f,-0.980785251f},{0.18906866f,-0.981963873f},{0.183039889f,-0.983105481f},{0.177004218f,-0.984210074f},
 {0.170961887f,-0.985277653f},{0.164913118f,-0.986308098f},{0.15885815f,-0.987301409f},{0.152797192f,-0.988257587f},
 {0.146730468f,-0.989176512f},{0.140658244f,-0.990058184f},{0.134580702f,-0.990902662f},{0.128498107f,-0.991709769f},
 {0.122410677f,-0.992479563f},{0.116318628f,-0.993211925f},{0.110222206f,-0.993906975f},{0.104121633f,-0.994564593f},
 {0.0980171412f,-0.99518472f},{0.0919089541f,-0.995767415f},{0.0857973099f,-0.996312618f},{0.0796824396f,-0.996820271f},
 {0.0735645667f,-0.997290432f},{0.0674439222f,-0.997723043f},{0.061320737f,-0.998118103f},{0.0551952459f,-0.998475552f},
 {0.0490676761f,-0.99879545f},{0.0429382585f,-0.999077737f},{0.0368072242f,-0.999322355f},{0.030674804f,-0.999529421f},
 {0.024541229f,-0.999698818f},{0.0184067301f,-0.999830604f},{0.0122715384f,-0.999924719f},{0.00613588467f,-0.999981165f},
 {1.0f,0.f},{0.999957621f,-0.00920375437f},{0.999830604f,-0.0184067301f},{0.999618828f,-0.027608145f},
 {0.999322355f,-0.0368072242f},{0.998941302f,-0.0460031815f},{0.998475552f,-0.0551952459f},{0.997925282f,-0.0643826276f},
 {0.997290432f,-0.0735645667f},{0.996571124f,-0.0827402622f},{0.995767415f,-0.0919089541f},{0.994879305f,-0.10106986f},
 {0.993906975f,-0.110222206f},{0.992850423f,-0.119365215f},{0.991709769f,-0.128498107f},{0.990485072f,-0.137620121f},
 {0.989176512f,-0.146730468f},{0.987784147f,-0.155828401f},{0.986308098f,-0.164913118f},{0.984748483f,-0.173983872f},
 {0.983105481f,-0.183039889f},{0.981379211f,-0.192080393f},{0.979569793f,-0.201104641f},{0.977677345f,-0.210111842f},
 {0.975702107f,-0.219101235f},{0.973644257f,-0.228072077f},{0.971503913f,-0.237023607f},{0.969281256f,-0.24595505f},
 {0.966976464f,-0.254865646f},{0.964589775f,-0.263754666f},{0.962121427f,-0.272621363f},{0.95957154f,-0.281464934f},
 {0.956940353f,-0.290284663f},{0.954228103f,-0.299079835f},{0.95143503f,-0.307849646f},{0.94856137f,-0.316593379f},
 {0.945607305f,-0.32531029f},{0.94257319f,-0.333999664f},{0.939459205f,-0.342660725f},{0.936265647f,-0.351292759f},
 {0.932992816f,-0.359895051f},{0.929640889f,-0.368466824f},{0.926210225f,-0.377007425f},{0.92270112f,-0.385516047f},
 {0.919113874f,-0.393992037f},{0.915448725f,-0.402434647f},{0.91170603f,-0.410843164f},{0.907886088f,-0.419216901f},
 {0.903989315f,-0.427555084f},{0.900015891f,-0.435857087f},{0.895966232f,-0.444122136f},{0.891840696f,-0.452349573f},
 {0.887639642f,-0.460538715f},{0.883363366f,-0.468688816f},{0.879012227f,-0.47679922f},{0.874586642f,-0.484869242f},
 {0.870086968f,-0.492898196f},{0.865513623f,-0.500885367f},{0.860866964f,-0.50883013f},{0.856147349f,-0.516731799f},
 {0.851355195f,-0.524589658f},{0.84649092f,-0.532403111f},{0.841554999f,-0.540171444f},{0.836547732f,-0.547894061f},
 {0.831469595f,-0.555570245f},{0.826321065f,-0.563199341f},{0.8211025f,-0.570780754f},{0.815814435f,-0.578313768f},
 {0.81045717f,-0.585797846f},{0.805031359f,-0.593232274f},{0.799537241f,-0.600616455f},{0.793975472f,-0.607949793f},
 {0.78834641f,-0.615231574f},{0.78265059f,-0.622461259f},{0.77688849f,-0.629638255f},{0.771060526f,-0.636761844f},
 {0.765167236f,-0.643831551f},{0.759209216f,-0.65084666f},{0.753186822f,-0.657806695f},{0.747100592f,-0.664710999f},
 {0.740951121f,-0.671558976f},{0.734738886f,-0.678350031f},{0.728464365f,-0.685083687f},{0.722128212f,-0.691759229f},
 {0.715730846f,-0.698376238f},{0.709272802f,-0.704934061f},{0.702754736f,-0.711432219f},{0.696177125f,-0.717870057f},
 {0.689540565f,-0.724247098f},{0.682845533f,-0.730562747f},{0.676092684f,-0.736816585f},{0.669282615f,-0.743007958f},
 {0.662415802f,-0.749136388f},{0.655492842f,-0.755201399f},{0.64851439f,-0.761202395f},{0.641481042f,-0.767138898f},
 {0.634393275f,-0.773010433f},{0.627251804f,-0.778816521f},{0.620057225f,-0.784556568f},{0.612810075f,-0.790230215f},
 {0.605511069f,-0.795836926f},{0.598160684f,-0.801376164f},{0.590759695f,-0.806847572f},{0.583308637f,-0.812250614f},
 {0.575808167f,-0.817584813f},{0.568258941f,-0.82284981f},{0.560661554f,-0.82804507f},{0.553016722f,-0.833170176f},
 {0.545324981f,-0.838224709f},{0.537587047f,-0.843208253f},{0.529803634f,-0.848120332f},{0.521975279f,-0.852960587f},
 {0.514102757f,-0.857728601f},{0.506186664f,-0.862423956f},{0.498227656f,-0.867046237f},{0.490226477f,-0.871595085f},
 {0.482183784f,-0.876070082f},{0.474100202f,-0.880470872f},{0.465976506f,-0.884797096f},{0.457813293f,-0.889048338f},
 {0.449611336f,-0.893224299f},{0.441371262f,-0.897324562f},{0.433093816f,-0.901348829f},{0.424779683f,-0.905296743f},
 {0.416429549f,-0.909168005f},{0.408044159f,-0.912962198f},{0.399624199f,-0.916679084f},{0.391170382f,-0.920318305f},
 {0.382683426f,-0.923879504f},{0.374164075f,-0.927362502f},{0.365612984f,-0.93076694f},{0.357030958f,-0.934092522f},
 {0.348418683f,-0.937339008f},{0.339776874f,-0.940506041f},{0.331106305f,-0.943593442f},{0.322407693f,-0.946600914f},
 {0.313681751f,-0.949528158f},{0.304929227f,-0.952374995f},{0.296150893f,-0.955141187f},{0.287347466f,-0.957826436f},
 {0.27851969f,-0.960430503f},{0.269668311f,-0.962953269f},{0.260794103f,-0.965394437f},{0.251897812f,-0.967753828f},
 {0.242980182f,-0.970031261f},{0.234041959f,-0.972226501f},{0.225083917f,-0.974339366f},{0.216106802f,-0.976369739f},
 {0.207111374f,-0.97831738f},{0.198098406f,-0.980182111f},{0.18906866f,-0.981963873f},{0.180022895f,-0.983662426f},
 {0.170961887f,-0.985277653f},{0.161886394f,-0.986809373f},{0.152797192f,-0.988257587f},{0.143695027f,-0.989621997f},
 {0.134580702f,-0.990902662f},{0.125454977f,-0.992099285f},{0.116318628f,-0.993211925f},{0.107172422f,-0.994240463f},
 {0.0980171412f,-0.99518472f},{0.0888535529f,-0.996044695f},{0.0796824396f,-0.996820271f},{0.070504576f,-0.997511446f},
 {0.061320737f,-0.998118103f},{0.052131705f,-0.998640239f},{0.0429382585f,-0.999077737f},{0.0337411724f,-0.999430597f},
 {0.024541229f,-0.999698818f},{0.015339206f,-0.99988234f},{0.00613588467f,-0.999981165f},{-0.00306795677f,-0.999995291f},
 {-0.0122715384f,-0.999924719f},{-0.0214740802f,-0.99976939f},{-0.030674804f,-0.999529421f},{-0.0398729257f,-0.999204755f},
 {-0.0490676761f,-0.99879545f},{-0.0582582653f,-0.998301566f},{-0.0674439222f,-0.997723043f},{-0.0766238645f,-0.997060061f},
 {-0.0857973099f,-0.996312618f},{-0.0949634984f,-0.995480776f},{-0.104121633f,-0.994564593f},{-0.113270953f,-0.993564129f},
 {-0.122410677f,-0.992479563f},{-0.13154003f,-0.991310835f},{-0.140658244f,-0.990058184f},{-0.149764538f,-0.988721669f},
 {-0.15885815f,-0.987301409f},{-0.167938292f,-0.985797524f},{-0.177004218f,-0.984210074f},{-0.186055154f,-0.982539296f},
 {-0.195090324f,-0.980785251f},{-0.204108968f,-0.978948176f},{-0.213110313f,-0.977028131f},{-0.222093627f,-0.975025356f},
 {-0.231058106f,-0.972939968f},{-0.24000302f,-0.970772147f},{-0.248927608f,-0.968522072f},{-0.257831097f,-0.966189981f},
 {-0.266712755f,-0.963776052f},{-0.275571823f,-0.961280465f},{-0.284407526f,-0.958703458f},{-0.293219149f,-0.95604527f},
 {-0.302005947f,-0.953306019f},{-0.310767144f,-0.950486064f},{-0.319502026f,-0.947585583f},{-0.328209847f,-0.944604814f},
 {-0.336889863f,-0.941544056f},{-0.345541328f,-0.938403547f},{-0.354163527f,-0.935183525f},{-0.362755716f,-0.931884289f},
 {-0.371317208f,-0.928506076f},{-0.379847199f,-0.925049245f},{-0.388345033f,-0.921514034f},{-0.396809995f,-0.917900801f},
 {-0.405241311f,-0.914209783f},{-0.413638324f,-0.910441279f},{-0.422000259f,-0.906595707f},{-0.430326492f,-0.902673304f},
 {-0.438616246f,-0.898674488f},{-0.446868837f,-0.894599497f},{-0.455083579f,-0.890448749f},{-0.463259786f,-0.886222541f},
 {-0.471396744f,-0.881921291f},{-0.479493767f,-0.877545297f},{-0.487550169f,-0.873094976f},{-0.495565265f,-0.868570685f},
 {-0.50353837f,-0.863972843f},{-0.511468828f,-0.859301805f},{-0.519356012f,-0.854557991f},{-0.527199149f,-0.849741757f},
 {-0.534997642f,-0.84485358f},{-0.542750776f,-0.839893818f},{-0.550457954f,-0.834862888f},{-0.558118522f,-0.829761207f},
 {-0.565731823f,-0.824589312f},{-0.573297143f,-0.819347501f},{-0.580813944f,-0.81403631f},{-0.588281572f,-0.808656156f},
 {-0.59569931f,-0.803207517f},{-0.603066623f,-0.797690868f},{-0.610382795f,-0.792106569f},{-0.61764729f,-0.786455214f},
 {-0.624859512f,-0.780737221f},{-0.632018745f,-0.774953127f},{-0.639124453f,-0.769103348f},{-0.64617604f,-0.763188422f},
 {-0.653172851f,-0.757208824f},{-0.660114348f,-0.751165152f},{-0.666999936f,-0.745057762f},{-0.673829019f,-0.73888731f},
 {-0.680601001f,-0.732654274f},{-0.687315345f,-0.726359129f},{-0.693971455f,-0.720002532f},{-0.700568795f,-0.71358484f},
 {1.0f,0.f},{0.999924719f,-0.0122715384f},{0.999698818f,-0.024541229f},{0.999322355f,-0.0368072242f},
 {0.99879545f,-0.0490676761f},{0.998118103f,-0.061320737f},{0.997290432f,-0.0735645667f},{0.996312618f,-0.0857973099f},
 {0.99518472f,-0.0980171412f},{0.993906975f,-0.110222206f},{0.992479563f,-0.122410677f},{0.990902662f,-0.134580702f},
 {0.989176512f,-0.146730468f},{0.987301409f,-0.15885815f},{0.985277653f,-0.170961887f},{0.983105481f,-0.183039889f},
 {0.980785251f,-0.195090324f},{0.97831738f,-0.207111374f},{0.975702107f,-0.219101235f},{0.972939968f,-0.231058106f},
 {0.970031261f,-0.242980182f},{0.966976464f,-0.254865646f},{0.963776052f,-0.266712755f},{0.960430503f,-0.27851969f},
 {0.956940353f,-0.290284663f},{0.953306019f,-0.302005947f},{0.949528158f,-0.313681751f},{0.945607305f,-0.32531029f},
 {0.941544056f,-0.336889863f},{0.937339008f,-0.348418683f},{0.932992816f,-0.359895051f},{0.928506076f,-0.371317208f},
 {0.923879504f,-0.382683426f},{0.919113874f,-0.393992037f},{0.914209783f,-0.405241311f},{0.909168005f,-0.416429549f},
 {0.903989315f,-0.427555084f},{0.898674488f,-0.438616246f},{0.893224299f,-0.449611336f},{0.887639642f,-0.460538715f},
 {0.881921291f,-0.471396744f},{0.876070082f,-0.482183784f},{0.870086968f,-0.492898196f},{0.863972843f,-0.50353837f},
 {0.857728601f,-0.514102757f},{0.851355195f,-0.524589658f},{0.84485358f,-0.534997642f},{0.838224709f,-0.545324981f},
 {0.831469595f,-0.555570245f},{0.824589312f,-0.565731823f},{0.817584813f,-0.575808167f},{0.81045717f,-0.585797846f},
 {0.803207517f,-0.59569931f},{0.795836926f,-0.605511069f},{0.78834641f,-0.615231574f},{0.780737221f,-0.624859512f},
 {0.773010433f,-0.634393275f},{0.765167236f,-0.643831551f},{0.757208824f,-0.653172851f},{0.749136388f,-0.662415802f},
 {0.740951121f,-0.671558976f},{0.732654274f,-0.680601001f},{0.724247098f,-0.689540565f},{0.715730846f,-0.698376238f},
 {0.707106769f,-0.707106769f},{0.698376238f,-0.715730846f},{0.689540565f,-0.724247098f},{0.680601001f,-0.732654274f},
 {0.671558976f,-0.740951121f},{0.662415802f,-0.749136388f},{0.653172851f,-0.757208824f},{0.643831551f,-0.765167236f},
 {0.634393275f,-0.773010433f},{0.624859512f,-0.780737221f},{0.615231574f,-0.78834641f},{0.605511069f,-0.795836926f},
 {0.59569931f,-0.803207517f},{0.585797846f,-0.81045717f},{0.575808167f,-0.817584813f},{0.565731823f,-0.824589312f},
 {0.555570245f,-0.831469595f},{0.545324981f,-0.838224709f},{0.534997642f,-0.84485358f},{0.524589658f,-0.851355195f},
 {0.514102757f,-0.857728601f},{0.50353837f,-0.863972843f},{0.492898196f,-0.870086968f},{0.482183784f,-0.876070082f},
 {0.471396744f,-0.881921291f},{0.460538715f,-0.887639642f},{0.449611336f,-0.893224299f},{0.438616246f,-0.898674488f},
 {0.427555084f,-0.903989315f},{0.416429549f,-0.909168005f},{0.405241311f,-0.914209783f},{0.393992037f,-0.919113874f},
 {0.382683426f,-0.923879504f},{0.371317208f,-0.928506076f},{0.359895051f,-0.932992816f},{0.348418683f,-0.937339008f},
 {0.336889863f,-0.941544056f},{0.32531029f,-0.945607305f},{0.313681751f,-0.949528158f},{0.302005947f,-0.953306019f},
 {0.290284663f,-0.956940353f},{0.27851969f,-0.960430503f},{0.266712755f,-0.963776052f},{0.254865646f,-0.966976464f},
 {0.242980182f,-0.970031261f},{0.231058106f,-0.972939968f},{0.219101235f,-0.975702107f},{0.207111374f,-0.97831738f},
 {0.195090324f,-0.980785251f},{0.183039889f,-0.983105481f},{0.170961887f,-0.985277653f},{0.15885815f,-0.987301409f},
 {0.146730468f,-0.989176512f},{0.134580702f,-0.990902662f},{0.122410677f,-0.992479563f},{0.110222206f,-0.993906975f},
 {0.0980171412f,-0.99518472f},{0.0857973099f,-0.996312618f},{0.0735645667f,-0.997290432f},{0.061320737f,-0.998118103f},
 {0.0490676761f,-0.99879545f},{0.0368072242f,-0.999322355f},{0.024541229f,-0.999698818f},{0.0122715384f,-0.999924719f},
 {6.12323426e-17f,-1.0f},{-0.0122715384f,-0.999924719f},{-0.024541229f,-0.999698818f},{-0.0368072242f,-0.999322355f},
 {-0.0490676761f,-0.99879545f},{-0.061320737f,-0.998118103f},{-0.0735645667f,-0.997290432f},{-0.0857973099f,-0.996312618f},
 {-0.0980171412f,-0.99518472f},{-0.110222206f,-0.993906975f},{-0.122410677f,-0.992479563f},{-0.134580702f,-0.990902662f},
 {-0.146730468f,-0.989176512f},{-0.15885815f,-0.987301409f},{-0.170961887f,-0.985277653f},{-0.183039889f,-0.983105481f},
 {-0.195090324f,-0.980785251f},{-0.207111374f,-0.97831738f},{-0.219101235f,-0.975702107f},{-0.231058106f,-0.972939968f},
 {-0.242980182f,-0.970031261f},{-0.254865646f,-0.966976464f},{-0.266712755f,-0.963776052f},{-0.27851969f,-0.960430503f},
 {-0.290284663f,-0.956940353f},{-0.302005947f,-0.953306019f},{-0.313681751f,-0.949528158f},{-0.32531029f,-0.945607305f},
 {-0.336889863f,-0.941544056f},{-0.348418683f,-0.937339008f},{-0.359895051f,-0.932992816f},{-0.371317208f,-0.928506076f},
 {-0.382683426f,-0.923879504f},{-0.393992037f,-0.919113874f},{-0.405241311f,-0.914209783f},{-0.416429549f,-0.909168005f},
 {-0.427555084f,-0.903989315f},{-0.438616246f,-0.898674488f},{-0.449611336f,-0.893224299f},{-0.460538715f,-0.887639642f},
 {-0.471396744f,-0.881921291f},{-0.482183784f,-0.876070082f},{-0.492898196f,-0.870086968f},{-0.50353837f,-0.863972843f},
 {-0.514102757f,-0.857728601f},{-0.524589658f,-0.851355195f},{-0.534997642f,-0.84485358f},{-0.545324981f,-0.838224709f},
 {-0.555570245f,-0.831469595f},{-0.565731823f,-0.824589312f},{-0.575808167f,-0.817584813f},{-0.585797846f,-0.81045717f},
 {-0.59569931f,-0.803207517f},{-0.605511069f,-0.795836926f},{-0.615231574f,-0.78834641f},{-0.624859512f,-0.780737221f},
 {-0.634393275f,-0.773010433f},{-0.643831551f,-0.765167236f},{-0.653172851f,-0.757208824f},{-0.662415802f,-0.749136388f},
 {-0.671558976f,-0.740951121f},{-0.680601001f,-0.732654274f},{-0.689540565f,-0.724247098f},{-0.698376238f,-0.715730846f},
 {-0.707106769f,-0.707106769f},{-0.715730846f,-0.698376238f},{-0.724247098f,-0.689540565f},{-0.732654274f,-0.680601001f},
 {-0.740951121f,-0.671558976f},{-0.749136388f,-0.662415802f},{-0.757208824f,-0.653172851f},{-0.765167236f,-0.643831551f},
 {-0.773010433f,-0.634393275f},{-0.780737221f,-0.624859512f},{-0.78834641f,-0.615231574f},{-0.795836926f,-0.605511069f},
 {-0.803207517f,-0.59569931f},{-0.81045717f,-0.585797846f},{-0.817584813f,-0.575808167f},{-0.824589312f,-0.565731823f},
 {-0.831469595f,-0.555570245f},{-0.838224709f,-0.545324981f},{-0.84485358f,-0.534997642f},{-0.851355195f,-0.524589658f},
 {-0.857728601f,-0.514102757f},{-0.863972843f,-0.50353837f},{-0.870086968f,-0.492898196f},{-0.876070082f,-0.482183784f},
 {-0.881921291f,-0.471396744f},{-0.887639642f,-0.460538715f},{-0.893224299f,-0.449611336f},{-0.898674488f,-0.438616246f},
 {-0.903989315f,-0.427555084f},{-0.909168005f,-0.416429549f},{-0.914209783f,-0.405241311f},{-0.919113874f,-0.393992037f},
 {-0.923879504f,-0.382683426f},{-0.928506076f,-0.371317208f},{-0.932992816f,-0.359895051f},{-0.937339008f,-0.348418683f},
 {-0.941544056f,-0.336889863f},{-0.945607305f,-0.32531029f},{-0.949528158f,-0.313681751f},{-0.953306019f,-0.302005947f},
 {-0.956940353f,-0.290284663f},{-0.960430503f,-0.27851969f},{-0.963776052f,-0.266712755f},{-0.966976464f,-0.254865646f},
 {-0.970031261f,-0.242980182f},{-0.972939968f,-0.231058106f},{-0.975702107f,-0.219101235f},{-0.97831738f,-0.207111374f},
 {-0.980785251f,-0.195090324f},{-0.983105481f,-0.183039889f},{-0.985277653f,-0.170961887f},{-0.987301409f,-0.15885815f},
 {-0.989176512f,-0.146730468f},{-0.990902662f,-0.134580702f},{-0.992479563f,-0.122410677f},{-0.993906975f,-0.110222206f},
 {-0.99518472f,-0.0980171412f},{-0.996312618f,-0.0857973099f},{-0.997290432f,-0.0735645667f},{-0.998118103f,-0.061320737f},
 {-0.99879545f,-0.0490676761f},{-0.999322355f,-0.0368072242f},{-0.999698818f,-0.024541229f},{-0.999924719f,-0.0122715384f},
 {1.0f,0.f},{0.99988234f,-0.015339206f},{0.999529421f,-0.030674804f},{0.998941302f,-0.0460031815f},
 {0.998118103f,-0.061320737f},{0.997060061f,-0.0766238645f},{0.995767415f,-0.0919089541f},{0.994240463f,-0.107172422f},
 {0.992479563f,-0.122410677f},{0.990485072f,-0.137620121f},{0.988257587f,-0.152797192f},{0.985797524f,-0.167938292f},
 {0.983105481f,-0.183039889f},{0.980182111f,-0.198098406f},{0.977028131f,-0.213110313f},{0.973644257f,-0.228072077f},
 {0.970031261f,-0.242980182f},{0.966189981f,-0.257831097f},{0.962121427f,-0.272621363f},{0.957826436f,-0.287347466f},
 {0.953306019f,-0.302005947f},{0.94856137f,-0.316593379f},{0.943593442f,-0.331106305f},{0.938403547f,-0.345541328f},
 {0.932992816f,-0.359895051f},{0.927362502f,-0.374164075f},{0.921514034f,-0.388345033f},{0.915448725f,-0.402434647f},
 {0.909168005f,-0.416429549f},{0.902673304f,-0.430326492f},{0.895966232f,-0.444122136f},{0.889048338f,-0.457813293f},
 {0.881921291f,-0.471396744f},{0.874586642f,-0.484869242f},{0.867046237f,-0.498227656f},{0.859301805f,-0.511468828f},
 {0.851355195f,-0.524589658f},{0.843208253f,-0.537587047f},{0.834862888f,-0.550457954f},{0.826321065f,-0.563199341f},
 {0.817584813f,-0.575808167f},{0.808656156f,-0.588281572f},{0.799537241f,-0.600616455f},{0.790230215f,-0.612810075f},
 {0.780737221f,-0.624859512f},{0.771060526f,-0.636761844f},{0.761202395f,-0.64851439f},{0.751165152f,-0.660114348f},
 {0.740951121f,-0.671558976f},{0.730562747f,-0.682845533f},{0.720002532f,-0.693971455f},{0.709272802f,-0.704934061f},
 {0.698376238f,-0.715730846f},{0.687315345f,-0.726359129f},{0.676092684f,-0.736816585f},{0.664710999f,-0.747100592f},
 {0.653172851f,-0.757208824f},{0.641481042f,-0.767138898f},{0.629638255f,-0.77688849f},{0.61764729f,-0.786455214f},
 {0.605511069f,-0.795836926f},{0.593232274f,-0.805031359f},{0.580813944f,-0.81403631f},{0.568258941f,-0.82284981f},
 {0.555570245f,-0.831469595f},{0.542750776f,-0.839893818f},{0.529803634f,-0.848120332f},{0.516731799f,-0.856147349f},
 {0.50353837f,-0.863972843f},{0.490226477f,-0.871595085f},{0.47679922f,-0.879012227f},{0.463259786f,-0.886222541f},
 {0.449611336f,-0.893224299f},{0.435857087f,-0.900015891f},{0.422000259f,-0.906595707f},{0.408044159f,-0.912962198f},
 {0.393992037f,-0.919113874f},{0.379847199f,-0.925049245f},{0.365612984f,-0.93076694f},{0.351292759f,-0.936265647f},
 {0.336889863f,-0.941544056f},{0.322407693f,-0.946600914f},{0.307849646f,-0.95143503f},{0.293219149f,-0.95604527f},
 {0.27851969f,-0.960430503f},{0.263754666f,-0.964589775f},{0.248927608f,-0.968522072f},{0.234041959f,-0.972226501f},
 {0.219101235f,-0.975702107f},{0.204108968f,-0.978948176f},{0.18906866f,-0.981963873f},{0.173983872f,-0.984748483f},
 {0.15885815f,-0.987301409f},{0.143695027f,-0.989621997f},{0.128498107f,-0.991709769f},{0.113270953f,-0.993564129f},
 {0.0980171412f,-0.99518472f},{0.0827402622f,-0.996571124f},{0.0674439222f,-0.997723043f},{0.052131705f,-0.998640239f},
 {0.0368072242f,-0.999322355f},{0.0214740802f,-0.99976939f},{0.00613588467f,-0.999981165f},{-0.00920375437f,-0.999957621f},
 {-0.024541229f,-0.999698818f},{-0.0398729257f,-0.999204755f},{-0.0551952459f,-0.998475552f},{-0.070504576f,-0.997511446f},
 {-0.0857973099f,-0.996312618f},{-0.10106986f,-0.994879305f},{-0.116318628f,-0.993211925f},{-0.13154003f,-0.991310835f},
 {-0.146730468f,-0.989176512f},{-0.161886394f,-0.986809373f},{-0.177004218f,-0.984210074f},{-0.192080393f,-0.981379211f},
 {-0.207111374f,-0.97831738f},{-0.222093627f,-0.975025356f},{-0.237023607f,-0.971503913f},{-0.251897812f,-0.967753828f},
 {-0.266712755f,-0.963776052f},{-0.281464934f,-0.95957154f},{-0.296150893f,-0.955141187f},{-0.310767144f,-0.950486064f},
 {-0.32531029f,-0.945607305f},{-0.339776874f,-0.940506041f},{-0.354163527f,-0.935183525f},{-0.368466824f,-0.929640889f},
 {-0.382683426f,-0.923879504f},{-0.396809995f,-0.917900801f},{-0.410843164f,-0.91170603f},{-0.424779683f,-0.905296743f},
 {-0.438616246f,-0.898674488f},{-0.452349573f,-0.891840696f},{-0.465976506f,-0.884797096f},{-0.479493767f,-0.877545297f},
 {-0.492898196f,-0.870086968f},{-0.506186664f,-0.862423956f},{-0.519356012f,-0.854557991f},{-0.532403111f,-0.84649092f},
 {-0.545324981f,-0.838224709f},{-0.558118522f,-0.829761207f},{-0.570780754f,-0.8211025f},{-0.583308637f,-0.812250614f},
 {-0.59569931f,-0.803207517f},{-0.607949793f,-0.793975472f},{-0.620057225f,-0.784556568f},{-0.632018745f,-0.774953127f},
 {-0.643831551f,-0.765167236f},{-0.655492842f,-0.755201399f},{-0.666999936f,-0.745057762f},{-0.678350031f,-0.734738886f},
 {-0.689540565f,-0.724247098f},{-0.700568795f,-0.71358484f},{-0.711432219f,-0.702754736f},{-0.722128212f,-0.691759229f},
 {-0.732654274f,-0.680601001f},{-0.743007958f,-0.669282615f},{-0.753186822f,-0.657806695f},{-0.763188422f,-0.64617604f},
 {-0.773010433f,-0.634393275f},{-0.78265059f,-0.622461259f},{-0.792106569f,-0.610382795f},{-0.801376164f,-0.598160684f},
 {-0.81045717f,-0.585797846f},{-0.819347501f,-0.573297143f},{-0.82804507f,-0.560661554f},{-0.836547732f,-0.547894061f},
 {-0.84485358f,-0.534997642f},{-0.852960587f,-0.521975279f},{-0.860866964f,-0.50883013f},{-0.868570685f,-0.495565265f},
 {-0.876070082f,-0.482183784f},{-0.883363366f,-0.468688816f},{-0.890448749f,-0.455083579f},{-0.897324562f,-0.441371262f},
 {-0.903989315f,-0.427555084f},{-0.910441279f,-0.413638324f},{-0.916679084f,-0.399624199f},{-0.92270112f,-0.385516047f},
 {-0.928506076f,-0.371317208f},{-0.934092522f,-0.357030958f},{-0.939459205f,-0.342660725f},{-0.944604814f,-0.328209847f},
 {-0.949528158f,-0.313681751f},{-0.954228103f,-0.299079835f},{-0.958703458f,-0.284407526f},{-0.962953269f,-0.269668311f},
 {-0.966976464f,-0.254865646f},{-0.970772147f,-0.24000302f},{-0.974339366f,-0.225083917f},{-0.977677345f,-0.210111842f},
 {-0.980785251f,-0.195090324f},{-0.983662426f,-0.180022895f},{-0.986308098f,-0.164913118f},{-0.988721669f,-0.149764538f},
 {-0.990902662f,-0.134580702f},{-0.992850423f,-0.119365215f},{-0.994564593f,-0.104121633f},{-0.996044695f,-0.0888535529f},
 {-0.997290432f,-0.0735645667f},{-0.998301566f,-0.0582582653f},{-0.999077737f,-0.0429382585f},{-0.999618828f,-0.027608145f},
 {-0.999924719f,-0.0122715384f},{-0.999995291f,0.00306795677f},{-0.999830604f,0.0184067301f},{-0.999430597f,0.0337411724f},
 {-0.99879545f,0.0490676761f},{-0.997925282f,0.0643826276f},{-0.996820271f,0.0796824396f},{-0.995480776f,0.0949634984f},
 {-0.993906975f,0.110222206f},{-0.992099285f,0.125454977f},{-0.990058184f,0.140658244f},{-0.987784147f,0.155828401f},
 {-0.985277653f,0.170961887f},{-0.982539296f,0.186055154f},{-0.979569793f,0.201104641f},{-0.976369739f,0.216106802f},
 {-0.972939968f,0.231058106f},{-0.969281256f,0.24595505f},{-0.965394437f,0.260794103f},{-0.961280465f,0.275571823f},
 {-0.956940353f,0.290284663f},{-0.952374995f,0.304929227f},{-0.947585583f,0.319502026f},{-0.94257319f,0.333999664f},
 {-0.937339008f,0.348418683f},{-0.931884289f,0.362755716f},{-0.926210225f,0.377007425f},{-0.920318305f,0.391170382f},
 {-0.914209783f,0.405241311f},{-0.907886088f,0.419216901f},{-0.901348829f,0.433093816f},{-0.894599497f,0.446868837f},
 {-0.887639642f,0.460538715f},{-0.880470872f,0.474100202f},{-0.873094976f,0.487550169f},{-0.865513623f,0.500885367f},
 {-0.857728601f,0.514102757f},{-0.849741757f,0.527199149f},{-0.841554999f,0.540171444f},{-0.833170176f,0.553016722f},
 {-0.824589312f,0.565731823f},{-0.815814435f,0.578313768f},{-0.806847572f,0.590759695f},{-0.797690868f,0.603066623f},
 {-0.78834641f,0.615231574f},{-0.778816521f,0.627251804f},{-0.769103348f,0.639124453f},{-0.759209216f,0.65084666f},
 {-0.749136388f,0.662415802f},{-0.73888731f,0.673829019f},{-0.728464365f,0.685083687f},{-0.717870057f,0.696177125f},
 {1.0f,0.f},{0.999830604f,-0.0184067301f},{0.999322355f,-0.0368072242f},{0.998475552f,-0.0551952459f},
 {0.997290432f,-0.0735645667f},{0.995767415f,-0.0919089541f},{0.993906975f,-0.110222206f},{0.991709769f,-0.128498107f},
 {0.989176512f,-0.146730468f},{0.986308098f,-0.164913118f},{0.983105481f,-0.183039889f},{0.979569793f,-0.201104641f},
 {0.975702107f,-0.219101235f},{0.971503913f,-0.237023607f},{0.966976464f,-0.254865646f},{0.962121427f,-0.272621363f},
 {0.956940353f,-0.290284663f},{0.95143503f,-0.307849646f},{0.945607305f,-0.32531029f},{0.939459205f,-0.342660725f},
 {0.932992816f,-0.359895051f},{0.926210225f,-0.377007425f},{0.919113874f,-0.393992037f},{0.91170603f,-0.410843164f},
 {0.903989315f,-0.427555084f},{0.895966232f,-0.444122136f},{0.887639642f,-0.460538715f},{0.879012227f,-0.47679922f},
 {0.870086968f,-0.492898196f},{0.860866964f,-0.50883013f},{0.851355195f,-0.524589658f},{0.841554999f,-0.540171444f},
 {0.831469595f,-0.555570245f},{0.8211025f,-0.570780754f},{0.81045717f,-0.585797846f},{0.799537241f,-0.600616455f},
 {0.78834641f,-0.615231574f},{0.77688849f,-0.629638255f},{0.765167236f,-0.643831551f},{0.753186822f,-0.657806695f},
 {0.740951121f,-0.671558976f},{0.728464365f,-0.685083687f},{0.715730846f,-0.698376238f},{0.702754736f,-0.711432219f},
 {0.689540565f,-0.724247098f},{0.676092684f,-0.736816585f},{0.662415802f,-0.749136388f},{0.64851439f,-0.761202395f},
 {0.634393275f,-0.773010433f},{0.620057225f,-0.784556568f},{0.605511069f,-0.795836926f},{0.590759695f,-0.806847572f},
 {0.575808167f,-0.817584813f},{0.560661554f,-0.82804507f},{0.545324981f,-0.838224709f},{0.529803634f,-0.848120332f},
 {0.514102757f,-0.857728601f},{0.498227656f,-0.867046237f},{0.482183784f,-0.876070082f},{0.465976506f,-0.884797096f},
 {0.449611336f,-0.893224299f},{0.433093816f,-0.901348829f},{0.416429549f,-0.909168005f},{0.399624199f,-0.916679084f},
 {0.382683426f,-0.923879504f},{0.365612984f,-0.93076694f},{0.348418683f,-0.937339008f},{0.331106305f,-0.943593442f},
 {0.313681751f,-0.949528158f},{0.296150893f,-0.955141187f},{0.27851969f,-0.960430503f},{0.260794103f,-0.965394437f},
 {0.242980182f,-0.970031261f},{0.225083917f,-0.974339366f},{0.207111374f,-0.97831738f},{0.18906866f,-0.981963873f},
 {0.170961887f,-0.985277653f},{0.152797192f,-0.988257587f},{0.134580702f,-0.990902662f},{0.116318628f,-0.993211925f},
 {0.0980171412f,-0.99518472f},{0.0796824396f,-0.996820271f},{0.061320737f,-0.998118103f},{0.0429382585f,-0.999077737f},
 {0.024541229f,-0.999698818f},{0.00613588467f,-0.999981165f},{-0.0122715384f,-0.999924719f},{-0.030674804f,-0.999529421f},
 {-0.0490676761f,-0.99879545f},{-0.0674439222f,-0.997723043f},{-0.0857973099f,-0.996312618f},{-0.104121633f,-0.994564593f},
 {-0.122410677f,-0.992479563f},{-0.140658244f,-0.990058184f},{-0.15885815f,-0.987301409f},{-0.177004218f,-0.984210074f},
 {-0.195090324f,-0.980785251f},{-0.213110313f,-0.977028131f},{-0.231058106f,-0.972939968f},{-0.248927608f,-0.968522072f},
 {-0.266712755f,-0.963776052f},{-0.284407526f,-0.958703458f},{-0.302005947f,-0.953306019f},{-0.319502026f,-0.947585583f},
 {-0.336889863f,-0.941544056f},{-0.354163527f,-0.935183525f},{-0.371317208f,-0.928506076f},{-0.388345033f,-0.921514034f},
 {-0.405241311f,-0.914209783f},{-0.422000259f,-0.906595707f},{-0.438616246f,-0.898674488f},{-0.455083579f,-0.890448749f},
 {-0.471396744f,-0.881921291f},{-0.487550169f,-0.873094976f},{-0.50353837f,-0.863972843f},{-0.519356012f,-0.854557991f},
 {-0.534997642f,-0.84485358f},{-0.550457954f,-0.834862888f},{-0.565731823f,-0.824589312f},{-0.580813944f,-0.81403631f},
 {-0.59569931f,-0.803207517f},{-0.610382795f,-0.792106569f},{-0.624859512f,-0.780737221f},{-0.639124453f,-0.769103348f},
 {-0.653172851f,-0.757208824f},{-0.666999936f,-0.745057762f},{-0.680601001f,-0.732654274f},{-0.693971455f,-0.720002532f},
 {-0.707106769f,-0.707106769f},{-0.720002532f,-0.693971455f},{-0.732654274f,-0.680601001f},{-0.745057762f,-0.666999936f},
 {-0.757208824f,-0.653172851f},{-0.769103348f,-0.639124453f},{-0.780737221f,-0.624859512f},{-0.792106569f,-0.610382795f},
 {-0.803207517f,-0.59569931f},{-0.81403631f,-0.580813944f},{-0.824589312f,-0.565731823f},{-0.834862888f,-0.550457954f},
 {-0.84485358f,-0.534997642f},{-0.854557991f,-0.519356012f},{-0.863972843f,-0.50353837f},{-0.873094976f,-0.487550169f},
 {-0.881921291f,-0.471396744f},{-0.890448749f,-0.455083579f},{-0.898674488f,-0.438616246f},{-0.906595707f,-0.422000259f},
 {-0.914209783f,-0.405241311f},{-0.921514034f,-0.388345033f},{-0.928506076f,-0.371317208f},{-0.935183525f,-0.354163527f},
 {-0.941544056f,-0.336889863f},{-0.947585583f,-0.319502026f},{-0.953306019f,-0.302005947f},{-0.958703458f,-0.284407526f},
 {-0.963776052f,-0.266712755f},{-0.968522072f,-0.248927608f},{-0.972939968f,-0.231058106f},{-0.977028131f,-0.213110313f},
 {-0.980785251f,-0.195090324f},{-0.984210074f,-0.177004218f},{-0.987301409f,-0.15885815f},{-0.990058184f,-0.140658244f},
 {-0.992479563f,-0.122410677f},{-0.994564593f,-0.104121633f},{-0.996312618f,-0.0857973099f},{-0.997723043f,-0.0674439222f},
 {-0.99879545f,-0.0490676761f},{-0.999529421f,-0.030674804f},{-0.999924719f,-0.0122715384f},{-0.999981165f,0.00613588467f},
 {-0.999698818f,0.024541229f},{-0.999077737f,0.0429382585f},{-0.998118103f,0.061320737f},{-0.996820271f,0.0796824396f},
 {-0.99518472f,0.0980171412f},{-0.993211925f,0.116318628f},{-0.990902662f,0.134580702f},{-0.988257587f,0.152797192f},
 {-0.985277653f,0.170961887f},{-0.981963873f,0.18906866f},{-0.97831738f,0.207111374f},{-0.974339366f,0.225083917f},
 {-0.970031261f,0.242980182f},{-0.965394437f,0.260794103f},{-0.960430503f,0.27851969f},{-0.955141187f,0.296150893f},
 {-0.949528158f,0.313681751f},{-0.943593442f,0.331106305f},{-0.937339008f,0.348418683f},{-0.93076694f,0.365612984f},
 {-0.923879504f,0.382683426f},{-0.916679084f,0.399624199f},{-0.909168005f,0.416429549f},{-0.901348829f,0.433093816f},
 {-0.893224299f,0.449611336f},{-0.884797096f,0.465976506f},{-0.876070082f,0.482183784f},{-0.867046237f,0.498227656f},
 {-0.857728601f,0.514102757f},{-0.848120332f,0.529803634f},{-0.838224709f,0.545324981f},{-0.82804507f,0.560661554f},
 {-0.817584813f,0.575808167f},{-0.806847572f,0.590759695f},{-0.795836926f,0.605511069f},{-0.784556568f,0.620057225f},
 {-0.773010433f,0.634393275f},{-0.761202395f,0.64851439f},{-0.749136388f,0.662415802f},{-0.736816585f,0.676092684f},
 {-0.724247098f,0.689540565f},{-0.711432219f,0.702754736f},{-0.698376238f,0.715730846f},{-0.685083687f,0.728464365f},
 {-0.671558976f,0.740951121f},{-0.657806695f,0.753186822f},{-0.643831551f,0.765167236f},{-0.629638255f,0.77688849f},
 {-0.615231574f,0.78834641f},{-0.600616455f,0.799537241f},{-0.585797846f,0.81045717f},{-0.570780754f,0.8211025f},
 {-0.555570245f,0.831469595f},{-0.540171444f,0.841554999f},{-0.524589658f,0.851355195f},{-0.50883013f,0.860866964f},
 {-0.492898196f,0.870086968f},{-0.47679922f,0.879012227f},{-0.460538715f,0.887639642f},{-0.444122136f,0.895966232f},
 {-0.427555084f,0.903989315f},{-0.410843164f,0.91170603f},{-0.393992037f,0.919113874f},{-0.377007425f,0.926210225f},
 {-0.359895051f,0.932992816f},{-0.342660725f,0.939459205f},{-0.32531029f,0.945607305f},{-0.307849646f,0.95143503f},
 {-0.290284663f,0.956940353f},{-0.272621363f,0.962121427f},{-0.254865646f,0.966976464f},{-0.237023607f,0.971503913f},
 {-0.219101235f,0.975702107f},{-0.201104641f,0.979569793f},{-0.183039889f,0.983105481f},{-0.164913118f,0.986308098f},
 {-0.146730468f,0.989176512f},{-0.128498107f,0.991709769f},{-0.110222206f,0.993906975f},{-0.0919089541f,0.995767415f},
 {-0.0735645667f,0.997290432f},{-0.0551952459f,0.998475552f},{-0.0368072242f,0.999322355f},{-0.0184067301f,0.999830604f},
 {1.0f,0.f},{0.99976939f,-0.0214740802f},{0.999077737f,-0.0429382585f},{0.997925282f,-0.0643826276f},
 {0.996312618f,-0.0857973099f},{0.994240463f,-0.107172422f},{0.991709769f,-0.128498107f},{0.988721669f,-0.149764538f},
 {0.985277653f,-0.170961887f},{0.981379211f,-0.192080393f},{0.977028131f,-0.213110313f},{0.972226501f,-0.234041959f},
 {0.966976464f,-0.254865646f},{0.961280465f,-0.275571823f},{0.955141187f,-0.296150893f},{0.94856137f,-0.316593379f},
 {0.941544056f,-0.336889863f},{0.934092522f,-0.357030958f},{0.926210225f,-0.377007425f},{0.917900801f,-0.396809995f},
 {0.909168005f,-0.416429549f},{0.900015891f,-0.435857087f},{0.890448749f,-0.455083579f},{0.880470872f,-0.474100202f},
 {0.870086968f,-0.492898196f},{0.859301805f,-0.511468828f},{0.848120332f,-0.529803634f},{0.836547732f,-0.547894061f},
 {0.824589312f,-0.565731823f},{0.812250614f,-0.583308637f},{0.799537241f,-0.600616455f},{0.786455214f,-0.61764729f},
 {0.773010433f,-0.634393275f},{0.759209216f,-0.65084666f},{0.745057762f,-0.666999936f},{0.730562747f,-0.682845533f},
 {0.715730846f,-0.698376238f},{0.700568795f,-0.71358484f},{0.685083687f,-0.728464365f},{0.669282615f,-0.743007958f},
 {0.653172851f,-0.757208824f},{0.636761844f,-0.771060526f},{0.620057225f,-0.784556568f},{0.603066623f,-0.797690868f},
 {0.585797846f,-0.81045717f},{0.568258941f,-0.82284981f},{0.550457954f,-0.834862888f},{0.532403111f,-0.84649092f},
 {0.514102757f,-0.857728601f},{0.495565265f,-0.868570685f},{0.47679922f,-0.879012227f},{0.457813293f,-0.889048338f},
 {0.438616246f,-0.898674488f},{0.419216901f,-0.907886088f},{0.399624199f,-0.916679084f},{0.379847199f,-0.925049245f},
 {0.359895051f,-0.932992816f},{0.339776874f,-0.940506041f},{0.319502026f,-0.947585583f},{0.299079835f,-0.954228103f},
 {0.27851969f,-0.960430503f},{0.257831097f,-0.966189981f},{0.237023607f,-0.971503913f},{0.216106802f,-0.976369739f},
 {0.195090324f,-0.980785251f},{0.173983872f,-0.984748483f},{0.152797192f,-0.988257587f},{0.13154003f,-0.991310835f},
 {0.110222206f,-0.993906975f},{0.0888535529f,-0.996044695f},{0.0674439222f,-0.997723043f},{0.0460031815f,-0.998941302f},
 {0.024541229f,-0.999698818f},{0.00306795677f,-0.999995291f},{-0.0184067301f,-0.999830604f},{-0.0398729257f,-0.999204755f},
 {-0.061320737f,-0.998118103f},{-0.0827402622f,-0.996571124f},{-0.104121633f,-0.994564593f},{-0.125454977f,-0.992099285f},
 {-0.146730468f,-0.989176512f},{-0.167938292f,-0.985797524f},{-0.18906866f,-0.981963873f},{-0.210111842f,-0.977677345f},
 {-0.231058106f,-0.972939968f},{-0.251897812f,-0.967753828f},{-0.272621363f,-0.962121427f},{-0.293219149f,-0.95604527f},
 {-0.313681751f,-0.949528158f},{-0.333999664f,-0.94257319f},{-0.354163527f,-0.935183525f},{-0.374164075f,-0.927362502f},
 {-0.393992037f,-0.919113874f},{-0.413638324f,-0.910441279f},{-0.433093816f,-0.901348829f},{-0.452349573f,-0.891840696f},
 {-0.471396744f,-0.881921291f},{-0.490226477f,-0.871595085f},{-0.50883013f,-0.860866964f},{-0.527199149f,-0.849741757f},
 {-0.545324981f,-0.838224709f},{-0.563199341f,-0.826321065f},{-0.580813944f,-0.81403631f},{-0.598160684f,-0.801376164f},
 {-0.615231574f,-0.78834641f},{-0.632018745f,-0.774953127f},{-0.64851439f,-0.761202395f},{-0.664710999f,-0.747100592f},
 {-0.680601001f,-0.732654274f},{-0.696177125f,-0.717870057f},{-0.711432219f,-0.702754736f},{-0.726359129f,-0.687315345f},
 {-0.740951121f,-0.671558976f},{-0.755201399f,-0.655492842f},{-0.769103348f,-0.639124453f},{-0.78265059f,-0.622461259f},
 {-0.795836926f,-0.605511069f},{-0.808656156f,-0.588281572f},{-0.8211025f,-0.570780754f},{-0.833170176f,-0.553016722f},
 {-0.84485358f,-0.534997642f},{-0.856147349f,-0.516731799f},{-0.867046237f,-0.498227656f},{-0.877545297f,-0.479493767f},
 {-0.887639642f,-0.460538715f},{-0.897324562f,-0.441371262f},{-0.906595707f,-0.422000259f},{-0.915448725f,-0.402434647f},
 {-0.923879504f,-0.382683426f},{-0.931884289f,-0.362755716f},{-0.939459205f,-0.342660725f},{-0.946600914f,-0.322407693f},
 {-0.953306019f,-0.302005947f},{-0.95957154f,-0.281464934f},{-0.965394437f,-0.260794103f},{-0.970772147f,-0.24000302f},
 {-0.975702107f,-0.219101235f},{-0.980182111f,-0.198098406f},{-0.984210074f,-0.177004218f},{-0.987784147f,-0.155828401f},
 {-0.990902662f,-0.134580702f},{-0.993564129f,-0.113270953f},{-0.995767415f,-0.0919089541f},{-0.997511446f,-0.070504576f},
 {-0.99879545f,-0.0490676761f},{-0.999618828f,-0.027608145f},{-0.999981165f,-0.00613588467f},{-0.99988234f,0.015339206f},
 {-0.999322355f,0.0368072242f},{-0.998301566f,0.0582582653f},{-0.996820271f,0.0796824396f},{-0.994879305f,0.10106986f},
 {-0.992479563f,0.122410677f},{-0.989621997f,0.143695027f},{-0.986308098f,0.164913118f},{-0.982539296f,0.186055154f},
 {-0.97831738f,0.207111374f},{-0.973644257f,0.228072077f},{-0.968522072f,0.248927608f},{-0.962953269f,0.269668311f},
 {-0.956940353f,0.290284663f},{-0.950486064f,0.310767144f},{-0.943593442f,0.331106305f},{-0.936265647f,0.351292759f},
 {-0.928506076f,0.371317208f},{-0.920318305f,0.391170382f},{-0.91170603f,0.410843164f},{-0.902673304f,0.430326492f},
 {-0.893224299f,0.449611336f},{-0.883363366f,0.468688816f},{-0.873094976f,0.487550169f},{-0.862423956f,0.506186664f},
 {-0.851355195f,0.524589658f},{-0.839893818f,0.542750776f},{-0.82804507f,0.560661554f},{-0.815814435f,0.578313768f},
 {-0.803207517f,0.59569931f},{-0.790230215f,0.612810075f},{-0.77688849f,0.629638255f},{-0.763188422f,0.64617604f},
 {-0.749136388f,0.662415802f},{-0.734738886f,0.678350031f},{-0.720002532f,0.693971455f},{-0.704934061f,0.709272802f},
 {-0.689540565f,0.724247098f},{-0.673829019f,0.73888731f},{-0.657806695f,0.753186822f},{-0.641481042f,0.767138898f},
 {-0.624859512f,0.780737221f},{-0.607949793f,0.793975472f},{-0.590759695f,0.806847572f},{-0.573297143f,0.819347501f},
 {-0.555570245f,0.831469595f},{-0.537587047f,0.843208253f},{-0.519356012f,0.854557991f},{-0.500885367f,0.865513623f},
 {-0.482183784f,0.876070082f},{-0.463259786f,0.886222541f},{-0.444122136f,0.895966232f},{-0.424779683f,0.905296743f},
 {-0.405241311f,0.914209783f},{-0.385516047f,0.92270112f},{-0.365612984f,0.93076694f},{-0.345541328f,0.938403547f},
 {-0.32531029f,0.945607305f},{-0.304929227f,0.952374995f},{-0.284407526f,0.958703458f},{-0.263754666f,0.964589775f},
 {-0.242980182f,0.970031261f},{-0.222093627f,0.975025356f},{-0.201104641f,0.979569793f},{-0.180022895f,0.983662426f},
 {-0.15885815f,0.987301409f},{-0.137620121f,0.990485072f},{-0.116318628f,0.993211925f},{-0.0949634984f,0.995480776f},
 {-0.0735645667f,0.997290432f},{-0.052131705f,0.998640239f},{-0.030674804f,0.999529421f},{-0.00920375437f,0.999957621f},
 {0.0122715384f,0.999924719f},{0.0337411724f,0.999430597f},{0.0551952459f,0.998475552f},{0.0766238645f,0.997060061f},
 {0.0980171412f,0.99518472f},{0.119365215f,0.992850423f},{0.140658244f,0.990058184f},{0.161886394f,0.986809373f},
 {0.183039889f,0.983105481f},{0.204108968f,0.978948176f},{0.225083917f,0.974339366f},{0.24595505f,0.969281256f},
 {0.266712755f,0.963776052f},{0.287347466f,0.957826436f},{0.307849646f,0.95143503f},{0.328209847f,0.944604814f},
 {0.348418683f,0.937339008f},{0.368466824f,0.929640889f},{0.388345033f,0.921514034f},{0.408044159f,0.912962198f},
 {0.427555084f,0.903989315f},{0.446868837f,0.894599497f},{0.465976506f,0.884797096f},{0.484869242f,0.874586642f},
 {0.50353837f,0.863972843f},{0.521975279f,0.852960587f},{0.540171444f,0.841554999f},{0.558118522f,0.829761207f},
 {0.575808167f,0.817584813f},{0.593232274f,0.805031359f},{0.610382795f,0.792106569f},{0.627251804f,0.778816521f},
 {0.643831551f,0.765167236f},{0.660114348f,0.751165152f},{0.676092684f,0.736816585f},{0.691759229f,0.722128212f},
};
__device__ const cf T2B[224] = {
 {1.0f,0.f},{0.999698818f,-0.024541229f},{0.99879545f,-0.0490676761f},{0.997290432f,-0.0735645667f},
 {0.99518472f,-0.0980171412f},{0.992479563f,-0.122410677f},{0.989176512f,-0.146730468f},{0.985277653f,-0.170961887f},
 {0.980785251f,-0.195090324f},{0.975702107f,-0.219101235f},{0.970031261f,-0.242980182f},{0.963776052f,-0.266712755f},
 {0.956940353f,-0.290284663f},{0.949528158f,-0.313681751f},{0.941544056f,-0.336889863f},{0.932992816f,-0.359895051f},
 {0.923879504f,-0.382683426f},{0.914209783f,-0.405241311f},{0.903989315f,-0.427555084f},{0.893224299f,-0.449611336f},
 {0.881921291f,-0.471396744f},{0.870086968f,-0.492898196f},{0.857728601f,-0.514102757f},{0.84485358f,-0.534997642f},
 {0.831469595f,-0.555570245f},{0.817584813f,-0.575808167f},{0.803207517f,-0.59569931f},{0.78834641f,-0.615231574f},
 {0.773010433f,-0.634393275f},{0.757208824f,-0.653172851f},{0.740951121f,-0.671558976f},{0.724247098f,-0.689540565f},
 {1.0f,0.f},{0.99879545f,-0.0490676761f},{0.99518472f,-0.0980171412f},{0.989176512f,-0.146730468f},
 {0.980785251f,-0.195090324f},{0.970031261f,-0.242980182f},{0.956940353f,-0.290284663f},{0.941544056f,-0.336889863f},
 {0.923879504f,-0.382683426f},{0.903989315f,-0.427555084f},{0.881921291f,-0.471396744f},{0.857728601f,-0.514102757f},
 {0.831469595f,-0.555570245f},{0.803207517f,-0.59569931f},{0.773010433f,-0.634393275f},{0.740951121f,-0.671558976f},
 {0.707106769f,-0.707106769f},{0.671558976f,-0.740951121f},{0.634393275f,-0.773010433f},{0.59569931f,-0.803207517f},
 {0.555570245f,-0.831469595f},{0.514102757f,-0.857728601f},{0.471396744f,-0.881921291f},{0.427555084f,-0.903989315f},
 {0.382683426f,-0.923879504f},{0.336889863f,-0.941544056f},{0.290284663f,-0.956940353f},{0.242980182f,-0.970031261f},
 {0.195090324f,-0.980785251f},{0.146730468f,-0.989176512f},{0.0980171412f,-0.99518472f},{0.0490676761f,-0.99879545f},
 {1.0f,0.f},{0.997290432f,-0.0735645667f},{0.989176512f,-0.146730468f},{0.975702107f,-0.219101235f},
 {0.956940353f,-0.290284663f},{0.932992816f,-0.359895051f},{0.903989315f,-0.427555084f},{0.870086968f,-0.492898196f},
 {0.831469595f,-0.555570245f},{0.78834641f,-0.615231574f},{0.740951121f,-0.671558976f},{0.689540565f,-0.724247098f},
 {0.634393275f,-0.773010433f},{0.575808167f,-0.817584813f},{0.514102757f,-0.857728601f},{0.449611336f,-0.893224299f},
 {0.382683426f,-0.923879504f},{0.313681751f,-0.949528158f},{0.242980182f,-0.970031261f},{0.170961887f,-0.985277653f},
 {0.0980171412f,-0.99518472f},{0.024541229f,-0.999698818f},{-0.0490676761f,-0.99879545f},{-0.122410677f,-0.992479563f},
 {-0.195090324f,-0.980785251f},{-0.266712755f,-0.963776052f},{-0.336889863f,-0.941544056f},{-0.405241311f,-0.914209783f},
 {-0.471396744f,-0.881921291f},{-0.534997642f,-0.84485358f},{-0.59569931f,-0.803207517f},{-0.653172851f,-0.757208824f},
 {1.0f,0.f},{0.99518472f,-0.0980171412f},{0.980785251f,-0.195090324f},{0.956940353f,-0.290284663f},
 {0.923879504f,-0.382683426f},{0.881921291f,-0.471396744f},{0.831469595f,-0.555570245f},{0.773010433f,-0.634393275f},
 {0.707106769f,-0.707106769f},{0.634393275f,-0.773010433f},{0.555570245f,-0.831469595f},{0.471396744f,-0.881921291f},
 {0.382683426f,-0.923879504f},{0.290284663f,-0.956940353f},{0.195090324f,-0.980785251f},{0.0980171412f,-0.99518472f},
 {6.12323426e-17f,-1.0f},{-0.0980171412f,-0.99518472f},{-0.195090324f,-0.980785251f},{-0.290284663f,-0.956940353f},
 {-0.382683426f,-0.923879504f},{-0.471396744f,-0.881921291f},{-0.555570245f,-0.831469595f},{-0.634393275f,-0.773010433f},
 {-0.707106769f,-0.707106769f},{-0.773010433f,-0.634393275f},{-0.831469595f,-0.555570245f},{-0.881921291f,-0.471396744f},
 {-0.923879504f,-0.382683426f},{-0.956940353f,-0.290284663f},{-0.980785251f,-0.195090324f},{-0.99518472f,-0.0980171412f},
 {1.0f,0.f},{0.992479563f,-0.122410677f},{0.970031261f,-0.242980182f},{0.932992816f,-0.359895051f},
 {0.881921291f,-0.471396744f},{0.817584813f,-0.575808167f},{0.740951121f,-0.671558976f},{0.653172851f,-0.757208824f},
 {0.555570245f,-0.831469595f},{0.449611336f,-0.893224299f},{0.336889863f,-0.941544056f},{0.219101235f,-0.975702107f},
 {0.0980171412f,-0.99518472f},{-0.024541229f,-0.999698818f},{-0.146730468f,-0.989176512f},{-0.266712755f,-0.963776052f},
 {-0.382683426f,-0.923879504f},{-0.492898196f,-0.870086968f},{-0.59569931f,-0.803207517f},{-0.689540565f,-0.724247098f},
 {-0.773010433f,-0.634393275f},{-0.84485358f,-0.534997642f},{-0.903989315f,-0.427555084f},{-0.949528158f,-0.313681751f},
 {-0.980785251f,-0.195090324f},{-0.997290432f,-0.0735645667f},{-0.99879545f,0.0490676761f},{-0.985277653f,0.170961887f},
 {-0.956940353f,0.290284663f},{-0.914209783f,0.405241311f},{-0.857728601f,0.514102757f},{-0.78834641f,0.615231574f},
 {1.0f,0.f},{0.989176512f,-0.146730468f},{0.956940353f,-0.290284663f},{0.903989315f,-0.427555084f},
 {0.831469595f,-0.555570245f},{0.740951121f,-0.671558976f},{0.634393275f,-0.773010433f},{0.514102757f,-0.857728601f},
 {0.382683426f,-0.923879504f},{0.242980182f,-0.970031261f},{0.0980171412f,-0.99518472f},{-0.0490676761f,-0.99879545f},
 {-0.195090324f,-0.980785251f},{-0.336889863f,-0.941544056f},{-0.471396744f,-0.881921291f},{-0.59569931f,-0.803207517f},
 {-0.707106769f,-0.707106769f},{-0.803207517f,-0.59569931f},{-0.881921291f,-0.471396744f},{-0.941544056f,-0.336889863f},
 {-0.980785251f,-0.195090324f},{-0.99879545f,-0.0490676761f},{-0.99518472f,0.0980171412f},{-0.970031261f,0.242980182f},
 {-0.923879504f,0.382683426f},{-0.857728601f,0.514102757f},{-0.773010433f,0.634393275f},{-0.671558976f,0.740951121f},
 {-0.555570245f,0.831469595f},{-0.427555084f,0.903989315f},{-0.290284663f,0.956940353f},{-0.146730468f,0.989176512f},
 {1.0f,0.f},{0.985277653f,-0.170961887f},{0.941544056f,-0.336889863f},{0.870086968f,-0.492898196f},
 {0.773010433f,-0.634393275f},{0.653172851f,-0.757208824f},{0.514102757f,-0.857728601f},{0.359895051f,-0.932992816f},
 {0.195090324f,-0.980785251f},{0.024541229f,-0.999698818f},{-0.146730468f,-0.989176512f},{-0.313681751f,-0.949528158f},
 {-0.471396744f,-0.881921291f},{-0.615231574f,-0.78834641f},{-0.740951121f,-0.671558976f},{-0.84485358f,-0.534997642f},
 {-0.923879504f,-0.382683426f},{-0.975702107f,-0.219101235f},{-0.99879545f,-0.0490676761f},{-0.992479563f,0.122410677f},
 {-0.956940353f,0.290284663f},{-0.893224299f,0.449611336f},{-0.803207517f,0.59569931f},{-0.689540565f,0.724247098f},
 {-0.555570245f,0.831469595f},{-0.405241311f,0.914209783f},{-0.242980182f,0.970031261f},{-0.0735645667f,0.997290432f},
 {0.0980171412f,0.99518472f},{0.266712755f,0.963776052f},{0.427555084f,0.903989315f},{0.575808167f,0.817584813f},
};
__device__ const cf T2C[28] = {
 {1.0f,0.f},{0.980785251f,-0.195090324f},{0.923879504f,-0.382683426f},{0.831469595f,-0.555570245f},
 {1.0f,0.f},{0.923879504f,-0.382683426f},{0.707106769f,-0.707106769f},{0.382683426f,-0.923879504f},
 {1.0f,0.f},{0.831469595f,-0.555570245f},{0.382683426f,-0.923879504f},{-0.195090324f,-0.980785251f},
 {1.0f,0.f},{0.707106769f,-0.707106769f},{6.12323426e-17f,-1.0f},{-0.707106769f,-0.707106769f},
 {1.0f,0.f},{0.555570245f,-0.831469595f},{-0.382683426f,-0.923879504f},{-0.980785251f,-0.195090324f},
 {1.0f,0.f},{0.382683426f,-0.923879504f},{-0.707106769f,-0.707106769f},{-0.923879504f,0.382683426f},
 {1.0f,0.f},{0.195090324f,-0.980785251f},{-0.923879504f,-0.382683426f},{-0.555570245f,0.831469595f},
};

#define DBG_NOMFMA 0
#define DBG_NODMA 0
#define DBG_NOTR 0
#define DBG_TRONLY 0
#define DBG_LGKM0 0
#define DBG_VM0 0
#define DBG_UPFRONT 0
#define DBG_UPW 0
#define DBG_UPX 0
#define DBG_XG 0
#define DBG_XI 2
#define DBG_ACCPIN 0
#define DBG_XLIVE 1
#define DBG_K3WARM 0
#define DBG_PRIO 3
#define DBG_PF3 1
#define DBG_FFTDELAY 0
#define DBG_WPF 1
#define DBG_NOFIRST 0
#define DBG_NOSECOND 0
#if defined(__HIP_DEVICE_COMPILE__)
#pragma clang attribute push (__attribute__((target("no-packed-fp32-ops"))), apply_to = function)
#endif
typedef short bf16x8 __attribute__((ext_vector_type(8)));
typedef short s16x4 __attribute__((ext_vector_type(4)));
typedef float f32x4 __attribute__((ext_vector_type(4)));
typedef float f32x2 __attribute__((ext_vector_type(2)));
typedef unsigned u32x4 __attribute__((ext_vector_type(4)));
typedef unsigned u32x2 __attribute__((ext_vector_type(2)));
typedef __bf16 bf16x2v __attribute__((ext_vector_type(2)));
#define DI __device__ __forceinline__
#define TIDX ((int)__builtin_amdgcn_workitem_id_x())
#define BIDX ((int)__builtin_amdgcn_workgroup_id_x())
#define WG_SYNC() do { __builtin_amdgcn_fence(__ATOMIC_RELEASE, "workgroup"); __builtin_amdgcn_s_barrier(); __builtin_amdgcn_fence(__ATOMIC_ACQUIRE, "workgroup"); } while (0)
DI int lane_id() { int r; asm volatile("v_mbcnt_lo_u32_b32 %0, -1, 0\n\tv_mbcnt_hi_u32_b32 %0, -1, %0" : "=v"(r)); return r; }
DI float u2f(unsigned u) { return __builtin_bit_cast(float, u); }
DI float i2f(int u) { return __builtin_bit_cast(float, u); }
DI int f2i(float f) { return __builtin_bit_cast(int, f); }
DI cf mkc(float x, float y) { cf r; r.x = x; r.y = y; return r; }
#define STAMP(ws, slot, i) do {} while (0)
#define RSTAMP(ws, slot, i) do {} while (0)
#define STAMP2(ws, slot, i) do {} while (0)

constexpr int S_ = 2048, D_ = 448, DF_ = 225, FF_ = 1792;
constexpr size_t OFF_XNF = 0;
constexpr size_t OFF_S1  = OFF_XNF + (size_t)S_ * D_ * 2;
constexpr size_t OFF_WP  = OFF_S1 + (size_t)S_ * DF_ * 8;
constexpr size_t OFF_WDF = OFF_WP + (size_t)S_ * DF_ * 4;
constexpr size_t OFF_H   = OFF_WDF + (size_t)FF_ * D_ * 2;
constexpr size_t OFF_S2  = OFF_H + (size_t)S_ * FF_ * 2;
constexpr size_t OFF_Y   = OFF_S2 + (size_t)S_ * DF_ * 8;
constexpr size_t OFF_CNT = OFF_Y + (size_t)S_ * D_ * 2;

DI unsigned pack_bf16(float lo, float hi) {
  f32x2 v = {lo, hi};
  bf16x2v r = __builtin_convertvector(v, bf16x2v);
  return __builtin_bit_cast(unsigned, r);
}
DI float bf_lo(unsigned u) { return u2f(u << 16); }
DI float bf_hi(unsigned u) { return u2f(u & 0xffff0000u); }
DI float rbf(float f) { return bf_lo(pack_bf16(f, f)); }

DI float fdiv(float n, float d, float r) {
  const float q = n * r;
  return __builtin_fmaf(__builtin_fmaf(-q, d, n), r, q);
}
#define DBG_ST 0
DI void st16(void* p, u32x4 v) {
  if (DBG_ST == 1) __builtin_nontemporal_store(v, (u32x4*)p);
  else if (DBG_ST == 2) asm volatile("global_store_dwordx4 %0, %1, off sc1\n\ts_nop 1" :: "v"(p), "v"(v) : "memory");
  else if (DBG_ST == 3) asm volatile("global_store_dwordx4 %0, %1, off sc0 sc1\n\ts_nop 1" :: "v"(p), "v"(v) : "memory");
  else *(u32x4*)p = v;
}
DI void st8(void* p, u32x2 v) {
  if (DBG_ST == 1) __builtin_nontemporal_store(v, (u32x2*)p);
  else if (DBG_ST == 2) asm volatile("global_store_dwordx2 %0, %1, off sc1" :: "v"(p), "v"(v) : "memory");
  else if (DBG_ST == 3) asm volatile("global_store_dwordx2 %0, %1, off sc0 sc1" :: "v"(p), "v"(v) : "memory");
  else *(u32x2*)p = v;
}
DI void st16_sc1(char* ubase, unsigned off, u32x4 v) {
  asm volatile("global_store_dwordx4 %0, %1, %2 sc1\n\ts_nop 1" :: "v"(off), "v"(v), "s"(ubase) : "memory");
}
typedef __amdgpu_buffer_rsrc_t rsrc_t;
DI rsrc_t mk_rsrc(const char* ubase, unsigned bytes) { return __builtin_amdgcn_make_buffer_rsrc((void*)ubase, 0, bytes, 0x00020000); }
DI u32x4 ld16_sc1(rsrc_t r, unsigned voff, unsigned soff) { return __builtin_bit_cast(u32x4, __builtin_amdgcn_raw_buffer_load_b128(r, voff, soff, 16)); }
DI u32x2 ld8_sc1(rsrc_t r, unsigned voff, unsigned soff) { return __builtin_bit_cast(u32x2, __builtin_amdgcn_raw_buffer_load_b64(r, voff, soff, 16)); }
template <int CTRL, int RMASK>
DI float dpp_add(float v) {
  int r = __builtin_amdgcn_update_dpp(0, f2i(v), CTRL, RMASK, 0xf, true);
  return v + i2f(r);
}
DI float wave_sum(float v) {
  v = dpp_add<0x111, 0xf>(v);
  v = dpp_add<0x112, 0xf>(v);
  v = dpp_add<0x114, 0xf>(v);
  v = dpp_add<0x118, 0xf>(v);
  v = dpp_add<0x142, 0xa>(v);
  v = dpp_add<0x143, 0xc>(v);
  return i2f(__builtin_amdgcn_readlane(f2i(v), 63));
}

DI cf cadd(cf a, cf b) { return mkc(a.x + b.x, a.y + b.y); }
DI cf csub(cf a, cf b) { return mkc(a.x - b.x, a.y - b.y); }
DI cf cmul(cf a, cf b) { return mkc(a.x * b.x - a.y * b.y, a.x * b.y + a.y * b.x); }
DI cf cmulc(cf a, cf b) { return mkc(a.x * b.x + a.y * b.y, a.y * b.x - a.x * b.y); }
template <bool INV> DI cf ctw(cf a, cf w) { return INV ? cmulc(a, w) : cmul(a, w); }
template <bool INV> DI cf rot90(cf a) { return INV ? mkc(-a.y, a.x) : mkc(a.y, -a.x); }

template <bool INV> DI void dft4(cf& c0, cf& c1, cf& c2, cf& c3) {
  cf d0 = cadd(c0, c2), d1 = csub(c0, c2), d2 = cadd(c1, c3), d3 = rot90<INV>(csub(c1, c3));
  c0 = cadd(d0, d2); c2 = csub(d0, d2); c1 = cadd(d1, d3); c3 = csub(d1, d3);
}
template <bool INV> DI void dft8(cf (&a)[8]) {
  float R = 0.70710678118654752f;
  cf b0 = cadd(a[0], a[4]), b4 = csub(a[0], a[4]);
  cf b1 = cadd(a[1], a[5]), b5 = csub(a[1], a[5]);
  cf b2 = cadd(a[2], a[6]), b6 = csub(a[2], a[6]);
  cf b3 = cadd(a[3], a[7]), b7 = csub(a[3], a[7]);
  if (INV) {
    b5 = mkc(R * (b5.x - b5.y), R * (b5.x + b5.y));
    b7 = mkc(-R * (b7.x + b7.y), R * (b7.x - b7.y));
  } else {
    b5 = mkc(R * (b5.x + b5.y), R * (b5.y - b5.x));
    b7 = mkc(R * (b7.y - b7.x), -R * (b7.x + b7.y));
  }
  b6 = rot90<INV>(b6);
  dft4<INV>(b0, b1, b2, b3);
  dft4<INV>(b4, b5, b6, b7);
  a[0] = b0; a[2] = b1; a[4] = b2; a[6] = b3;
  a[1] = b4; a[3] = b5; a[5] = b6; a[7] = b7;
}
template <bool INV> DI void dft7(cf (&z)[7]) {
  const float C1 = 0.62348980185873353f, C2 = -0.22252093395631440f, C3 = -0.90096886790241913f;
  const float S1 = 0.78183148246802981f, S2 = 0.97492791218182361f, S3 = 0.43388373911755812f;
  cf t1 = cadd(z[1], z[6]), t2 = cadd(z[2], z[5]), t3 = cadd(z[3], z[4]);
  cf u1 = csub(z[1], z[6]), u2 = csub(z[2], z[5]), u3 = csub(z[3], z[4]);
  cf z0 = z[0];
  cf a1 = mkc(z0.x + C1 * t1.x + C2 * t2.x + C3 * t3.x, z0.y + C1 * t1.y + C2 * t2.y + C3 * t3.y);
  cf a2 = mkc(z0.x + C2 * t1.x + C3 * t2.x + C1 * t3.x, z0.y + C2 * t1.y + C3 * t2.y + C1 * t3.y);
  cf a3 = mkc(z0.x + C3 * t1.x + C1 * t2.x + C2 * t3.x, z0.y + C3 * t1.y + C1 * t2.y + C2 * t3.y);
  cf b1 = mkc(S1 * u1.x + S2 * u2.x + S3 * u3.x, S1 * u1.y + S2 * u2.y + S3 * u3.y);
  cf b2 = mkc(S2 * u1.x - S3 * u2.x - S1 * u3.x, S2 * u1.y - S3 * u2.y - S1 * u3.y);
  cf b3 = mkc(S3 * u1.x - S1 * u2.x + S2 * u3.x, S3 * u1.y - S1 * u2.y + S2 * u3.y);
  z[0] = mkc(z0.x + t1.x + t2.x + t3.x, z0.y + t1.y + t2.y + t3.y);
  cf r1 = rot90<INV>(b1), r2 = rot90<INV>(b2), r3 = rot90<INV>(b3);
  z[1] = cadd(a1, r1); z[6] = csub(a1, r1);
  z[2] = cadd(a2, r2); z[5] = csub(a2, r2);
  z[3] = cadd(a3, r3); z[4] = csub(a3, r3);
}

#define WAVE_FENCE() do { __builtin_amdgcn_fence(__ATOMIC_RELEASE, "wavefront"); __builtin_amdgcn_wave_barrier(); __builtin_amdgcn_fence(__ATOMIC_ACQUIRE, "wavefront"); } while (0)

template <bool INV>
DI void fft448(cf (&z)[7], cf (&o)[8], const cf (&t1)[6], const cf (&t2)[7], char* buf, int l) {
  dft7<INV>(z);
#pragma unroll
  for (int q = 1; q < 7; ++q) z[q] = ctw<INV>(z[q], t1[q - 1]);
  cf* b1 = (cf*)buf;
#pragma unroll
  for (int q = 0; q < 7; ++q) b1[q * 72 + l] = z[q];
  WAVE_FENCE();
  const int lc = l < 56 ? l : 55;
  const int q = lc >> 3, l0 = lc & 7;
#pragma unroll
  for (int j = 0; j < 8; ++j) o[j] = b1[q * 72 + l0 + 8 * j];
  dft8<INV>(o);
#pragma unroll
  for (int b = 1; b < 8; ++b) o[b] = ctw<INV>(o[b], t2[b - 1]);
  WAVE_FENCE();
  if (l < 56) {
#pragma unroll
    for (int b = 0; b < 8; ++b) *(cf*)(buf + (q * 8 + b) * 80 + l0 * 8) = o[b];
  }
  WAVE_FENCE();
#pragma unroll
  for (int j = 0; j < 4; ++j) {
    f32x4 v = *(const f32x4*)(buf + lc * 80 + j * 16);
    o[2 * j] = mkc(v[0], v[1]); o[2 * j + 1] = mkc(v[2], v[3]);
  }
  dft8<INV>(o);
  WAVE_FENCE();
}
DI void load_tw448(cf (&t1)[6], cf (&t2)[7], int l) {
#pragma unroll
  for (int q = 1; q < 7; ++q) t1[q - 1] = T448A[(q - 1) * 64 + l];
  const int l0 = l & 7;
#pragma unroll
  for (int b = 1; b < 8; ++b) t2[b - 1] = T448B[(b - 1) * 8 + l0];
}

__global__ __launch_bounds__(512) void k1_ln_rowfft(const unsigned short* __restrict__ x, const unsigned short* __restrict__ wr,
                                                    const unsigned short* __restrict__ wi, const unsigned short* __restrict__ wg,
                                                    const unsigned short* __restrict__ wu, const unsigned short* __restrict__ wd, char* __restrict__ ws) {
  __shared__ __attribute__((aligned(16))) char lds[4][4608];
  const int b = BIDX, tid = TIDX, w = __builtin_amdgcn_readfirstlane(tid >> 6), l = tid & 63;
  const int rb8 = (b & 7) * 32 + (b >> 3);
  const int rowa = rb8 * 8 + 2 * (w & 3);
  char* buf = lds[w & 3];
  STAMP(ws, 8192 + BIDX * 8 + (TIDX >> 6), 0);
  if (b == 0 && tid < 32) ((unsigned*)(ws + OFF_CNT))[tid * 32] = 0u;

  if (w >= 4) {
    const int w4 = w - 4;
    const int tg = b >> 2, kq = b & 3;
    const int t = 4 * tg + w4;
    const int k2 = 57 * kq + l;
    const bool ok1 = l < 57 && k2 < DF_;
    unsigned short vr[8], vi[8];
    if (ok1) {
      const int kbase = (t >> 5) + 8 * ((t >> 2) & 7) + 128 * (t & 3);
#pragma unroll
      for (int e = 0; e < 8; ++e) {
        const int k1 = kbase + 64 * (e >> 2) + 512 * (e & 3);
        vr[e] = wr[k1 * DF_ + k2]; vi[e] = wi[k1 * DF_ + k2];
      }
    }
    if (ok1) {
      u32x4* dst = (u32x4*)(ws + OFF_WP + ((size_t)k2 * 256 + t) * 32);
      dst[0] = u32x4{vr[0] | ((unsigned)vi[0] << 16), vr[1] | ((unsigned)vi[1] << 16), vr[2] | ((unsigned)vi[2] << 16), vr[3] | ((unsigned)vi[3] << 16)};
      dst[1] = u32x4{vr[4] | ((unsigned)vi[4] << 16), vr[5] | ((unsigned)vi[5] << 16), vr[6] | ((unsigned)vi[6] << 16), vr[7] | ((unsigned)vi[7] << 16)};
    }
    if (DBG_WPF && w4 < 3) {
      const char* src = (const char*)(w4 == 0 ? wg : w4 == 1 ? wu : wd) + (size_t)b * 6272;
      unsigned d0, d1;
      asm volatile("global_load_dword %0, %1, %2" : "=v"(d0) : "v"((unsigned)l * 64u), "s"(src) : "memory");
      asm volatile("global_load_dword %0, %1, %2" : "=v"(d1) : "v"(l < 34 ? (unsigned)l * 64u + 4096u : 0u), "s"(src) : "memory");
      asm volatile("s_waitcnt vmcnt(0)" :: "v"(d0), "v"(d1) : "memory");
    }
    STAMP(ws, 8192 + BIDX * 8 + (TIDX >> 6), 4);
    return;
  }
  u32x4 xa = {0, 0, 0, 0}, xb = {0, 0, 0, 0};
  if (l < 56) {
    xa = *(const u32x4*)(x + (size_t)rowa * D_ + 8 * l);
    xb = *(const u32x4*)(x + (size_t)(rowa + 1) * D_ + 8 * l);
  }
  cf t1[6], t2[7];
  load_tw448(t1, t2, l);
  STAMP(ws, 8192 + BIDX * 8 + (TIDX >> 6), 1);
  float fa[8], fb[8];
#pragma unroll
  for (int j = 0; j < 4; ++j) {
    fa[2 * j] = bf_lo(xa[j]); fa[2 * j + 1] = bf_hi(xa[j]);
    fb[2 * j] = bf_lo(xb[j]); fb[2 * j + 1] = bf_hi(xb[j]);
  }
  float sa = 0.f, sb = 0.f;
#pragma unroll
  for (int j = 0; j < 8; ++j) { sa += fa[j]; sb += fb[j]; }
  const float ma = wave_sum(sa) * (1.0f / D_), mb = wave_sum(sb) * (1.0f / D_);
  float va = 0.f, vb = 0.f;
  if (l < 56) {
#pragma unroll
    for (int j = 0; j < 8; ++j) { float da = fa[j] - ma, db = fb[j] - mb; va += da * da; vb += db * db; }
  }
  va = wave_sum(va) * (1.0f / D_); vb = wave_sum(vb) * (1.0f / D_);
  const float mab = rbf(ma), mbb = rbf(mb);
  const float da_ = rbf(__builtin_sqrtf(rbf(rbf(va) + 1e-5f))), db_ = rbf(__builtin_sqrtf(rbf(rbf(vb) + 1e-5f)));
  const float ra_ = 1.0f / da_, rb_ = 1.0f / db_;
  u32x4 na, nb;
#pragma unroll
  for (int j = 0; j < 4; ++j) {
    unsigned pa = pack_bf16(fa[2 * j] - mab, fa[2 * j + 1] - mab);
    unsigned pb = pack_bf16(fb[2 * j] - mbb, fb[2 * j + 1] - mbb);
    na[j] = pack_bf16(fdiv(bf_lo(pa), da_, ra_), fdiv(bf_hi(pa), da_, ra_));
    nb[j] = pack_bf16(fdiv(bf_lo(pb), db_, rb_), fdiv(bf_hi(pb), db_, rb_));
  }
  if (l < 56) {
    const int ks = l >> 2, g = l & 3;
    const int rb16 = rowa >> 4, r = rowa & 15;
    char* dst = ws + OFF_XNF + ((size_t)(rb16 * 14 + ks) * 64 + g * 16 + r) * 16;
    st16(dst, na);
    st16(dst + 16, nb);
    u32x4 p0, p1;
#pragma unroll
    for (int j = 0; j < 2; ++j) {
      p0[2 * j] = (na[j] & 0xffffu) | (nb[j] << 16);
      p0[2 * j + 1] = (na[j] >> 16) | (nb[j] & 0xffff0000u);
      p1[2 * j] = (na[j + 2] & 0xffffu) | (nb[j + 2] << 16);
      p1[2 * j + 1] = (na[j + 2] >> 16) | (nb[j + 2] & 0xffff0000u);
    }
    *(u32x4*)(buf + l * 32) = p0;
    *(u32x4*)(buf + l * 32 + 16) = p1;
  }
  WAVE_FENCE();
  cf z[7], o[8];
#pragma unroll
  for (int j = 0; j < 7; ++j) {
    unsigned u = *(const unsigned*)(buf + (l + 64 * j) * 4);
    z[j] = mkc(bf_lo(u), bf_hi(u));
  }
  WAVE_FENCE();
  STAMP(ws, 8192 + BIDX * 8 + (TIDX >> 6), 2);
  fft448<false>(z, o, t1, t2, buf, l);
  STAMP(ws, 8192 + BIDX * 8 + (TIDX >> 6), 3);
  cf* zb = (cf*)buf;
  if (l < 56) {
    const int q = l >> 3, bb = l & 7;
#pragma unroll
    for (int a = 0; a < 8; ++a) zb[7 * (bb + 8 * a) + q] = o[a];
  }
  WAVE_FENCE();
  float* s1 = (float*)(ws + OFF_S1) + ((size_t)rb8 * DF_ * 8 + 2 * w) * 2;
#pragma unroll
  for (int i = 0; i < 4; ++i) {
    const int k = l + 64 * i;
    if (k <= 224) {
      cf zk = zb[k], zn = zb[k == 0 ? 0 : 448 - k];
      f32x4 v = {zk.x + zn.x, zk.y - zn.y, zk.y + zn.y, zn.x - zk.x};
      st16(s1 + (size_t)k * 16, __builtin_bit_cast(u32x4, v));
    }
  }
  STAMP(ws, 8192 + BIDX * 8 + (TIDX >> 6), 4);
}

constexpr int K2_WLDS = 448 * 256;
constexpr int K2_FBUF = 2304 * 8;
constexpr int K2_LDS = K2_WLDS + 2 * K2_FBUF + 128;
typedef __attribute__((address_space(3))) unsigned lds_u32;
DI void sub_sync(unsigned cnt_addr, unsigned target, bool arrive) {
  unsigned vt, st; unsigned long long ex;
  const unsigned one = 1u;
  if (arrive) {
    asm volatile("s_waitcnt lgkmcnt(0)\n\t"
                 "s_mov_b64 %0, exec\n\t"
                 "s_mov_b64 exec, 1\n\t"
                 "ds_add_u32 %1, %2\n\t"
                 "s_mov_b64 exec, %0"
                 : "=&s"(ex) : "v"(cnt_addr), "v"(one) : "memory");
  }
  unsigned guard = 1u << 22;
  asm volatile("1:\n\t"
               "ds_read_b32 %0, %3\n\t"
               "s_waitcnt lgkmcnt(0)\n\t"
               "v_readfirstlane_b32 %1, %0\n\t"
               "s_nop 3\n\t"
               "s_cmp_ge_u32 %1, %4\n\t"
               "s_cbranch_scc1 2f\n\t"
               "s_sub_u32 %2, %2, 1\n\t"
               "s_cbranch_scc1 2f\n\t"
               "s_sleep 1\n\t"
               "s_branch 1b\n\t"
               "2:"
               : "=&v"(vt), "=&s"(st), "+s"(guard) : "v"(cnt_addr), "s"(target) : "memory", "scc");
}
DI void publish(unsigned lds_cnt, unsigned n, unsigned* gcnt) {
  unsigned vt, st; unsigned long long ex;
  const unsigned one = 1u;
  asm volatile("s_waitcnt vmcnt(0) lgkmcnt(0)\n\t"
               "s_mov_b64 %2, exec\n\t"
               "s_mov_b64 exec, 1\n\t"
               "ds_add_rtn_u32 %0, %3, %4\n\t"
               "s_waitcnt lgkmcnt(0)\n\t"
               "v_readfirstlane_b32 %1, %0\n\t"
               "s_mov_b64 exec, %2"
               : "=&v"(vt), "=&s"(st), "=&s"(ex) : "v"(lds_cnt), "v"(one) : "memory");
  if (st + 1 == n) {
    if (lane_id() == 0) __hip_atomic_fetch_add(gcnt, 1u, __ATOMIC_RELAXED, __HIP_MEMORY_SCOPE_AGENT);
  }
}
DI void poll_counters(unsigned* gc, int ns, unsigned want, unsigned extra0) {
  const int l = lane_id();
  unsigned* p = gc + (l < ns ? l : 0) * 32;
  const unsigned target = l < ns ? want + (l == 0 ? extra0 : 0u) : 0u;
  for (unsigned it = 0; it < (1u << 20); ++it) {
    const unsigned v = __hip_atomic_load(p, __ATOMIC_RELAXED, __HIP_MEMORY_SCOPE_AGENT);
    if (__builtin_amdgcn_ballot_w64(v < target) == 0ull) break;
    __builtin_amdgcn_s_sleep(8);
  }
  asm volatile("" ::: "memory");
}
DI int padi(int i) { return i + 4 * (i >> 5); }

DI float gelu_tanh(float g) {
  float z = g * (0.7978845608028654f + 0.0356774081363001f * g * g);
  float e = __builtin_amdgcn_exp2f(-2.8853900817779268f * z);
  return g * __builtin_amdgcn_rcpf(1.f + e);
}

typedef __attribute__((address_space(3))) s16x4* lds_s16x4_ptr;

#define TRREAD2(lo, hi, addr, off) \
  asm volatile("ds_read_b64_tr_b16 %0, %2 offset:%3\n\tds_read_b64_tr_b16 %1, %2 offset:%4" : "+v"(lo), "+v"(hi) : "v"(addr), "i"(off), "i"((off) + 512))

typedef float f32x16 __attribute__((ext_vector_type(16)));
#define K2_ROUND0S_ASM \
  "s_add_u32 s60, %[gblo], 0\n\t" \
  "s_addc_u32 s61, %[gbhi], 0\n\t" \
  "s_add_u32 m0, %[ldo], 0\n\t" \
  "s_nop 0\n\t" \
  "global_load_lds_dwordx4 %[lo0], s[60:61]\n\t" \
  "s_add_u32 s60, %[gblo], 28672\n\t" \
  "s_addc_u32 s61, %[gbhi], 0\n\t" \
  "s_add_u32 m0, %[ldo], 1024\n\t" \
  "s_nop 0\n\t" \
  "global_load_lds_dwordx4 %[lo1], s[60:61]\n\t" \
  "s_add_u32 s60, %[ublo], 0\n\t" \
  "s_addc_u32 s61, %[ubhi], 0\n\t" \
  "s_add_u32 m0, %[ldo], 8192\n\t" \
  "s_nop 0\n\t" \
  "global_load_lds_dwordx4 %[lo0], s[60:61]\n\t" \
  "s_add_u32 s60, %[ublo], 28672\n\t" \
  "s_addc_u32 s61, %[ubhi], 0\n\t" \
  "s_add_u32 m0, %[ldo], 9216\n\t" \
  "s_nop 0\n\t" \
  "global_load_lds_dwordx4 %[lo1], s[60:61]\n\t" \
  "s_add_u32 s62, %[xslo], 0\n\t" \
  "s_addc_u32 s63, %[xshi], 0\n\t" \
  "global_load_dwordx4 v[144:147], %[voff], s[62:63] offset:0\n\t" \
  "s_add_u32 s62, %[xslo], 12288\n\t" \
  "s_addc_u32 s63, %[xshi], 0\n\t" \
  "global_load_dwordx4 v[200:203], %[voff], s[62:63] offset:2048\n\t" \
  "s_add_u32 s62, %[xslo], 0\n\t" \
  "s_addc_u32 s63, %[xshi], 0\n\t" \
  "global_load_dwordx4 v[148:151], %[voff], s[62:63] offset:1024\n\t" \
  "s_add_u32 s62, %[xslo], 12288\n\t" \
  "s_addc_u32 s63, %[xshi], 0\n\t" \
  "global_load_dwordx4 v[204:207], %[voff], s[62:63] offset:3072\n\t" \
  "s_add_u32 s60, %[gblo], 229376\n\t" \
  "s_addc_u32 s61, %[gbhi], 0\n\t" \
  "s_add_u32 m0, %[ldo], 16384\n\t" \
  "s_nop 0\n\t" \
  "global_load_lds_dwordx4 %[lo0], s[60:61]\n\t" \
  "s_add_u32 s60, %[gblo], 258048\n\t" \
  "s_addc_u32 s61, %[gbhi], 0\n\t" \
  "s_add_u32 m0, %[ldo], 17408\n\t" \
  "s_nop 0\n\t" \
  "global_load_lds_dwordx4 %[lo1], s[60:61]\n\t" \
  "s_add_u32 s60, %[ublo], 229376\n\t" \
  "s_addc_u32 s61, %[ubhi], 0\n\t" \
  "s_add_u32 m0, %[ldo], 24576\n\t" \
  "s_nop 0\n\t" \
  "global_load_lds_dwordx4 %[lo0], s[60:61]\n\t" \
  "s_add_u32 s60, %[ublo], 258048\n\t" \
  "s_addc_u32 s61, %[ubhi], 0\n\t" \
  "s_add_u32 m0, %[ldo], 25600\n\t" \
  "s_nop 0\n\t" \
  "global_load_lds_dwordx4 %[lo1], s[60:61]\n\t" \
  "s_add_u32 s62, %[xslo], 0\n\t" \
  "s_addc_u32 s63, %[xshi], 0\n\t" \
  "global_load_dwordx4 v[152:155], %[voff], s[62:63] offset:2048\n\t" \
  "s_add_u32 s62, %[xslo], 16384\n\t" \
  "s_addc_u32 s63, %[xshi], 0\n\t" \
  "global_load_dwordx4 v[208:211], %[voff], s[62:63] offset:0\n\t" \
  "s_add_u32 s62, %[xslo], 0\n\t" \
  "s_addc_u32 s63, %[xshi], 0\n\t" \
  "global_load_dwordx4 v[156:159], %[voff], s[62:63] offset:3072\n\t" \
  "s_add_u32 s62, %[xslo], 16384\n\t" \
  "s_addc_u32 s63, %[xshi], 0\n\t" \
  "global_load_dwordx4 v[212:215], %[voff], s[62:63] offset:1024\n\t" \
  "s_add_u32 s60, %[gblo], 458752\n\t" \
  "s_addc_u32 s61, %[gbhi], 0\n\t" \
  "s_add_u32 m0, %[ldo], 32768\n\t" \
  "s_nop 0\n\t" \
  "global_load_lds_dwordx4 %[lo0], s[60:61]\n\t" \
  "s_add_u32 s60, %[gblo], 487424\n\t" \
  "s_addc_u32 s61, %[gbhi], 0\n\t" \
  "s_add_u32 m0, %[ldo], 33792\n\t" \
  "s_nop 0\n\t" \
  "global_load_lds_dwordx4 %[lo1], s[60:61]\n\t" \
  "s_add_u32 s60, %[ublo], 458752\n\t" \
  "s_addc_u32 s61, %[ubhi], 0\n\t" \
  "s_add_u32 m0, %[ldo], 40960\n\t" \
  "s_nop 0\n\t" \
  "global_load_lds_dwordx4 %[lo0], s[60:61]\n\t" \
  "s_add_u32 s60, %[ublo], 487424\n\t" \
  "s_addc_u32 s61, %[ubhi], 0\n\t" \
  "s_add_u32 m0, %[ldo], 41984\n\t" \
  "s_nop 0\n\t" \
  "global_load_lds_dwordx4 %[lo1], s[60:61]\n\t" \
  "s_add_u32 s62, %[xslo], 4096\n\t" \
  "s_addc_u32 s63, %[xshi], 0\n\t" \
  "global_load_dwordx4 v[160:163], %[voff], s[62:63] offset:0\n\t" \
  "s_add_u32 s62, %[xslo], 16384\n\t" \
  "s_addc_u32 s63, %[xshi], 0\n\t" \
  "global_load_dwordx4 v[216:219], %[voff], s[62:63] offset:2048\n\t" \
  "s_add_u32 s62, %[xslo], 4096\n\t" \
  "s_addc_u32 s63, %[xshi], 0\n\t" \
  "global_load_dwordx4 v[164:167], %[voff], s[62:63] offset:1024\n\t" \
  "s_add_u32 s62, %[xslo], 16384\n\t" \
  "s_addc_u32 s63, %[xshi], 0\n\t" \
  "global_load_dwordx4 v[220:223], %[voff], s[62:63] offset:3072\n\t" \
  "s_waitcnt vmcnt(16)\n\t" \
  "s_waitcnt lgkmcnt(0)\n\t" \
  "s_mov_b64 s[64:65], exec\n\t" \
  "s_mov_b64 exec, 1\n\t" \
  "ds_add_u32 %[cnt], %[one]\n\t" \
  "s_mov_b64 exec, s[64:65]\n\t" \
  "s_mov_b32 s67, 0x400000\n\t" \
  "1:\n\t" \
  "ds_read_b32 %[vt], %[cnt]\n\t" \
  "s_waitcnt lgkmcnt(0)\n\t" \
  "v_readfirstlane_b32 s66, %[vt]\n\t" \
  "s_nop 3\n\t" \
  "s_cmp_ge_u32 s66, 4\n\t" \
  "s_cbranch_scc1 2f\n\t" \
  "s_sub_u32 s67, s67, 1\n\t" \
  "s_cbranch_scc1 2f\n\t" \
  "s_sleep 1\n\t" \
  "s_branch 1b\n\t" \
  "2:\n\t" \
  "ds_read_b64_tr_b16 v[96:97], %[ad0] offset:0\n\t" \
  "ds_read_b64_tr_b16 v[98:99], %[ad0] offset:512\n\t" \
  "ds_read_b64_tr_b16 v[100:101], %[ad1] offset:0\n\t" \
  "ds_read_b64_tr_b16 v[102:103], %[ad1] offset:512\n\t" \
  "ds_read_b64_tr_b16 v[104:105], %[ad2] offset:0\n\t" \
  "ds_read_b64_tr_b16 v[106:107], %[ad2] offset:512\n\t" \
  "ds_read_b64_tr_b16 v[108:109], %[ad3] offset:0\n\t" \
  "ds_read_b64_tr_b16 v[110:111], %[ad3] offset:512\n\t" \
  "s_waitcnt lgkmcnt(4)\n\t" \
  "ds_read_b64_tr_b16 v[112:113], %[ad0] offset:8192\n\t" \
  "ds_read_b64_tr_b16 v[114:115], %[ad0] offset:8704\n\t" \
  "ds_read_b64_tr_b16 v[116:117], %[ad1] offset:8192\n\t" \
  "ds_read_b64_tr_b16 v[118:119], %[ad1] offset:8704\n\t" \
  "ds_read_b64_tr_b16 v[120:121], %[ad2] offset:8192\n\t" \
  "ds_read_b64_tr_b16 v[122:123], %[ad2] offset:8704\n\t" \
  "ds_read_b64_tr_b16 v[124:125], %[ad3] offset:8192\n\t" \
  "ds_read_b64_tr_b16 v[126:127], %[ad3] offset:8704\n\t" \
  "s_waitcnt lgkmcnt(6)\n\t" \
  "v_mfma_f32_16x16x32_bf16 v[32:35], v[96:99], v[144:147], 0\n\t" \
  "ds_read_b64_tr_b16 v[128:129], %[ad0] offset:4096\n\t" \
  "ds_read_b64_tr_b16 v[130:131], %[ad0] offset:4608\n\t" \
  "s_add_u32 s60, %[gblo], 688128\n\t" \
  "s_addc_u32 s61, %[gbhi], 0\n\t" \
  "s_add_u32 m0, %[ldo], 49152\n\t" \
  "s_nop 0\n\t" \
  "global_load_lds_dwordx4 %[lo0], s[60:61]\n\t" \
  "v_mfma_f32_16x16x32_bf16 v[64:67], v[96:99], v[200:203], 0\n\t" \
  "s_add_u32 s60, %[gblo], 716800\n\t" \
  "s_addc_u32 s61, %[gbhi], 0\n\t" \
  "s_add_u32 m0, %[ldo], 50176\n\t" \
  "s_nop 0\n\t" \
  "global_load_lds_dwordx4 %[lo1], s[60:61]\n\t" \
  "v_mfma_f32_16x16x32_bf16 v[36:39], v[100:103], v[144:147], 0\n\t" \
  "ds_read_b64_tr_b16 v[132:133], %[ad1] offset:4096\n\t" \
  "ds_read_b64_tr_b16 v[134:135], %[ad1] offset:4608\n\t" \
  "s_add_u32 s60, %[ublo], 688128\n\t" \
  "s_addc_u32 s61, %[ubhi], 0\n\t" \
  "s_add_u32 m0, %[ldo], 57344\n\t" \
  "s_nop 0\n\t" \
  "global_load_lds_dwordx4 %[lo0], s[60:61]\n\t" \
  "v_mfma_f32_16x16x32_bf16 v[68:71], v[100:103], v[200:203], 0\n\t" \
  "s_add_u32 s60, %[ublo], 716800\n\t" \
  "s_addc_u32 s61, %[ubhi], 0\n\t" \
  "s_add_u32 m0, %[ldo], 58368\n\t" \
  "s_nop 0\n\t" \
  "global_load_lds_dwordx4 %[lo1], s[60:61]\n\t" \
  "v_mfma_f32_16x16x32_bf16 v[40:43], v[104:107], v[144:147], 0\n\t" \
  "ds_read_b64_tr_b16 v[136:137], %[ad2] offset:4096\n\t" \
  "ds_read_b64_tr_b16 v[138:139], %[ad2] offset:4608\n\t" \
  "s_add_u32 s62, %[xslo], 4096\n\t" \
  "s_addc_u32 s63, %[xshi], 0\n\t" \
  "global_load_dwordx4 v[168:171], %[voff], s[62:63] offset:2048\n\t" \
  "v_mfma_f32_16x16x32_bf16 v[72:75], v[104:107], v[200:203], 0\n\t" \
  "s_add_u32 s62, %[xslo], 20480\n\t" \
  "s_addc_u32 s63, %[xshi], 0\n\t" \
  "global_load_dwordx4 v[224:227], %[voff], s[62:63] offset:0\n\t" \
  "v_mfma_f32_16x16x32_bf16 v[44:47], v[108:111], v[144:147], 0\n\t" \
  "ds_read_b64_tr_b16 v[140:141], %[ad3] offset:4096\n\t" \
  "ds_read_b64_tr_b16 v[142:143], %[ad3] offset:4608\n\t" \
  "s_add_u32 s62, %[xslo], 4096\n\t" \
  "s_addc_u32 s63, %[xshi], 0\n\t" \
  "global_load_dwordx4 v[172:175], %[voff], s[62:63] offset:3072\n\t" \
  "v_mfma_f32_16x16x32_bf16 v[76:79], v[108:111], v[200:203], 0\n\t" \
  "s_add_u32 s62, %[xslo], 20480\n\t" \
  "s_addc_u32 s63, %[xshi], 0\n\t" \
  "global_load_dwordx4 v[228:231], %[voff], s[62:63] offset:1024\n\t" \
  "s_waitcnt lgkmcnt(6)\n\t" \
  "v_mfma_f32_16x16x32_bf16 v[48:51], v[112:115], v[144:147], 0\n\t" \
  "ds_read_b64_tr_b16 v[96:97], %[ad0] offset:12288\n\t" \
  "ds_read_b64_tr_b16 v[98:99], %[ad0] offset:12800\n\t" \
  "v_mfma_f32_16x16x32_bf16 v[80:83], v[112:115], v[200:203], 0\n\t" \
  "v_mfma_f32_16x16x32_bf16 v[52:55], v[116:119], v[144:147], 0\n\t" \
  "ds_read_b64_tr_b16 v[100:101], %[ad1] offset:12288\n\t" \
  "ds_read_b64_tr_b16 v[102:103], %[ad1] offset:12800\n\t" \
  "v_mfma_f32_16x16x32_bf16 v[84:87], v[116:119], v[200:203], 0\n\t" \
  "v_mfma_f32_16x16x32_bf16 v[56:59], v[120:123], v[144:147], 0\n\t" \
  "ds_read_b64_tr_b16 v[104:105], %[ad2] offset:12288\n\t" \
  "ds_read_b64_tr_b16 v[106:107], %[ad2] offset:12800\n\t" \
  "v_mfma_f32_16x16x32_bf16 v[88:91], v[120:123], v[200:203], 0\n\t" \
  "v_mfma_f32_16x16x32_bf16 v[60:63], v[124:127], v[144:147], 0\n\t" \
  "ds_read_b64_tr_b16 v[108:109], %[ad3] offset:12288\n\t" \
  "ds_read_b64_tr_b16 v[110:111], %[ad3] offset:12800\n\t" \
  "v_mfma_f32_16x16x32_bf16 v[92:95], v[124:127], v[200:203], 0\n\t" \
  "s_waitcnt vmcnt(16)\n\t" \
  "s_waitcnt lgkmcnt(0)\n\t" \
  "s_mov_b64 s[64:65], exec\n\t" \
  "s_mov_b64 exec, 1\n\t" \
  "ds_add_u32 %[cnt], %[one]\n\t" \
  "s_mov_b64 exec, s[64:65]\n\t" \
  "s_mov_b32 s67, 0x400000\n\t" \
  "1:\n\t" \
  "ds_read_b32 %[vt], %[cnt]\n\t" \
  "s_waitcnt lgkmcnt(0)\n\t" \
  "v_readfirstlane_b32 s66, %[vt]\n\t" \
  "s_nop 3\n\t" \
  "s_cmp_ge_u32 s66, 8\n\t" \
  "s_cbranch_scc1 2f\n\t" \
  "s_sub_u32 s67, s67, 1\n\t" \
  "s_cbranch_scc1 2f\n\t" \
  "s_sleep 1\n\t" \
  "s_branch 1b\n\t" \
  "2:\n\t" \
  "s_waitcnt lgkmcnt(6)\n\t" \
  "v_mfma_f32_16x16x32_bf16 v[32:35], v[128:131], v[148:151], v[32:35]\n\t" \
  "ds_read_b64_tr_b16 v[112:113], %[ad0] offset:16384\n\t" \
  "ds_read_b64_tr_b16 v[114:115], %[ad0] offset:16896\n\t" \
  "v_mfma_f32_16x16x32_bf16 v[64:67], v[128:131], v[204:207], v[64:67]\n\t" \
  "v_mfma_f32_16x16x32_bf16 v[36:39], v[132:135], v[148:151], v[36:39]\n\t" \
  "ds_read_b64_tr_b16 v[116:117], %[ad1] offset:16384\n\t" \
  "ds_read_b64_tr_b16 v[118:119], %[ad1] offset:16896\n\t" \
  "v_mfma_f32_16x16x32_bf16 v[68:71], v[132:135], v[204:207], v[68:71]\n\t" \
  "v_mfma_f32_16x16x32_bf16 v[40:43], v[136:139], v[148:151], v[40:43]\n\t" \
  "ds_read_b64_tr_b16 v[120:121], %[ad2] offset:16384\n\t" \
  "ds_read_b64_tr_b16 v[122:123], %[ad2] offset:16896\n\t" \
  "v_mfma_f32_16x16x32_bf16 v[72:75], v[136:139], v[204:207], v[72:75]\n\t" \
  "v_mfma_f32_16x16x32_bf16 v[44:47], v[140:143], v[148:151], v[44:47]\n\t" \
  "ds_read_b64_tr_b16 v[124:125], %[ad3] offset:16384\n\t" \
  "ds_read_b64_tr_b16 v[126:127], %[ad3] offset:16896\n\t" \
  "v_mfma_f32_16x16x32_bf16 v[76:79], v[140:143], v[204:207], v[76:79]\n\t" \
  "s_waitcnt lgkmcnt(6)\n\t" \
  "v_mfma_f32_16x16x32_bf16 v[48:51], v[96:99], v[148:151], v[48:51]\n\t" \
  "ds_read_b64_tr_b16 v[128:129], %[ad0] offset:24576\n\t" \
  "ds_read_b64_tr_b16 v[130:131], %[ad0] offset:25088\n\t" \
  "v_mfma_f32_16x16x32_bf16 v[80:83], v[96:99], v[204:207], v[80:83]\n\t" \
  "v_mfma_f32_16x16x32_bf16 v[52:55], v[100:103], v[148:151], v[52:55]\n\t" \
  "ds_read_b64_tr_b16 v[132:133], %[ad1] offset:24576\n\t" \
  "ds_read_b64_tr_b16 v[134:135], %[ad1] offset:25088\n\t" \
  "v_mfma_f32_16x16x32_bf16 v[84:87], v[100:103], v[204:207], v[84:87]\n\t" \
  "v_mfma_f32_16x16x32_bf16 v[56:59], v[104:107], v[148:151], v[56:59]\n\t" \
  "ds_read_b64_tr_b16 v[136:137], %[ad2] offset:24576\n\t" \
  "ds_read_b64_tr_b16 v[138:139], %[ad2] offset:25088\n\t" \
  "v_mfma_f32_16x16x32_bf16 v[88:91], v[104:107], v[204:207], v[88:91]\n\t" \
  "v_mfma_f32_16x16x32_bf16 v[60:63], v[108:111], v[148:151], v[60:63]\n\t" \
  "ds_read_b64_tr_b16 v[140:141], %[ad3] offset:24576\n\t" \
  "ds_read_b64_tr_b16 v[142:143], %[ad3] offset:25088\n\t" \
  "v_mfma_f32_16x16x32_bf16 v[92:95], v[108:111], v[204:207], v[92:95]\n\t" \
  "s_waitcnt lgkmcnt(6)\n\t" \
  "v_mfma_f32_16x16x32_bf16 v[32:35], v[112:115], v[152:155], v[32:35]\n\t" \
  "ds_read_b64_tr_b16 v[96:97], %[ad0] offset:20480\n\t" \
  "ds_read_b64_tr_b16 v[98:99], %[ad0] offset:20992\n\t" \
  "s_add_u32 s60, %[gblo], 917504\n\t" \
  "s_addc_u32 s61, %[gbhi], 0\n\t" \
  "s_add_u32 m0, %[ldo], 65536\n\t" \
  "s_nop 0\n\t" \
  "global_load_lds_dwordx4 %[lo0], s[60:61]\n\t" \
  "v_mfma_f32_16x16x32_bf16 v[64:67], v[112:115], v[208:211], v[64:67]\n\t" \
  "s_add_u32 s60, %[gblo], 946176\n\t" \
  "s_addc_u32 s61, %[gbhi], 0\n\t" \
  "s_add_u32 m0, %[ldo], 66560\n\t" \
  "s_nop 0\n\t" \
  "global_load_lds_dwordx4 %[lo1], s[60:61]\n\t" \
  "v_mfma_f32_16x16x32_bf16 v[36:39], v[116:119], v[152:155], v[36:39]\n\t" \
  "ds_read_b64_tr_b16 v[100:101], %[ad1] offset:20480\n\t" \
  "ds_read_b64_tr_b16 v[102:103], %[ad1] offset:20992\n\t" \
  "s_add_u32 s60, %[ublo], 917504\n\t" \
  "s_addc_u32 s61, %[ubhi], 0\n\t" \
  "s_add_u32 m0, %[ldo], 73728\n\t" \
  "s_nop 0\n\t" \
  "global_load_lds_dwordx4 %[lo0], s[60:61]\n\t" \
  "v_mfma_f32_16x16x32_bf16 v[68:71], v[116:119], v[208:211], v[68:71]\n\t" \
  "s_add_u32 s60, %[ublo], 946176\n\t" \
  "s_addc_u32 s61, %[ubhi], 0\n\t" \
  "s_add_u32 m0, %[ldo], 74752\n\t" \
  "s_nop 0\n\t" \
  "global_load_lds_dwordx4 %[lo1], s[60:61]\n\t" \
  "v_mfma_f32_16x16x32_bf16 v[40:43], v[120:123], v[152:155], v[40:43]\n\t" \
  "ds_read_b64_tr_b16 v[104:105], %[ad2] offset:20480\n\t" \
  "ds_read_b64_tr_b16 v[106:107], %[ad2] offset:20992\n\t" \
  "s_add_u32 s62, %[xslo], 8192\n\t" \
  "s_addc_u32 s63, %[xshi], 0\n\t" \
  "global_load_dwordx4 v[176:179], %[voff], s[62:63] offset:0\n\t" \
  "v_mfma_f32_16x16x32_bf16 v[72:75], v[120:123], v[208:211], v[72:75]\n\t" \
  "s_add_u32 s62, %[xslo], 20480\n\t" \
  "s_addc_u32 s63, %[xshi], 0\n\t" \
  "global_load_dwordx4 v[232:235], %[voff], s[62:63] offset:2048\n\t" \
  "v_mfma_f32_16x16x32_bf16 v[44:47], v[124:127], v[152:155], v[44:47]\n\t" \
  "ds_read_b64_tr_b16 v[108:109], %[ad3] offset:20480\n\t" \
  "ds_read_b64_tr_b16 v[110:111], %[ad3] offset:20992\n\t" \
  "s_add_u32 s62, %[xslo], 8192\n\t" \
  "s_addc_u32 s63, %[xshi], 0\n\t" \
  "global_load_dwordx4 v[180:183], %[voff], s[62:63] offset:1024\n\t" \
  "v_mfma_f32_16x16x32_bf16 v[76:79], v[124:127], v[208:211], v[76:79]\n\t" \
  "s_add_u32 s62, %[xslo], 20480\n\t" \
  "s_addc_u32 s63, %[xshi], 0\n\t" \
  "global_load_dwordx4 v[236:239], %[voff], s[62:63] offset:3072\n\t" \
  "s_waitcnt lgkmcnt(6)\n\t" \
  "v_mfma_f32_16x16x32_bf16 v[48:51], v[128:131], v[152:155], v[48:51]\n\t" \
  "ds_read_b64_tr_b16 v[112:113], %[ad0] offset:28672\n\t" \
  "ds_read_b64_tr_b16 v[114:115], %[ad0] offset:29184\n\t" \
  "s_add_u32 s62, %[xslo], 114688\n\t" \
  "s_addc_u32 s63, %[xshi], 0\n\t" \
  "global_load_dwordx4 v[144:147], %[voff], s[62:63] offset:0\n\t" \
  "v_mfma_f32_16x16x32_bf16 v[80:83], v[128:131], v[208:211], v[80:83]\n\t" \
  "s_add_u32 s62, %[xslo], 126976\n\t" \
  "s_addc_u32 s63, %[xshi], 0\n\t" \
  "global_load_dwordx4 v[200:203], %[voff], s[62:63] offset:2048\n\t" \
  "v_mfma_f32_16x16x32_bf16 v[52:55], v[132:135], v[152:155], v[52:55]\n\t" \
  "ds_read_b64_tr_b16 v[116:117], %[ad1] offset:28672\n\t" \
  "ds_read_b64_tr_b16 v[118:119], %[ad1] offset:29184\n\t" \
  "s_add_u32 s62, %[xslo], 114688\n\t" \
  "s_addc_u32 s63, %[xshi], 0\n\t" \
  "global_load_dwordx4 v[148:151], %[voff], s[62:63] offset:1024\n\t" \
  "v_mfma_f32_16x16x32_bf16 v[84:87], v[132:135], v[208:211], v[84:87]\n\t" \
  "s_add_u32 s62, %[xslo], 126976\n\t" \
  "s_addc_u32 s63, %[xshi], 0\n\t" \
  "global_load_dwordx4 v[204:207], %[voff], s[62:63] offset:3072\n\t" \
  "v_mfma_f32_16x16x32_bf16 v[56:59], v[136:139], v[152:155], v[56:59]\n\t" \
  "ds_read_b64_tr_b16 v[120:121], %[ad2] offset:28672\n\t" \
  "ds_read_b64_tr_b16 v[122:123], %[ad2] offset:29184\n\t" \
  "v_mfma_f32_16x16x32_bf16 v[88:91], v[136:139], v[208:211], v[88:91]\n\t" \
  "v_mfma_f32_16x16x32_bf16 v[60:63], v[140:143], v[152:155], v[60:63]\n\t" \
  "ds_read_b64_tr_b16 v[124:125], %[ad3] offset:28672\n\t" \
  "ds_read_b64_tr_b16 v[126:127], %[ad3] offset:29184\n\t" \
  "v_mfma_f32_16x16x32_bf16 v[92:95], v[140:143], v[208:211], v[92:95]\n\t" \
  "s_waitcnt vmcnt(20)\n\t" \
  "s_waitcnt lgkmcnt(0)\n\t" \
  "s_mov_b64 s[64:65], exec\n\t" \
  "s_mov_b64 exec, 1\n\t" \
  "ds_add_u32 %[cnt], %[one]\n\t" \
  "s_mov_b64 exec, s[64:65]\n\t" \
  "s_mov_b32 s67, 0x400000\n\t" \
  "1:\n\t" \
  "ds_read_b32 %[vt], %[cnt]\n\t" \
  "s_waitcnt lgkmcnt(0)\n\t" \
  "v_readfirstlane_b32 s66, %[vt]\n\t" \
  "s_nop 3\n\t" \
  "s_cmp_ge_u32 s66, 12\n\t" \
  "s_cbranch_scc1 2f\n\t" \
  "s_sub_u32 s67, s67, 1\n\t" \
  "s_cbranch_scc1 2f\n\t" \
  "s_sleep 1\n\t" \
  "s_branch 1b\n\t" \
  "2:\n\t" \
  "s_waitcnt lgkmcnt(6)\n\t" \
  "v_mfma_f32_16x16x32_bf16 v[32:35], v[96:99], v[156:159], v[32:35]\n\t" \
  "ds_read_b64_tr_b16 v[128:129], %[ad0] offset:32768\n\t" \
  "ds_read_b64_tr_b16 v[130:131], %[ad0] offset:33280\n\t" \
  "v_mfma_f32_16x16x32_bf16 v[64:67], v[96:99], v[212:215], v[64:67]\n\t" \
  "v_mfma_f32_16x16x32_bf16 v[36:39], v[100:103], v[156:159], v[36:39]\n\t" \
  "ds_read_b64_tr_b16 v[132:133], %[ad1] offset:32768\n\t" \
  "ds_read_b64_tr_b16 v[134:135], %[ad1] offset:33280\n\t" \
  "v_mfma_f32_16x16x32_bf16 v[68:71], v[100:103], v[212:215], v[68:71]\n\t" \
  "v_mfma_f32_16x16x32_bf16 v[40:43], v[104:107], v[156:159], v[40:43]\n\t" \
  "ds_read_b64_tr_b16 v[136:137], %[ad2] offset:32768\n\t" \
  "ds_read_b64_tr_b16 v[138:139], %[ad2] offset:33280\n\t" \
  "v_mfma_f32_16x16x32_bf16 v[72:75], v[104:107], v[212:215], v[72:75]\n\t" \
  "v_mfma_f32_16x16x32_bf16 v[44:47], v[108:111], v[156:159], v[44:47]\n\t" \
  "ds_read_b64_tr_b16 v[140:141], %[ad3] offset:32768\n\t" \
  "ds_read_b64_tr_b16 v[142:143], %[ad3] offset:33280\n\t" \
  "v_mfma_f32_16x16x32_bf16 v[76:79], v[108:111], v[212:215], v[76:79]\n\t" \
  "s_waitcnt lgkmcnt(6)\n\t" \
  "v_mfma_f32_16x16x32_bf16 v[48:51], v[112:115], v[156:159], v[48:51]\n\t" \
  "ds_read_b64_tr_b16 v[96:97], %[ad0] offset:40960\n\t" \
  "ds_read_b64_tr_b16 v[98:99], %[ad0] offset:41472\n\t" \
  "v_mfma_f32_16x16x32_bf16 v[80:83], v[112:115], v[212:215], v[80:83]\n\t" \
  "v_mfma_f32_16x16x32_bf16 v[52:55], v[116:119], v[156:159], v[52:55]\n\t" \
  "ds_read_b64_tr_b16 v[100:101], %[ad1] offset:40960\n\t" \
  "ds_read_b64_tr_b16 v[102:103], %[ad1] offset:41472\n\t" \
  "v_mfma_f32_16x16x32_bf16 v[84:87], v[116:119], v[212:215], v[84:87]\n\t" \
  "v_mfma_f32_16x16x32_bf16 v[56:59], v[120:123], v[156:159], v[56:59]\n\t" \
  "ds_read_b64_tr_b16 v[104:105], %[ad2] offset:40960\n\t" \
  "ds_read_b64_tr_b16 v[106:107], %[ad2] offset:41472\n\t" \
  "v_mfma_f32_16x16x32_bf16 v[88:91], v[120:123], v[212:215], v[88:91]\n\t" \
  "v_mfma_f32_16x16x32_bf16 v[60:63], v[124:127], v[156:159], v[60:63]\n\t" \
  "ds_read_b64_tr_b16 v[108:109], %[ad3] offset:40960\n\t" \
  "ds_read_b64_tr_b16 v[110:111], %[ad3] offset:41472\n\t" \
  "v_mfma_f32_16x16x32_bf16 v[92:95], v[124:127], v[212:215], v[92:95]\n\t" \
  "s_waitcnt lgkmcnt(6)\n\t" \
  "v_mfma_f32_16x16x32_bf16 v[32:35], v[128:131], v[160:163], v[32:35]\n\t" \
  "ds_read_b64_tr_b16 v[112:113], %[ad0] offset:36864\n\t" \
  "ds_read_b64_tr_b16 v[114:115], %[ad0] offset:37376\n\t" \
  "s_add_u32 s60, %[gblo], 1146880\n\t" \
  "s_addc_u32 s61, %[gbhi], 0\n\t" \
  "s_add_u32 m0, %[ldo], 81920\n\t" \
  "s_nop 0\n\t" \
  "global_load_lds_dwordx4 %[lo0], s[60:61]\n\t" \
  "v_mfma_f32_16x16x32_bf16 v[64:67], v[128:131], v[216:219], v[64:67]\n\t" \
  "s_add_u32 s60, %[gblo], 1175552\n\t" \
  "s_addc_u32 s61, %[gbhi], 0\n\t" \
  "s_add_u32 m0, %[ldo], 82944\n\t" \
  "s_nop 0\n\t" \
  "global_load_lds_dwordx4 %[lo1], s[60:61]\n\t" \
  "v_mfma_f32_16x16x32_bf16 v[36:39], v[132:135], v[160:163], v[36:39]\n\t" \
  "ds_read_b64_tr_b16 v[116:117], %[ad1] offset:36864\n\t" \
  "ds_read_b64_tr_b16 v[118:119], %[ad1] offset:37376\n\t" \
  "s_add_u32 s60, %[ublo], 1146880\n\t" \
  "s_addc_u32 s61, %[ubhi], 0\n\t" \
  "s_add_u32 m0, %[ldo], 90112\n\t" \
  "s_nop 0\n\t" \
  "global_load_lds_dwordx4 %[lo0], s[60:61]\n\t" \
  "v_mfma_f32_16x16x32_bf16 v[68:71], v[132:135], v[216:219], v[68:71]\n\t" \
  "s_add_u32 s60, %[ublo], 1175552\n\t" \
  "s_addc_u32 s61, %[ubhi], 0\n\t" \
  "s_add_u32 m0, %[ldo], 91136\n\t" \
  "s_nop 0\n\t" \
  "global_load_lds_dwordx4 %[lo1], s[60:61]\n\t" \
  "v_mfma_f32_16x16x32_bf16 v[40:43], v[136:139], v[160:163], v[40:43]\n\t" \
  "ds_read_b64_tr_b16 v[120:121], %[ad2] offset:36864\n\t" \
  "ds_read_b64_tr_b16 v[122:123], %[ad2] offset:37376\n\t" \
  "s_add_u32 s62, %[xslo], 8192\n\t" \
  "s_addc_u32 s63, %[xshi], 0\n\t" \
  "global_load_dwordx4 v[184:187], %[voff], s[62:63] offset:2048\n\t" \
  "v_mfma_f32_16x16x32_bf16 v[72:75], v[136:139], v[216:219], v[72:75]\n\t" \
  "s_add_u32 s62, %[xslo], 24576\n\t" \
  "s_addc_u32 s63, %[xshi], 0\n\t" \
  "global_load_dwordx4 v[240:243], %[voff], s[62:63] offset:0\n\t" \
  "v_mfma_f32_16x16x32_bf16 v[44:47], v[140:143], v[160:163], v[44:47]\n\t" \
  "ds_read_b64_tr_b16 v[124:125], %[ad3] offset:36864\n\t" \
  "ds_read_b64_tr_b16 v[126:127], %[ad3] offset:37376\n\t" \
  "s_add_u32 s62, %[xslo], 8192\n\t" \
  "s_addc_u32 s63, %[xshi], 0\n\t" \
  "global_load_dwordx4 v[188:191], %[voff], s[62:63] offset:3072\n\t" \
  "v_mfma_f32_16x16x32_bf16 v[76:79], v[140:143], v[216:219], v[76:79]\n\t" \
  "s_add_u32 s62, %[xslo], 24576\n\t" \
  "s_addc_u32 s63, %[xshi], 0\n\t" \
  "global_load_dwordx4 v[244:247], %[voff], s[62:63] offset:1024\n\t" \
  "s_waitcnt lgkmcnt(6)\n\t" \
  "v_mfma_f32_16x16x32_bf16 v[48:51], v[96:99], v[160:163], v[48:51]\n\t" \
  "ds_read_b64_tr_b16 v[128:129], %[ad0] offset:45056\n\t" \
  "ds_read_b64_tr_b16 v[130:131], %[ad0] offset:45568\n\t" \
  "s_add_u32 s62, %[xslo], 114688\n\t" \
  "s_addc_u32 s63, %[xshi], 0\n\t" \
  "global_load_dwordx4 v[152:155], %[voff], s[62:63] offset:2048\n\t" \
  "v_mfma_f32_16x16x32_bf16 v[80:83], v[96:99], v[216:219], v[80:83]\n\t" \
  "s_add_u32 s62, %[xslo], 131072\n\t" \
  "s_addc_u32 s63, %[xshi], 0\n\t" \
  "global_load_dwordx4 v[208:211], %[voff], s[62:63] offset:0\n\t" \
  "v_mfma_f32_16x16x32_bf16 v[52:55], v[100:103], v[160:163], v[52:55]\n\t" \
  "ds_read_b64_tr_b16 v[132:133], %[ad1] offset:45056\n\t" \
  "ds_read_b64_tr_b16 v[134:135], %[ad1] offset:45568\n\t" \
  "s_add_u32 s62, %[xslo], 114688\n\t" \
  "s_addc_u32 s63, %[xshi], 0\n\t" \
  "global_load_dwordx4 v[156:159], %[voff], s[62:63] offset:3072\n\t" \
  "v_mfma_f32_16x16x32_bf16 v[84:87], v[100:103], v[216:219], v[84:87]\n\t" \
  "s_add_u32 s62, %[xslo], 131072\n\t" \
  "s_addc_u32 s63, %[xshi], 0\n\t" \
  "global_load_dwordx4 v[212:215], %[voff], s[62:63] offset:1024\n\t" \
  "v_mfma_f32_16x16x32_bf16 v[56:59], v[104:107], v[160:163], v[56:59]\n\t" \
  "ds_read_b64_tr_b16 v[136:137], %[ad2] offset:45056\n\t" \
  "ds_read_b64_tr_b16 v[138:139], %[ad2] offset:45568\n\t" \
  "v_mfma_f32_16x16x32_bf16 v[88:91], v[104:107], v[216:219], v[88:91]\n\t" \
  "v_mfma_f32_16x16x32_bf16 v[60:63], v[108:111], v[160:163], v[60:63]\n\t" \
  "ds_read_b64_tr_b16 v[140:141], %[ad3] offset:45056\n\t" \
  "ds_read_b64_tr_b16 v[142:143], %[ad3] offset:45568\n\t" \
  "v_mfma_f32_16x16x32_bf16 v[92:95], v[108:111], v[216:219], v[92:95]\n\t" \
  "s_waitcnt vmcnt(24)\n\t" \
  "s_waitcnt lgkmcnt(0)\n\t" \
  "s_mov_b64 s[64:65], exec\n\t" \
  "s_mov_b64 exec, 1\n\t" \
  "ds_add_u32 %[cnt], %[one]\n\t" \
  "s_mov_b64 exec, s[64:65]\n\t" \
  "s_mov_b32 s67, 0x400000\n\t" \
  "1:\n\t" \
  "ds_read_b32 %[vt], %[cnt]\n\t" \
  "s_waitcnt lgkmcnt(0)\n\t" \
  "v_readfirstlane_b32 s66, %[vt]\n\t" \
  "s_nop 3\n\t" \
  "s_cmp_ge_u32 s66, 16\n\t" \
  "s_cbranch_scc1 2f\n\t" \
  "s_sub_u32 s67, s67, 1\n\t" \
  "s_cbranch_scc1 2f\n\t" \
  "s_sleep 1\n\t" \
  "s_branch 1b\n\t" \
  "2:\n\t" \
  "s_waitcnt lgkmcnt(6)\n\t" \
  "v_mfma_f32_16x16x32_bf16 v[32:35], v[112:115], v[164:167], v[32:35]\n\t" \
  "ds_read_b64_tr_b16 v[96:97], %[ad0] offset:49152\n\t" \
  "ds_read_b64_tr_b16 v[98:99], %[ad0] offset:49664\n\t" \
  "v_mfma_f32_16x16x32_bf16 v[64:67], v[112:115], v[220:223], v[64:67]\n\t" \
  "v_mfma_f32_16x16x32_bf16 v[36:39], v[116:119], v[164:167], v[36:39]\n\t" \
  "ds_read_b64_tr_b16 v[100:101], %[ad1] offset:49152\n\t" \
  "ds_read_b64_tr_b16 v[102:103], %[ad1] offset:49664\n\t" \
  "v_mfma_f32_16x16x32_bf16 v[68:71], v[116:119], v[220:223], v[68:71]\n\t" \
  "v_mfma_f32_16x16x32_bf16 v[40:43], v[120:123], v[164:167], v[40:43]\n\t" \
  "ds_read_b64_tr_b16 v[104:105], %[ad2] offset:49152\n\t" \
  "ds_read_b64_tr_b16 v[106:107], %[ad2] offset:49664\n\t" \
  "v_mfma_f32_16x16x32_bf16 v[72:75], v[120:123], v[220:223], v[72:75]\n\t" \
  "v_mfma_f32_16x16x32_bf16 v[44:47], v[124:127], v[164:167], v[44:47]\n\t" \
  "ds_read_b64_tr_b16 v[108:109], %[ad3] offset:49152\n\t" \
  "ds_read_b64_tr_b16 v[110:111], %[ad3] offset:49664\n\t" \
  "v_mfma_f32_16x16x32_bf16 v[76:79], v[124:127], v[220:223], v[76:79]\n\t" \
  "s_waitcnt lgkmcnt(6)\n\t" \
  "v_mfma_f32_16x16x32_bf16 v[48:51], v[128:131], v[164:167], v[48:51]\n\t" \
  "ds_read_b64_tr_b16 v[112:113], %[ad0] offset:57344\n\t" \
  "ds_read_b64_tr_b16 v[114:115], %[ad0] offset:57856\n\t" \
  "v_mfma_f32_16x16x32_bf16 v[80:83], v[128:131], v[220:223], v[80:83]\n\t" \
  "v_mfma_f32_16x16x32_bf16 v[52:55], v[132:135], v[164:167], v[52:55]\n\t" \
  "ds_read_b64_tr_b16 v[116:117], %[ad1] offset:57344\n\t" \
  "ds_read_b64_tr_b16 v[118:119], %[ad1] offset:57856\n\t" \
  "v_mfma_f32_16x16x32_bf16 v[84:87], v[132:135], v[220:223], v[84:87]\n\t" \
  "v_mfma_f32_16x16x32_bf16 v[56:59], v[136:139], v[164:167], v[56:59]\n\t" \
  "ds_read_b64_tr_b16 v[120:121], %[ad2] offset:57344\n\t" \
  "ds_read_b64_tr_b16 v[122:123], %[ad2] offset:57856\n\t" \
  "v_mfma_f32_16x16x32_bf16 v[88:91], v[136:139], v[220:223], v[88:91]\n\t" \
  "v_mfma_f32_16x16x32_bf16 v[60:63], v[140:143], v[164:167], v[60:63]\n\t" \
  "ds_read_b64_tr_b16 v[124:125], %[ad3] offset:57344\n\t" \
  "ds_read_b64_tr_b16 v[126:127], %[ad3] offset:57856\n\t" \
  "v_mfma_f32_16x16x32_bf16 v[92:95], v[140:143], v[220:223], v[92:95]\n\t" \
  "s_waitcnt lgkmcnt(6)\n\t" \
  "v_mfma_f32_16x16x32_bf16 v[32:35], v[96:99], v[168:171], v[32:35]\n\t" \
  "ds_read_b64_tr_b16 v[128:129], %[ad0] offset:53248\n\t" \
  "ds_read_b64_tr_b16 v[130:131], %[ad0] offset:53760\n\t" \
  "s_add_u32 s60, %[gblo], 1376256\n\t" \
  "s_addc_u32 s61, %[gbhi], 0\n\t" \
  "s_add_u32 m0, %[ldo], 98304\n\t" \
  "s_nop 0\n\t" \
  "global_load_lds_dwordx4 %[lo0], s[60:61]\n\t" \
  "v_mfma_f32_16x16x32_bf16 v[64:67], v[96:99], v[224:227], v[64:67]\n\t" \
  "s_add_u32 s60, %[gblo], 1404928\n\t" \
  "s_addc_u32 s61, %[gbhi], 0\n\t" \
  "s_add_u32 m0, %[ldo], 99328\n\t" \
  "s_nop 0\n\t" \
  "global_load_lds_dwordx4 %[lo1], s[60:61]\n\t" \
  "v_mfma_f32_16x16x32_bf16 v[36:39], v[100:103], v[168:171], v[36:39]\n\t" \
  "ds_read_b64_tr_b16 v[132:133], %[ad1] offset:53248\n\t" \
  "ds_read_b64_tr_b16 v[134:135], %[ad1] offset:53760\n\t" \
  "s_add_u32 s60, %[ublo], 1376256\n\t" \
  "s_addc_u32 s61, %[ubhi], 0\n\t" \
  "s_add_u32 m0, %[ldo], 106496\n\t" \
  "s_nop 0\n\t" \
  "global_load_lds_dwordx4 %[lo0], s[60:61]\n\t" \
  "v_mfma_f32_16x16x32_bf16 v[68:71], v[100:103], v[224:227], v[68:71]\n\t" \
  "s_add_u32 s60, %[ublo], 1404928\n\t" \
  "s_addc_u32 s61, %[ubhi], 0\n\t" \
  "s_add_u32 m0, %[ldo], 107520\n\t" \
  "s_nop 0\n\t" \
  "global_load_lds_dwordx4 %[lo1], s[60:61]\n\t" \
  "v_mfma_f32_16x16x32_bf16 v[40:43], v[104:107], v[168:171], v[40:43]\n\t" \
  "ds_read_b64_tr_b16 v[136:137], %[ad2] offset:53248\n\t" \
  "ds_read_b64_tr_b16 v[138:139], %[ad2] offset:53760\n\t" \
  "s_add_u32 s62, %[xslo], 12288\n\t" \
  "s_addc_u32 s63, %[xshi], 0\n\t" \
  "global_load_dwordx4 v[192:195], %[voff], s[62:63] offset:0\n\t" \
  "v_mfma_f32_16x16x32_bf16 v[72:75], v[104:107], v[224:227], v[72:75]\n\t" \
  "s_add_u32 s62, %[xslo], 24576\n\t" \
  "s_addc_u32 s63, %[xshi], 0\n\t" \
  "global_load_dwordx4 v[248:251], %[voff], s[62:63] offset:2048\n\t" \
  "v_mfma_f32_16x16x32_bf16 v[44:47], v[108:111], v[168:171], v[44:47]\n\t" \
  "ds_read_b64_tr_b16 v[140:141], %[ad3] offset:53248\n\t" \
  "ds_read_b64_tr_b16 v[142:143], %[ad3] offset:53760\n\t" \
  "s_add_u32 s62, %[xslo], 12288\n\t" \
  "s_addc_u32 s63, %[xshi], 0\n\t" \
  "global_load_dwordx4 v[196:199], %[voff], s[62:63] offset:1024\n\t" \
  "v_mfma_f32_16x16x32_bf16 v[76:79], v[108:111], v[224:227], v[76:79]\n\t" \
  "s_add_u32 s62, %[xslo], 24576\n\t" \
  "s_addc_u32 s63, %[xshi], 0\n\t" \
  "global_load_dwordx4 v[252:255], %[voff], s[62:63] offset:3072\n\t" \
  "s_waitcnt lgkmcnt(6)\n\t" \
  "v_mfma_f32_16x16x32_bf16 v[48:51], v[112:115], v[168:171], v[48:51]\n\t" \
  "ds_read_b64_tr_b16 v[96:97], %[ad0] offset:61440\n\t" \
  "ds_read_b64_tr_b16 v[98:99], %[ad0] offset:61952\n\t" \
  "s_add_u32 s62, %[xslo], 118784\n\t" \
  "s_addc_u32 s63, %[xshi], 0\n\t" \
  "global_load_dwordx4 v[160:163], %[voff], s[62:63] offset:0\n\t" \
  "v_mfma_f32_16x16x32_bf16 v[80:83], v[112:115], v[224:227], v[80:83]\n\t" \
  "s_add_u32 s62, %[xslo], 131072\n\t" \
  "s_addc_u32 s63, %[xshi], 0\n\t" \
  "global_load_dwordx4 v[216:219], %[voff], s[62:63] offset:2048\n\t" \
  "v_mfma_f32_16x16x32_bf16 v[52:55], v[116:119], v[168:171], v[52:55]\n\t" \
  "ds_read_b64_tr_b16 v[100:101], %[ad1] offset:61440\n\t" \
  "ds_read_b64_tr_b16 v[102:103], %[ad1] offset:61952\n\t" \
  "s_add_u32 s62, %[xslo], 118784\n\t" \
  "s_addc_u32 s63, %[xshi], 0\n\t" \
  "global_load_dwordx4 v[164:167], %[voff], s[62:63] offset:1024\n\t" \
  "v_mfma_f32_16x16x32_bf16 v[84:87], v[116:119], v[224:227], v[84:87]\n\t" \
  "s_add_u32 s62, %[xslo], 131072\n\t" \
  "s_addc_u32 s63, %[xshi], 0\n\t" \
  "global_load_dwordx4 v[220:223], %[voff], s[62:63] offset:3072\n\t" \
  "v_mfma_f32_16x16x32_bf16 v[56:59], v[120:123], v[168:171], v[56:59]\n\t" \
  "ds_read_b64_tr_b16 v[104:105], %[ad2] offset:61440\n\t" \
  "ds_read_b64_tr_b16 v[106:107], %[ad2] offset:61952\n\t" \
  "v_mfma_f32_16x16x32_bf16 v[88:91], v[120:123], v[224:227], v[88:91]\n\t" \
  "v_mfma_f32_16x16x32_bf16 v[60:63], v[124:127], v[168:171], v[60:63]\n\t" \
  "ds_read_b64_tr_b16 v[108:109], %[ad3] offset:61440\n\t" \
  "ds_read_b64_tr_b16 v[110:111], %[ad3] offset:61952\n\t" \
  "v_mfma_f32_16x16x32_bf16 v[92:95], v[124:127], v[224:227], v[92:95]\n\t" \
  "s_waitcnt vmcnt(28)\n\t" \
  "s_waitcnt lgkmcnt(0)\n\t" \
  "s_mov_b64 s[64:65], exec\n\t" \
  "s_mov_b64 exec, 1\n\t" \
  "ds_add_u32 %[cnt], %[one]\n\t" \
  "s_mov_b64 exec, s[64:65]\n\t" \
  "s_mov_b32 s67, 0x400000\n\t" \
  "1:\n\t" \
  "ds_read_b32 %[vt], %[cnt]\n\t" \
  "s_waitcnt lgkmcnt(0)\n\t" \
  "v_readfirstlane_b32 s66, %[vt]\n\t" \
  "s_nop 3\n\t" \
  "s_cmp_ge_u32 s66, 20\n\t" \
  "s_cbranch_scc1 2f\n\t" \
  "s_sub_u32 s67, s67, 1\n\t" \
  "s_cbranch_scc1 2f\n\t" \
  "s_sleep 1\n\t" \
  "s_branch 1b\n\t" \
  "2:\n\t" \
  "s_waitcnt lgkmcnt(6)\n\t" \
  "v_add_u32 %[ad0], 0x10000, %[ad0]\n\t" \
  "v_add_u32 %[ad1], 0x10000, %[ad1]\n\t" \
  "v_add_u32 %[ad2], 0x10000, %[ad2]\n\t" \
  "v_add_u32 %[ad3], 0x10000, %[ad3]\n\t" \
  "v_mfma_f32_16x16x32_bf16 v[32:35], v[128:131], v[172:175], v[32:35]\n\t" \
  "ds_read_b64_tr_b16 v[112:113], %[ad0] offset:0\n\t" \
  "ds_read_b64_tr_b16 v[114:115], %[ad0] offset:512\n\t" \
  "v_mfma_f32_16x16x32_bf16 v[64:67], v[128:131], v[228:231], v[64:67]\n\t" \
  "v_mfma_f32_16x16x32_bf16 v[36:39], v[132:135], v[172:175], v[36:39]\n\t" \
  "ds_read_b64_tr_b16 v[116:117], %[ad1] offset:0\n\t" \
  "ds_read_b64_tr_b16 v[118:119], %[ad1] offset:512\n\t" \
  "v_mfma_f32_16x16x32_bf16 v[68:71], v[132:135], v[228:231], v[68:71]\n\t" \
  "v_mfma_f32_16x16x32_bf16 v[40:43], v[136:139], v[172:175], v[40:43]\n\t" \
  "ds_read_b64_tr_b16 v[120:121], %[ad2] offset:0\n\t" \
  "ds_read_b64_tr_b16 v[122:123], %[ad2] offset:512\n\t" \
  "v_mfma_f32_16x16x32_bf16 v[72:75], v[136:139], v[228:231], v[72:75]\n\t" \
  "v_mfma_f32_16x16x32_bf16 v[44:47], v[140:143], v[172:175], v[44:47]\n\t" \
  "ds_read_b64_tr_b16 v[124:125], %[ad3] offset:0\n\t" \
  "ds_read_b64_tr_b16 v[126:127], %[ad3] offset:512\n\t" \
  "v_mfma_f32_16x16x32_bf16 v[76:79], v[140:143], v[228:231], v[76:79]\n\t" \
  "s_waitcnt lgkmcnt(6)\n\t" \
  "v_mfma_f32_16x16x32_bf16 v[48:51], v[96:99], v[172:175], v[48:51]\n\t" \
  "ds_read_b64_tr_b16 v[128:129], %[ad0] offset:8192\n\t" \
  "ds_read_b64_tr_b16 v[130:131], %[ad0] offset:8704\n\t" \
  "v_mfma_f32_16x16x32_bf16 v[80:83], v[96:99], v[228:231], v[80:83]\n\t" \
  "v_mfma_f32_16x16x32_bf16 v[52:55], v[100:103], v[172:175], v[52:55]\n\t" \
  "ds_read_b64_tr_b16 v[132:133], %[ad1] offset:8192\n\t" \
  "ds_read_b64_tr_b16 v[134:135], %[ad1] offset:8704\n\t" \
  "v_mfma_f32_16x16x32_bf16 v[84:87], v[100:103], v[228:231], v[84:87]\n\t" \
  "v_mfma_f32_16x16x32_bf16 v[56:59], v[104:107], v[172:175], v[56:59]\n\t" \
  "ds_read_b64_tr_b16 v[136:137], %[ad2] offset:8192\n\t" \
  "ds_read_b64_tr_b16 v[138:139], %[ad2] offset:8704\n\t" \
  "v_mfma_f32_16x16x32_bf16 v[88:91], v[104:107], v[228:231], v[88:91]\n\t" \
  "v_mfma_f32_16x16x32_bf16 v[60:63], v[108:111], v[172:175], v[60:63]\n\t" \
  "ds_read_b64_tr_b16 v[140:141], %[ad3] offset:8192\n\t" \
  "ds_read_b64_tr_b16 v[142:143], %[ad3] offset:8704\n\t" \
  "v_mfma_f32_16x16x32_bf16 v[92:95], v[108:111], v[228:231], v[92:95]\n\t" \
  "s_waitcnt lgkmcnt(6)\n\t" \
  "v_mfma_f32_16x16x32_bf16 v[32:35], v[112:115], v[176:179], v[32:35]\n\t" \
  "ds_read_b64_tr_b16 v[96:97], %[ad0] offset:4096\n\t" \
  "ds_read_b64_tr_b16 v[98:99], %[ad0] offset:4608\n\t" \
  "v_mfma_f32_16x16x32_bf16 v[64:67], v[112:115], v[232:235], v[64:67]\n\t" \
  "v_mfma_f32_16x16x32_bf16 v[36:39], v[116:119], v[176:179], v[36:39]\n\t" \
  "ds_read_b64_tr_b16 v[100:101], %[ad1] offset:4096\n\t" \
  "ds_read_b64_tr_b16 v[102:103], %[ad1] offset:4608\n\t" \
  "v_mfma_f32_16x16x32_bf16 v[68:71], v[116:119], v[232:235], v[68:71]\n\t" \
  "v_mfma_f32_16x16x32_bf16 v[40:43], v[120:123], v[176:179], v[40:43]\n\t" \
  "ds_read_b64_tr_b16 v[104:105], %[ad2] offset:4096\n\t" \
  "ds_read_b64_tr_b16 v[106:107], %[ad2] offset:4608\n\t" \
  "v_mfma_f32_16x16x32_bf16 v[72:75], v[120:123], v[232:235], v[72:75]\n\t" \
  "v_mfma_f32_16x16x32_bf16 v[44:47], v[124:127], v[176:179], v[44:47]\n\t" \
  "ds_read_b64_tr_b16 v[108:109], %[ad3] offset:4096\n\t" \
  "ds_read_b64_tr_b16 v[110:111], %[ad3] offset:4608\n\t" \
  "v_mfma_f32_16x16x32_bf16 v[76:79], v[124:127], v[232:235], v[76:79]\n\t" \
  "s_waitcnt lgkmcnt(6)\n\t" \
  "v_mfma_f32_16x16x32_bf16 v[48:51], v[128:131], v[176:179], v[48:51]\n\t" \
  "ds_read_b64_tr_b16 v[112:113], %[ad0] offset:12288\n\t" \
  "ds_read_b64_tr_b16 v[114:115], %[ad0] offset:12800\n\t" \
  "s_add_u32 s62, %[xslo], 118784\n\t" \
  "s_addc_u32 s63, %[xshi], 0\n\t" \
  "global_load_dwordx4 v[168:171], %[voff], s[62:63] offset:2048\n\t" \
  "v_mfma_f32_16x16x32_bf16 v[80:83], v[128:131], v[232:235], v[80:83]\n\t" \
  "s_add_u32 s62, %[xslo], 135168\n\t" \
  "s_addc_u32 s63, %[xshi], 0\n\t" \
  "global_load_dwordx4 v[224:227], %[voff], s[62:63] offset:0\n\t" \
  "v_mfma_f32_16x16x32_bf16 v[52:55], v[132:135], v[176:179], v[52:55]\n\t" \
  "ds_read_b64_tr_b16 v[116:117], %[ad1] offset:12288\n\t" \
  "ds_read_b64_tr_b16 v[118:119], %[ad1] offset:12800\n\t" \
  "s_add_u32 s62, %[xslo], 118784\n\t" \
  "s_addc_u32 s63, %[xshi], 0\n\t" \
  "global_load_dwordx4 v[172:175], %[voff], s[62:63] offset:3072\n\t" \
  "v_mfma_f32_16x16x32_bf16 v[84:87], v[132:135], v[232:235], v[84:87]\n\t" \
  "s_add_u32 s62, %[xslo], 135168\n\t" \
  "s_addc_u32 s63, %[xshi], 0\n\t" \
  "global_load_dwordx4 v[228:231], %[voff], s[62:63] offset:1024\n\t" \
  "v_mfma_f32_16x16x32_bf16 v[56:59], v[136:139], v[176:179], v[56:59]\n\t" \
  "ds_read_b64_tr_b16 v[120:121], %[ad2] offset:12288\n\t" \
  "ds_read_b64_tr_b16 v[122:123], %[ad2] offset:12800\n\t" \
  "v_mfma_f32_16x16x32_bf16 v[88:91], v[136:139], v[232:235], v[88:91]\n\t" \
  "v_mfma_f32_16x16x32_bf16 v[60:63], v[140:143], v[176:179], v[60:63]\n\t" \
  "ds_read_b64_tr_b16 v[124:125], %[ad3] offset:12288\n\t" \
  "ds_read_b64_tr_b16 v[126:127], %[ad3] offset:12800\n\t" \
  "v_mfma_f32_16x16x32_bf16 v[92:95], v[140:143], v[232:235], v[92:95]\n\t" \
  "s_waitcnt vmcnt(20)\n\t" \
  "s_waitcnt lgkmcnt(0)\n\t" \
  "s_mov_b64 s[64:65], exec\n\t" \
  "s_mov_b64 exec, 1\n\t" \
  "ds_add_u32 %[cnt], %[one]\n\t" \
  "s_mov_b64 exec, s[64:65]\n\t" \
  "s_mov_b32 s67, 0x400000\n\t" \
  "1:\n\t" \
  "ds_read_b32 %[vt], %[cnt]\n\t" \
  "s_waitcnt lgkmcnt(0)\n\t" \
  "v_readfirstlane_b32 s66, %[vt]\n\t" \
  "s_nop 3\n\t" \
  "s_cmp_ge_u32 s66, 24\n\t" \
  "s_cbranch_scc1 2f\n\t" \
  "s_sub_u32 s67, s67, 1\n\t" \
  "s_cbranch_scc1 2f\n\t" \
  "s_sleep 1\n\t" \
  "s_branch 1b\n\t" \
  "2:\n\t" \
  "s_waitcnt lgkmcnt(6)\n\t" \
  "v_mfma_f32_16x16x32_bf16 v[32:35], v[96:99], v[180:183], v[32:35]\n\t" \
  "ds_read_b64_tr_b16 v[128:129], %[ad0] offset:16384\n\t" \
  "ds_read_b64_tr_b16 v[130:131], %[ad0] offset:16896\n\t" \
  "v_mfma_f32_16x16x32_bf16 v[64:67], v[96:99], v[236:239], v[64:67]\n\t" \
  "v_mfma_f32_16x16x32_bf16 v[36:39], v[100:103], v[180:183], v[36:39]\n\t" \
  "ds_read_b64_tr_b16 v[132:133], %[ad1] offset:16384\n\t" \
  "ds_read_b64_tr_b16 v[134:135], %[ad1] offset:16896\n\t" \
  "v_mfma_f32_16x16x32_bf16 v[68:71], v[100:103], v[236:239], v[68:71]\n\t" \
  "v_mfma_f32_16x16x32_bf16 v[40:43], v[104:107], v[180:183], v[40:43]\n\t" \
  "ds_read_b64_tr_b16 v[136:137], %[ad2] offset:16384\n\t" \
  "ds_read_b64_tr_b16 v[138:139], %[ad2] offset:16896\n\t" \
  "v_mfma_f32_16x16x32_bf16 v[72:75], v[104:107], v[236:239], v[72:75]\n\t" \
  "v_mfma_f32_16x16x32_bf16 v[44:47], v[108:111], v[180:183], v[44:47]\n\t" \
  "ds_read_b64_tr_b16 v[140:141], %[ad3] offset:16384\n\t" \
  "ds_read_b64_tr_b16 v[142:143], %[ad3] offset:16896\n\t" \
  "v_mfma_f32_16x16x32_bf16 v[76:79], v[108:111], v[236:239], v[76:79]\n\t" \
  "s_waitcnt lgkmcnt(6)\n\t" \
  "v_mfma_f32_16x16x32_bf16 v[48:51], v[112:115], v[180:183], v[48:51]\n\t" \
  "ds_read_b64_tr_b16 v[96:97], %[ad0] offset:24576\n\t" \
  "ds_read_b64_tr_b16 v[98:99], %[ad0] offset:25088\n\t" \
  "v_mfma_f32_16x16x32_bf16 v[80:83], v[112:115], v[236:239], v[80:83]\n\t" \
  "v_mfma_f32_16x16x32_bf16 v[52:55], v[116:119], v[180:183], v[52:55]\n\t" \
  "ds_read_b64_tr_b16 v[100:101], %[ad1] offset:24576\n\t" \
  "ds_read_b64_tr_b16 v[102:103], %[ad1] offset:25088\n\t" \
  "v_mfma_f32_16x16x32_bf16 v[84:87], v[116:119], v[236:239], v[84:87]\n\t" \
  "v_mfma_f32_16x16x32_bf16 v[56:59], v[120:123], v[180:183], v[56:59]\n\t" \
  "ds_read_b64_tr_b16 v[104:105], %[ad2] offset:24576\n\t" \
  "ds_read_b64_tr_b16 v[106:107], %[ad2] offset:25088\n\t" \
  "v_mfma_f32_16x16x32_bf16 v[88:91], v[120:123], v[236:239], v[88:91]\n\t" \
  "v_mfma_f32_16x16x32_bf16 v[60:63], v[124:127], v[180:183], v[60:63]\n\t" \
  "ds_read_b64_tr_b16 v[108:109], %[ad3] offset:24576\n\t" \
  "ds_read_b64_tr_b16 v[110:111], %[ad3] offset:25088\n\t" \
  "v_mfma_f32_16x16x32_bf16 v[92:95], v[124:127], v[236:239], v[92:95]\n\t" \
  "s_waitcnt lgkmcnt(6)\n\t" \
  "v_mfma_f32_16x16x32_bf16 v[32:35], v[128:131], v[184:187], v[32:35]\n\t" \
  "ds_read_b64_tr_b16 v[112:113], %[ad0] offset:20480\n\t" \
  "ds_read_b64_tr_b16 v[114:115], %[ad0] offset:20992\n\t" \
  "v_mfma_f32_16x16x32_bf16 v[64:67], v[128:131], v[240:243], v[64:67]\n\t" \
  "v_mfma_f32_16x16x32_bf16 v[36:39], v[132:135], v[184:187], v[36:39]\n\t" \
  "ds_read_b64_tr_b16 v[116:117], %[ad1] offset:20480\n\t" \
  "ds_read_b64_tr_b16 v[118:119], %[ad1] offset:20992\n\t" \
  "v_mfma_f32_16x16x32_bf16 v[68:71], v[132:135], v[240:243], v[68:71]\n\t" \
  "v_mfma_f32_16x16x32_bf16 v[40:43], v[136:139], v[184:187], v[40:43]\n\t" \
  "ds_read_b64_tr_b16 v[120:121], %[ad2] offset:20480\n\t" \
  "ds_read_b64_tr_b16 v[122:123], %[ad2] offset:20992\n\t" \
  "v_mfma_f32_16x16x32_bf16 v[72:75], v[136:139], v[240:243], v[72:75]\n\t" \
  "v_mfma_f32_16x16x32_bf16 v[44:47], v[140:143], v[184:187], v[44:47]\n\t" \
  "ds_read_b64_tr_b16 v[124:125], %[ad3] offset:20480\n\t" \
  "ds_read_b64_tr_b16 v[126:127], %[ad3] offset:20992\n\t" \
  "v_mfma_f32_16x16x32_bf16 v[76:79], v[140:143], v[240:243], v[76:79]\n\t" \
  "s_waitcnt lgkmcnt(6)\n\t" \
  "v_mfma_f32_16x16x32_bf16 v[48:51], v[96:99], v[184:187], v[48:51]\n\t" \
  "ds_read_b64_tr_b16 v[128:129], %[ad0] offset:28672\n\t" \
  "ds_read_b64_tr_b16 v[130:131], %[ad0] offset:29184\n\t" \
  "s_add_u32 s62, %[xslo], 122880\n\t" \
  "s_addc_u32 s63, %[xshi], 0\n\t" \
  "global_load_dwordx4 v[176:179], %[voff], s[62:63] offset:0\n\t" \
  "v_mfma_f32_16x16x32_bf16 v[80:83], v[96:99], v[240:243], v[80:83]\n\t" \
  "s_add_u32 s62, %[xslo], 135168\n\t" \
  "s_addc_u32 s63, %[xshi], 0\n\t" \
  "global_load_dwordx4 v[232:235], %[voff], s[62:63] offset:2048\n\t" \
  "v_mfma_f32_16x16x32_bf16 v[52:55], v[100:103], v[184:187], v[52:55]\n\t" \
  "ds_read_b64_tr_b16 v[132:133], %[ad1] offset:28672\n\t" \
  "ds_read_b64_tr_b16 v[134:135], %[ad1] offset:29184\n\t" \
  "s_add_u32 s62, %[xslo], 122880\n\t" \
  "s_addc_u32 s63, %[xshi], 0\n\t" \
  "global_load_dwordx4 v[180:183], %[voff], s[62:63] offset:1024\n\t" \
  "v_mfma_f32_16x16x32_bf16 v[84:87], v[100:103], v[240:243], v[84:87]\n\t" \
  "s_add_u32 s62, %[xslo], 135168\n\t" \
  "s_addc_u32 s63, %[xshi], 0\n\t" \
  "global_load_dwordx4 v[236:239], %[voff], s[62:63] offset:3072\n\t" \
  "v_mfma_f32_16x16x32_bf16 v[56:59], v[104:107], v[184:187], v[56:59]\n\t" \
  "ds_read_b64_tr_b16 v[136:137], %[ad2] offset:28672\n\t" \
  "ds_read_b64_tr_b16 v[138:139], %[ad2] offset:29184\n\t" \
  "v_mfma_f32_16x16x32_bf16 v[88:91], v[104:107], v[240:243], v[88:91]\n\t" \
  "v_mfma_f32_16x16x32_bf16 v[60:63], v[108:111], v[184:187], v[60:63]\n\t" \
  "ds_read_b64_tr_b16 v[140:141], %[ad3] offset:28672\n\t" \
  "ds_read_b64_tr_b16 v[142:143], %[ad3] offset:29184\n\t" \
  "v_mfma_f32_16x16x32_bf16 v[92:95], v[108:111], v[240:243], v[92:95]\n\t" \
  "s_waitcnt vmcnt(12)\n\t" \
  "s_waitcnt lgkmcnt(0)\n\t" \
  "s_mov_b64 s[64:65], exec\n\t" \
  "s_mov_b64 exec, 1\n\t" \
  "ds_add_u32 %[cnt], %[one]\n\t" \
  "s_mov_b64 exec, s[64:65]\n\t" \
  "s_mov_b32 s67, 0x400000\n\t" \
  "1:\n\t" \
  "ds_read_b32 %[vt], %[cnt]\n\t" \
  "s_waitcnt lgkmcnt(0)\n\t" \
  "v_readfirstlane_b32 s66, %[vt]\n\t" \
  "s_nop 3\n\t" \
  "s_cmp_ge_u32 s66, 28\n\t" \
  "s_cbranch_scc1 2f\n\t" \
  "s_sub_u32 s67, s67, 1\n\t" \
  "s_cbranch_scc1 2f\n\t" \
  "s_sleep 1\n\t" \
  "s_branch 1b\n\t" \
  "2:\n\t" \
  "s_waitcnt lgkmcnt(6)\n\t" \
  "v_mfma_f32_16x16x32_bf16 v[32:35], v[112:115], v[188:191], v[32:35]\n\t" \
  "ds_read_b64_tr_b16 v[96:97], %[ad0] offset:32768\n\t" \
  "ds_read_b64_tr_b16 v[98:99], %[ad0] offset:33280\n\t" \
  "v_mfma_f32_16x16x32_bf16 v[64:67], v[112:115], v[244:247], v[64:67]\n\t" \
  "v_mfma_f32_16x16x32_bf16 v[36:39], v[116:119], v[188:191], v[36:39]\n\t" \
  "ds_read_b64_tr_b16 v[100:101], %[ad1] offset:32768\n\t" \
  "ds_read_b64_tr_b16 v[102:103], %[ad1] offset:33280\n\t" \
  "v_mfma_f32_16x16x32_bf16 v[68:71], v[116:119], v[244:247], v[68:71]\n\t" \
  "v_mfma_f32_16x16x32_bf16 v[40:43], v[120:123], v[188:191], v[40:43]\n\t" \
  "ds_read_b64_tr_b16 v[104:105], %[ad2] offset:32768\n\t" \
  "ds_read_b64_tr_b16 v[106:107], %[ad2] offset:33280\n\t" \
  "v_mfma_f32_16x16x32_bf16 v[72:75], v[120:123], v[244:247], v[72:75]\n\t" \
  "v_mfma_f32_16x16x32_bf16 v[44:47], v[124:127], v[188:191], v[44:47]\n\t" \
  "ds_read_b64_tr_b16 v[108:109], %[ad3] offset:32768\n\t" \
  "ds_read_b64_tr_b16 v[110:111], %[ad3] offset:33280\n\t" \
  "v_mfma_f32_16x16x32_bf16 v[76:79], v[124:127], v[244:247], v[76:79]\n\t" \
  "s_waitcnt lgkmcnt(6)\n\t" \
  "v_mfma_f32_16x16x32_bf16 v[48:51], v[128:131], v[188:191], v[48:51]\n\t" \
  "ds_read_b64_tr_b16 v[112:113], %[ad0] offset:40960\n\t" \
  "ds_read_b64_tr_b16 v[114:115], %[ad0] offset:41472\n\t" \
  "v_mfma_f32_16x16x32_bf16 v[80:83], v[128:131], v[244:247], v[80:83]\n\t" \
  "v_mfma_f32_16x16x32_bf16 v[52:55], v[132:135], v[188:191], v[52:55]\n\t" \
  "ds_read_b64_tr_b16 v[116:117], %[ad1] offset:40960\n\t" \
  "ds_read_b64_tr_b16 v[118:119], %[ad1] offset:41472\n\t" \
  "v_mfma_f32_16x16x32_bf16 v[84:87], v[132:135], v[244:247], v[84:87]\n\t" \
  "v_mfma_f32_16x16x32_bf16 v[56:59], v[136:139], v[188:191], v[56:59]\n\t" \
  "ds_read_b64_tr_b16 v[120:121], %[ad2] offset:40960\n\t" \
  "ds_read_b64_tr_b16 v[122:123], %[ad2] offset:41472\n\t" \
  "v_mfma_f32_16x16x32_bf16 v[88:91], v[136:139], v[244:247], v[88:91]\n\t" \
  "v_mfma_f32_16x16x32_bf16 v[60:63], v[140:143], v[188:191], v[60:63]\n\t" \
  "ds_read_b64_tr_b16 v[124:125], %[ad3] offset:40960\n\t" \
  "ds_read_b64_tr_b16 v[126:127], %[ad3] offset:41472\n\t" \
  "v_mfma_f32_16x16x32_bf16 v[92:95], v[140:143], v[244:247], v[92:95]\n\t" \
  "s_waitcnt lgkmcnt(6)\n\t" \
  "v_mfma_f32_16x16x32_bf16 v[32:35], v[96:99], v[192:195], v[32:35]\n\t" \
  "ds_read_b64_tr_b16 v[128:129], %[ad0] offset:36864\n\t" \
  "ds_read_b64_tr_b16 v[130:131], %[ad0] offset:37376\n\t" \
  "v_mfma_f32_16x16x32_bf16 v[64:67], v[96:99], v[248:251], v[64:67]\n\t" \
  "v_mfma_f32_16x16x32_bf16 v[36:39], v[100:103], v[192:195], v[36:39]\n\t" \
  "ds_read_b64_tr_b16 v[132:133], %[ad1] offset:36864\n\t" \
  "ds_read_b64_tr_b16 v[134:135], %[ad1] offset:37376\n\t" \
  "v_mfma_f32_16x16x32_bf16 v[68:71], v[100:103], v[248:251], v[68:71]\n\t" \
  "v_mfma_f32_16x16x32_bf16 v[40:43], v[104:107], v[192:195], v[40:43]\n\t" \
  "ds_read_b64_tr_b16 v[136:137], %[ad2] offset:36864\n\t" \
  "ds_read_b64_tr_b16 v[138:139], %[ad2] offset:37376\n\t" \
  "v_mfma_f32_16x16x32_bf16 v[72:75], v[104:107], v[248:251], v[72:75]\n\t" \
  "v_mfma_f32_16x16x32_bf16 v[44:47], v[108:111], v[192:195], v[44:47]\n\t" \
  "ds_read_b64_tr_b16 v[140:141], %[ad3] offset:36864\n\t" \
  "ds_read_b64_tr_b16 v[142:143], %[ad3] offset:37376\n\t" \
  "v_mfma_f32_16x16x32_bf16 v[76:79], v[108:111], v[248:251], v[76:79]\n\t" \
  "s_waitcnt lgkmcnt(6)\n\t" \
  "v_mfma_f32_16x16x32_bf16 v[48:51], v[112:115], v[192:195], v[48:51]\n\t" \
  "ds_read_b64_tr_b16 v[96:97], %[ad0] offset:45056\n\t" \
  "ds_read_b64_tr_b16 v[98:99], %[ad0] offset:45568\n\t" \
  "s_add_u32 s62, %[xslo], 122880\n\t" \
  "s_addc_u32 s63, %[xshi], 0\n\t" \
  "global_load_dwordx4 v[184:187], %[voff], s[62:63] offset:2048\n\t" \
  "v_mfma_f32_16x16x32_bf16 v[80:83], v[112:115], v[248:251], v[80:83]\n\t" \
  "s_add_u32 s62, %[xslo], 139264\n\t" \
  "s_addc_u32 s63, %[xshi], 0\n\t" \
  "global_load_dwordx4 v[240:243], %[voff], s[62:63] offset:0\n\t" \
  "v_mfma_f32_16x16x32_bf16 v[52:55], v[116:119], v[192:195], v[52:55]\n\t" \
  "ds_read_b64_tr_b16 v[100:101], %[ad1] offset:45056\n\t" \
  "ds_read_b64_tr_b16 v[102:103], %[ad1] offset:45568\n\t" \
  "s_add_u32 s62, %[xslo], 122880\n\t" \
  "s_addc_u32 s63, %[xshi], 0\n\t" \
  "global_load_dwordx4 v[188:191], %[voff], s[62:63] offset:3072\n\t" \
  "v_mfma_f32_16x16x32_bf16 v[84:87], v[116:119], v[248:251], v[84:87]\n\t" \
  "s_add_u32 s62, %[xslo], 139264\n\t" \
  "s_addc_u32 s63, %[xshi], 0\n\t" \
  "global_load_dwordx4 v[244:247], %[voff], s[62:63] offset:1024\n\t" \
  "v_mfma_f32_16x16x32_bf16 v[56:59], v[120:123], v[192:195], v[56:59]\n\t" \
  "ds_read_b64_tr_b16 v[104:105], %[ad2] offset:45056\n\t" \
  "ds_read_b64_tr_b16 v[106:107], %[ad2] offset:45568\n\t" \
  "v_mfma_f32_16x16x32_bf16 v[88:91], v[120:123], v[248:251], v[88:91]\n\t" \
  "v_mfma_f32_16x16x32_bf16 v[60:63], v[124:127], v[192:195], v[60:63]\n\t" \
  "ds_read_b64_tr_b16 v[108:109], %[ad3] offset:45056\n\t" \
  "ds_read_b64_tr_b16 v[110:111], %[ad3] offset:45568\n\t" \
  "v_mfma_f32_16x16x32_bf16 v[92:95], v[124:127], v[248:251], v[92:95]\n\t" \
  "s_waitcnt lgkmcnt(8)\n\t" \
  "v_mfma_f32_16x16x32_bf16 v[32:35], v[128:131], v[196:199], v[32:35]\n\t" \
  "v_mfma_f32_16x16x32_bf16 v[64:67], v[128:131], v[252:255], v[64:67]\n\t" \
  "v_mfma_f32_16x16x32_bf16 v[36:39], v[132:135], v[196:199], v[36:39]\n\t" \
  "v_mfma_f32_16x16x32_bf16 v[68:71], v[132:135], v[252:255], v[68:71]\n\t" \
  "v_mfma_f32_16x16x32_bf16 v[40:43], v[136:139], v[196:199], v[40:43]\n\t" \
  "v_mfma_f32_16x16x32_bf16 v[72:75], v[136:139], v[252:255], v[72:75]\n\t" \
  "v_mfma_f32_16x16x32_bf16 v[44:47], v[140:143], v[196:199], v[44:47]\n\t" \
  "v_mfma_f32_16x16x32_bf16 v[76:79], v[140:143], v[252:255], v[76:79]\n\t" \
  "s_waitcnt lgkmcnt(0)\n\t" \
  "v_mfma_f32_16x16x32_bf16 v[48:51], v[96:99], v[196:199], v[48:51]\n\t" \
  "v_mfma_f32_16x16x32_bf16 v[80:83], v[96:99], v[252:255], v[80:83]\n\t" \
  "v_mfma_f32_16x16x32_bf16 v[52:55], v[100:103], v[196:199], v[52:55]\n\t" \
  "v_mfma_f32_16x16x32_bf16 v[84:87], v[100:103], v[252:255], v[84:87]\n\t" \
  "v_mfma_f32_16x16x32_bf16 v[56:59], v[104:107], v[196:199], v[56:59]\n\t" \
  "v_mfma_f32_16x16x32_bf16 v[88:91], v[104:107], v[252:255], v[88:91]\n\t" \
  "v_mfma_f32_16x16x32_bf16 v[60:63], v[108:111], v[196:199], v[60:63]\n\t" \
  "v_mfma_f32_16x16x32_bf16 v[92:95], v[108:111], v[252:255], v[92:95]\n\t" \
  "v_subrev_u32 %[ad0], 0x10000, %[ad0]\n\t" \
  "v_subrev_u32 %[ad1], 0x10000, %[ad1]\n\t" \
  "v_subrev_u32 %[ad2], 0x10000, %[ad2]\n\t" \
  "v_subrev_u32 %[ad3], 0x10000, %[ad3]\n\t" \
  "s_nop 7\n\t" \
  "s_nop 7\n\t" \
  "s_add_u32 s62, %[xslo], 126976\n\t" \
  "s_addc_u32 s63, %[xshi], 0\n\t" \
  "global_load_dwordx4 v[192:195], %[voff], s[62:63] offset:0\n\t" \
  "s_add_u32 s62, %[xslo], 139264\n\t" \
  "s_addc_u32 s63, %[xshi], 0\n\t" \
  "global_load_dwordx4 v[248:251], %[voff], s[62:63] offset:2048\n\t" \
  "s_add_u32 s62, %[xslo], 126976\n\t" \
  "s_addc_u32 s63, %[xshi], 0\n\t" \
  "global_load_dwordx4 v[196:199], %[voff], s[62:63] offset:1024\n\t" \
  "s_add_u32 s62, %[xslo], 139264\n\t" \
  "s_addc_u32 s63, %[xshi], 0\n\t" \
  "global_load_dwordx4 v[252:255], %[voff], s[62:63] offset:3072\n\t"

#define K12_ROUND0_ASM \
  "s_add_u32 s62, %[xslo], 0\n\t" \
  "s_addc_u32 s63, %[xshi], 0\n\t" \
  "global_load_dwordx4 v[144:147], %[voff], s[62:63] offset:0 sc1\n\t" \
  "s_add_u32 s62, %[xslo], 12288\n\t" \
  "s_addc_u32 s63, %[xshi], 0\n\t" \
  "global_load_dwordx4 v[200:203], %[voff], s[62:63] offset:2048 sc1\n\t" \
  "s_add_u32 s62, %[xslo], 0\n\t" \
  "s_addc_u32 s63, %[xshi], 0\n\t" \
  "global_load_dwordx4 v[148:151], %[voff], s[62:63] offset:1024 sc1\n\t" \
  "s_add_u32 s62, %[xslo], 12288\n\t" \
  "s_addc_u32 s63, %[xshi], 0\n\t" \
  "global_load_dwordx4 v[204:207], %[voff], s[62:63] offset:3072 sc1\n\t" \
  "s_add_u32 s62, %[xslo], 0\n\t" \
  "s_addc_u32 s63, %[xshi], 0\n\t" \
  "global_load_dwordx4 v[152:155], %[voff], s[62:63] offset:2048 sc1\n\t" \
  "s_add_u32 s62, %[xslo], 16384\n\t" \
  "s_addc_u32 s63, %[xshi], 0\n\t" \
  "global_load_dwordx4 v[208:211], %[voff], s[62:63] offset:0 sc1\n\t" \
  "s_add_u32 s62, %[xslo], 0\n\t" \
  "s_addc_u32 s63, %[xshi], 0\n\t" \
  "global_load_dwordx4 v[156:159], %[voff], s[62:63] offset:3072 sc1\n\t" \
  "s_add_u32 s62, %[xslo], 16384\n\t" \
  "s_addc_u32 s63, %[xshi], 0\n\t" \
  "global_load_dwordx4 v[212:215], %[voff], s[62:63] offset:1024 sc1\n\t" \
  "s_add_u32 s62, %[xslo], 4096\n\t" \
  "s_addc_u32 s63, %[xshi], 0\n\t" \
  "global_load_dwordx4 v[160:163], %[voff], s[62:63] offset:0 sc1\n\t" \
  "s_add_u32 s62, %[xslo], 16384\n\t" \
  "s_addc_u32 s63, %[xshi], 0\n\t" \
  "global_load_dwordx4 v[216:219], %[voff], s[62:63] offset:2048 sc1\n\t" \
  "s_add_u32 s62, %[xslo], 4096\n\t" \
  "s_addc_u32 s63, %[xshi], 0\n\t" \
  "global_load_dwordx4 v[164:167], %[voff], s[62:63] offset:1024 sc1\n\t" \
  "s_add_u32 s62, %[xslo], 16384\n\t" \
  "s_addc_u32 s63, %[xshi], 0\n\t" \
  "global_load_dwordx4 v[220:223], %[voff], s[62:63] offset:3072 sc1\n\t" \
  "s_add_u32 s62, %[xslo], 4096\n\t" \
  "s_addc_u32 s63, %[xshi], 0\n\t" \
  "global_load_dwordx4 v[168:171], %[voff], s[62:63] offset:2048 sc1\n\t" \
  "s_add_u32 s62, %[xslo], 20480\n\t" \
  "s_addc_u32 s63, %[xshi], 0\n\t" \
  "global_load_dwordx4 v[224:227], %[voff], s[62:63] offset:0 sc1\n\t" \
  "s_add_u32 s62, %[xslo], 4096\n\t" \
  "s_addc_u32 s63, %[xshi], 0\n\t" \
  "global_load_dwordx4 v[172:175], %[voff], s[62:63] offset:3072 sc1\n\t" \
  "s_add_u32 s62, %[xslo], 20480\n\t" \
  "s_addc_u32 s63, %[xshi], 0\n\t" \
  "global_load_dwordx4 v[228:231], %[voff], s[62:63] offset:1024 sc1\n\t" \
  "s_add_u32 s62, %[xslo], 8192\n\t" \
  "s_addc_u32 s63, %[xshi], 0\n\t" \
  "global_load_dwordx4 v[176:179], %[voff], s[62:63] offset:0 sc1\n\t" \
  "s_add_u32 s62, %[xslo], 20480\n\t" \
  "s_addc_u32 s63, %[xshi], 0\n\t" \
  "global_load_dwordx4 v[232:235], %[voff], s[62:63] offset:2048 sc1\n\t" \
  "s_add_u32 s62, %[xslo], 8192\n\t" \
  "s_addc_u32 s63, %[xshi], 0\n\t" \
  "global_load_dwordx4 v[180:183], %[voff], s[62:63] offset:1024 sc1\n\t" \
  "s_add_u32 s62, %[xslo], 20480\n\t" \
  "s_addc_u32 s63, %[xshi], 0\n\t" \
  "global_load_dwordx4 v[236:239], %[voff], s[62:63] offset:3072 sc1\n\t" \
  "s_add_u32 s62, %[xslo], 8192\n\t" \
  "s_addc_u32 s63, %[xshi], 0\n\t" \
  "global_load_dwordx4 v[184:187], %[voff], s[62:63] offset:2048 sc1\n\t" \
  "s_add_u32 s62, %[xslo], 24576\n\t" \
  "s_addc_u32 s63, %[xshi], 0\n\t" \
  "global_load_dwordx4 v[240:243], %[voff], s[62:63] offset:0 sc1\n\t" \
  "s_add_u32 s62, %[xslo], 8192\n\t" \
  "s_addc_u32 s63, %[xshi], 0\n\t" \
  "global_load_dwordx4 v[188:191], %[voff], s[62:63] offset:3072 sc1\n\t" \
  "s_add_u32 s62, %[xslo], 24576\n\t" \
  "s_addc_u32 s63, %[xshi], 0\n\t" \
  "global_load_dwordx4 v[244:247], %[voff], s[62:63] offset:1024 sc1\n\t" \
  "s_add_u32 s62, %[xslo], 12288\n\t" \
  "s_addc_u32 s63, %[xshi], 0\n\t" \
  "global_load_dwordx4 v[192:195], %[voff], s[62:63] offset:0 sc1\n\t" \
  "s_add_u32 s62, %[xslo], 24576\n\t" \
  "s_addc_u32 s63, %[xshi], 0\n\t" \
  "global_load_dwordx4 v[248:251], %[voff], s[62:63] offset:2048 sc1\n\t" \
  "s_add_u32 s62, %[xslo], 12288\n\t" \
  "s_addc_u32 s63, %[xshi], 0\n\t" \
  "global_load_dwordx4 v[196:199], %[voff], s[62:63] offset:1024 sc1\n\t" \
  "s_add_u32 s62, %[xslo], 24576\n\t" \
  "s_addc_u32 s63, %[xshi], 0\n\t" \
  "global_load_dwordx4 v[252:255], %[voff], s[62:63] offset:3072 sc1\n\t" \
  "ds_read_b64_tr_b16 v[96:97], %[ad0] offset:0\n\t" \
  "ds_read_b64_tr_b16 v[98:99], %[ad0] offset:512\n\t" \
  "ds_read_b64_tr_b16 v[100:101], %[ad1] offset:0\n\t" \
  "ds_read_b64_tr_b16 v[102:103], %[ad1] offset:512\n\t" \
  "ds_read_b64_tr_b16 v[104:105], %[ad2] offset:0\n\t" \
  "ds_read_b64_tr_b16 v[106:107], %[ad2] offset:512\n\t" \
  "ds_read_b64_tr_b16 v[108:109], %[ad3] offset:0\n\t" \
  "ds_read_b64_tr_b16 v[110:111], %[ad3] offset:512\n\t" \
  "s_waitcnt lgkmcnt(4)\n\t" \
  "ds_read_b64_tr_b16 v[112:113], %[ad0] offset:8192\n\t" \
  "ds_read_b64_tr_b16 v[114:115], %[ad0] offset:8704\n\t" \
  "ds_read_b64_tr_b16 v[116:117], %[ad1] offset:8192\n\t" \
  "ds_read_b64_tr_b16 v[118:119], %[ad1] offset:8704\n\t" \
  "ds_read_b64_tr_b16 v[120:121], %[ad2] offset:8192\n\t" \
  "ds_read_b64_tr_b16 v[122:123], %[ad2] offset:8704\n\t" \
  "ds_read_b64_tr_b16 v[124:125], %[ad3] offset:8192\n\t" \
  "ds_read_b64_tr_b16 v[126:127], %[ad3] offset:8704\n\t" \
  "s_waitcnt vmcnt(24)\n\t" \
  "s_waitcnt lgkmcnt(6)\n\t" \
  "v_mfma_f32_16x16x32_bf16 v[32:35], v[96:99], v[144:147], 0\n\t" \
  "ds_read_b64_tr_b16 v[128:129], %[ad0] offset:4096\n\t" \
  "ds_read_b64_tr_b16 v[130:131], %[ad0] offset:4608\n\t" \
  "v_mfma_f32_16x16x32_bf16 v[64:67], v[96:99], v[200:203], 0\n\t" \
  "v_mfma_f32_16x16x32_bf16 v[36:39], v[100:103], v[144:147], 0\n\t" \
  "ds_read_b64_tr_b16 v[132:133], %[ad1] offset:4096\n\t" \
  "ds_read_b64_tr_b16 v[134:135], %[ad1] offset:4608\n\t" \
  "v_mfma_f32_16x16x32_bf16 v[68:71], v[100:103], v[200:203], 0\n\t" \
  "v_mfma_f32_16x16x32_bf16 v[40:43], v[104:107], v[144:147], 0\n\t" \
  "ds_read_b64_tr_b16 v[136:137], %[ad2] offset:4096\n\t" \
  "ds_read_b64_tr_b16 v[138:139], %[ad2] offset:4608\n\t" \
  "v_mfma_f32_16x16x32_bf16 v[72:75], v[104:107], v[200:203], 0\n\t" \
  "v_mfma_f32_16x16x32_bf16 v[44:47], v[108:111], v[144:147], 0\n\t" \
  "ds_read_b64_tr_b16 v[140:141], %[ad3] offset:4096\n\t" \
  "ds_read_b64_tr_b16 v[142:143], %[ad3] offset:4608\n\t" \
  "v_mfma_f32_16x16x32_bf16 v[76:79], v[108:111], v[200:203], 0\n\t" \
  "s_waitcnt lgkmcnt(6)\n\t" \
  "v_mfma_f32_16x16x32_bf16 v[48:51], v[112:115], v[144:147], 0\n\t" \
  "ds_read_b64_tr_b16 v[96:97], %[ad0] offset:12288\n\t" \
  "ds_read_b64_tr_b16 v[98:99], %[ad0] offset:12800\n\t" \
  "v_mfma_f32_16x16x32_bf16 v[80:83], v[112:115], v[200:203], 0\n\t" \
  "v_mfma_f32_16x16x32_bf16 v[52:55], v[116:119], v[144:147], 0\n\t" \
  "ds_read_b64_tr_b16 v[100:101], %[ad1] offset:12288\n\t" \
  "ds_read_b64_tr_b16 v[102:103], %[ad1] offset:12800\n\t" \
  "v_mfma_f32_16x16x32_bf16 v[84:87], v[116:119], v[200:203], 0\n\t" \
  "v_mfma_f32_16x16x32_bf16 v[56:59], v[120:123], v[144:147], 0\n\t" \
  "ds_read_b64_tr_b16 v[104:105], %[ad2] offset:12288\n\t" \
  "ds_read_b64_tr_b16 v[106:107], %[ad2] offset:12800\n\t" \
  "v_mfma_f32_16x16x32_bf16 v[88:91], v[120:123], v[200:203], 0\n\t" \
  "v_mfma_f32_16x16x32_bf16 v[60:63], v[124:127], v[144:147], 0\n\t" \
  "ds_read_b64_tr_b16 v[108:109], %[ad3] offset:12288\n\t" \
  "ds_read_b64_tr_b16 v[110:111], %[ad3] offset:12800\n\t" \
  "v_mfma_f32_16x16x32_bf16 v[92:95], v[124:127], v[200:203], 0\n\t" \
  "s_waitcnt lgkmcnt(6)\n\t" \
  "v_mfma_f32_16x16x32_bf16 v[32:35], v[128:131], v[148:151], v[32:35]\n\t" \
  "ds_read_b64_tr_b16 v[112:113], %[ad0] offset:16384\n\t" \
  "ds_read_b64_tr_b16 v[114:115], %[ad0] offset:16896\n\t" \
  "v_mfma_f32_16x16x32_bf16 v[64:67], v[128:131], v[204:207], v[64:67]\n\t" \
  "v_mfma_f32_16x16x32_bf16 v[36:39], v[132:135], v[148:151], v[36:39]\n\t" \
  "ds_read_b64_tr_b16 v[116:117], %[ad1] offset:16384\n\t" \
  "ds_read_b64_tr_b16 v[118:119], %[ad1] offset:16896\n\t" \
  "v_mfma_f32_16x16x32_bf16 v[68:71], v[132:135], v[204:207], v[68:71]\n\t" \
  "v_mfma_f32_16x16x32_bf16 v[40:43], v[136:139], v[148:151], v[40:43]\n\t" \
  "ds_read_b64_tr_b16 v[120:121], %[ad2] offset:16384\n\t" \
  "ds_read_b64_tr_b16 v[122:123], %[ad2] offset:16896\n\t" \
  "v_mfma_f32_16x16x32_bf16 v[72:75], v[136:139], v[204:207], v[72:75]\n\t" \
  "v_mfma_f32_16x16x32_bf16 v[44:47], v[140:143], v[148:151], v[44:47]\n\t" \
  "ds_read_b64_tr_b16 v[124:125], %[ad3] offset:16384\n\t" \
  "ds_read_b64_tr_b16 v[126:127], %[ad3] offset:16896\n\t" \
  "v_mfma_f32_16x16x32_bf16 v[76:79], v[140:143], v[204:207], v[76:79]\n\t" \
  "s_waitcnt lgkmcnt(6)\n\t" \
  "v_mfma_f32_16x16x32_bf16 v[48:51], v[96:99], v[148:151], v[48:51]\n\t" \
  "ds_read_b64_tr_b16 v[128:129], %[ad0] offset:24576\n\t" \
  "ds_read_b64_tr_b16 v[130:131], %[ad0] offset:25088\n\t" \
  "v_mfma_f32_16x16x32_bf16 v[80:83], v[96:99], v[204:207], v[80:83]\n\t" \
  "v_mfma_f32_16x16x32_bf16 v[52:55], v[100:103], v[148:151], v[52:55]\n\t" \
  "ds_read_b64_tr_b16 v[132:133], %[ad1] offset:24576\n\t" \
  "ds_read_b64_tr_b16 v[134:135], %[ad1] offset:25088\n\t" \
  "v_mfma_f32_16x16x32_bf16 v[84:87], v[100:103], v[204:207], v[84:87]\n\t" \
  "v_mfma_f32_16x16x32_bf16 v[56:59], v[104:107], v[148:151], v[56:59]\n\t" \
  "ds_read_b64_tr_b16 v[136:137], %[ad2] offset:24576\n\t" \
  "ds_read_b64_tr_b16 v[138:139], %[ad2] offset:25088\n\t" \
  "v_mfma_f32_16x16x32_bf16 v[88:91], v[104:107], v[204:207], v[88:91]\n\t" \
  "v_mfma_f32_16x16x32_bf16 v[60:63], v[108:111], v[148:151], v[60:63]\n\t" \
  "ds_read_b64_tr_b16 v[140:141], %[ad3] offset:24576\n\t" \
  "ds_read_b64_tr_b16 v[142:143], %[ad3] offset:25088\n\t" \
  "v_mfma_f32_16x16x32_bf16 v[92:95], v[108:111], v[204:207], v[92:95]\n\t" \
  "s_waitcnt vmcnt(20)\n\t" \
  "s_waitcnt lgkmcnt(6)\n\t" \
  "v_mfma_f32_16x16x32_bf16 v[32:35], v[112:115], v[152:155], v[32:35]\n\t" \
  "ds_read_b64_tr_b16 v[96:97], %[ad0] offset:20480\n\t" \
  "ds_read_b64_tr_b16 v[98:99], %[ad0] offset:20992\n\t" \
  "v_mfma_f32_16x16x32_bf16 v[64:67], v[112:115], v[208:211], v[64:67]\n\t" \
  "v_mfma_f32_16x16x32_bf16 v[36:39], v[116:119], v[152:155], v[36:39]\n\t" \
  "ds_read_b64_tr_b16 v[100:101], %[ad1] offset:20480\n\t" \
  "ds_read_b64_tr_b16 v[102:103], %[ad1] offset:20992\n\t" \
  "v_mfma_f32_16x16x32_bf16 v[68:71], v[116:119], v[208:211], v[68:71]\n\t" \
  "v_mfma_f32_16x16x32_bf16 v[40:43], v[120:123], v[152:155], v[40:43]\n\t" \
  "ds_read_b64_tr_b16 v[104:105], %[ad2] offset:20480\n\t" \
  "ds_read_b64_tr_b16 v[106:107], %[ad2] offset:20992\n\t" \
  "v_mfma_f32_16x16x32_bf16 v[72:75], v[120:123], v[208:211], v[72:75]\n\t" \
  "v_mfma_f32_16x16x32_bf16 v[44:47], v[124:127], v[152:155], v[44:47]\n\t" \
  "ds_read_b64_tr_b16 v[108:109], %[ad3] offset:20480\n\t" \
  "ds_read_b64_tr_b16 v[110:111], %[ad3] offset:20992\n\t" \
  "v_mfma_f32_16x16x32_bf16 v[76:79], v[124:127], v[208:211], v[76:79]\n\t" \
  "s_waitcnt lgkmcnt(6)\n\t" \
  "v_mfma_f32_16x16x32_bf16 v[48:51], v[128:131], v[152:155], v[48:51]\n\t" \
  "ds_read_b64_tr_b16 v[112:113], %[ad0] offset:28672\n\t" \
  "ds_read_b64_tr_b16 v[114:115], %[ad0] offset:29184\n\t" \
  "v_mfma_f32_16x16x32_bf16 v[80:83], v[128:131], v[208:211], v[80:83]\n\t" \
  "v_mfma_f32_16x16x32_bf16 v[52:55], v[132:135], v[152:155], v[52:55]\n\t" \
  "ds_read_b64_tr_b16 v[116:117], %[ad1] offset:28672\n\t" \
  "ds_read_b64_tr_b16 v[118:119], %[ad1] offset:29184\n\t" \
  "v_mfma_f32_16x16x32_bf16 v[84:87], v[132:135], v[208:211], v[84:87]\n\t" \
  "v_mfma_f32_16x16x32_bf16 v[56:59], v[136:139], v[152:155], v[56:59]\n\t" \
  "ds_read_b64_tr_b16 v[120:121], %[ad2] offset:28672\n\t" \
  "ds_read_b64_tr_b16 v[122:123], %[ad2] offset:29184\n\t" \
  "v_mfma_f32_16x16x32_bf16 v[88:91], v[136:139], v[208:211], v[88:91]\n\t" \
  "s_add_u32 s62, %[xslo], 114688\n\t" \
  "s_addc_u32 s63, %[xshi], 0\n\t" \
  "global_load_dwordx4 v[144:147], %[voff], s[62:63] offset:0 sc1\n\t" \
  "s_add_u32 s62, %[xslo], 126976\n\t" \
  "s_addc_u32 s63, %[xshi], 0\n\t" \
  "global_load_dwordx4 v[200:203], %[voff], s[62:63] offset:2048 sc1\n\t" \
  "v_mfma_f32_16x16x32_bf16 v[60:63], v[140:143], v[152:155], v[60:63]\n\t" \
  "ds_read_b64_tr_b16 v[124:125], %[ad3] offset:28672\n\t" \
  "ds_read_b64_tr_b16 v[126:127], %[ad3] offset:29184\n\t" \
  "v_mfma_f32_16x16x32_bf16 v[92:95], v[140:143], v[208:211], v[92:95]\n\t" \
  "s_add_u32 s62, %[xslo], 114688\n\t" \
  "s_addc_u32 s63, %[xshi], 0\n\t" \
  "global_load_dwordx4 v[148:151], %[voff], s[62:63] offset:1024 sc1\n\t" \
  "s_add_u32 s62, %[xslo], 126976\n\t" \
  "s_addc_u32 s63, %[xshi], 0\n\t" \
  "global_load_dwordx4 v[204:207], %[voff], s[62:63] offset:3072 sc1\n\t" \
  "s_waitcnt lgkmcnt(6)\n\t" \
  "v_mfma_f32_16x16x32_bf16 v[32:35], v[96:99], v[156:159], v[32:35]\n\t" \
  "ds_read_b64_tr_b16 v[128:129], %[ad0] offset:32768\n\t" \
  "ds_read_b64_tr_b16 v[130:131], %[ad0] offset:33280\n\t" \
  "v_mfma_f32_16x16x32_bf16 v[64:67], v[96:99], v[212:215], v[64:67]\n\t" \
  "v_mfma_f32_16x16x32_bf16 v[36:39], v[100:103], v[156:159], v[36:39]\n\t" \
  "ds_read_b64_tr_b16 v[132:133], %[ad1] offset:32768\n\t" \
  "ds_read_b64_tr_b16 v[134:135], %[ad1] offset:33280\n\t" \
  "v_mfma_f32_16x16x32_bf16 v[68:71], v[100:103], v[212:215], v[68:71]\n\t" \
  "v_mfma_f32_16x16x32_bf16 v[40:43], v[104:107], v[156:159], v[40:43]\n\t" \
  "ds_read_b64_tr_b16 v[136:137], %[ad2] offset:32768\n\t" \
  "ds_read_b64_tr_b16 v[138:139], %[ad2] offset:33280\n\t" \
  "v_mfma_f32_16x16x32_bf16 v[72:75], v[104:107], v[212:215], v[72:75]\n\t" \
  "v_mfma_f32_16x16x32_bf16 v[44:47], v[108:111], v[156:159], v[44:47]\n\t" \
  "ds_read_b64_tr_b16 v[140:141], %[ad3] offset:32768\n\t" \
  "ds_read_b64_tr_b16 v[142:143], %[ad3] offset:33280\n\t" \
  "v_mfma_f32_16x16x32_bf16 v[76:79], v[108:111], v[212:215], v[76:79]\n\t" \
  "s_waitcnt lgkmcnt(6)\n\t" \
  "v_mfma_f32_16x16x32_bf16 v[48:51], v[112:115], v[156:159], v[48:51]\n\t" \
  "ds_read_b64_tr_b16 v[96:97], %[ad0] offset:40960\n\t" \
  "ds_read_b64_tr_b16 v[98:99], %[ad0] offset:41472\n\t" \
  "v_mfma_f32_16x16x32_bf16 v[80:83], v[112:115], v[212:215], v[80:83]\n\t" \
  "v_mfma_f32_16x16x32_bf16 v[52:55], v[116:119], v[156:159], v[52:55]\n\t" \
  "ds_read_b64_tr_b16 v[100:101], %[ad1] offset:40960\n\t" \
  "ds_read_b64_tr_b16 v[102:103], %[ad1] offset:41472\n\t" \
  "v_mfma_f32_16x16x32_bf16 v[84:87], v[116:119], v[212:215], v[84:87]\n\t" \
  "v_mfma_f32_16x16x32_bf16 v[56:59], v[120:123], v[156:159], v[56:59]\n\t" \
  "ds_read_b64_tr_b16 v[104:105], %[ad2] offset:40960\n\t" \
  "ds_read_b64_tr_b16 v[106:107], %[ad2] offset:41472\n\t" \
  "v_mfma_f32_16x16x32_bf16 v[88:91], v[120:123], v[212:215], v[88:91]\n\t" \
  "v_mfma_f32_16x16x32_bf16 v[60:63], v[124:127], v[156:159], v[60:63]\n\t" \
  "ds_read_b64_tr_b16 v[108:109], %[ad3] offset:40960\n\t" \
  "ds_read_b64_tr_b16 v[110:111], %[ad3] offset:41472\n\t" \
  "v_mfma_f32_16x16x32_bf16 v[92:95], v[124:127], v[212:215], v[92:95]\n\t" \
  "s_waitcnt vmcnt(20)\n\t" \
  "s_waitcnt lgkmcnt(6)\n\t" \
  "v_mfma_f32_16x16x32_bf16 v[32:35], v[128:131], v[160:163], v[32:35]\n\t" \
  "ds_read_b64_tr_b16 v[112:113], %[ad0] offset:36864\n\t" \
  "ds_read_b64_tr_b16 v[114:115], %[ad0] offset:37376\n\t" \
  "v_mfma_f32_16x16x32_bf16 v[64:67], v[128:131], v[216:219], v[64:67]\n\t" \
  "v_mfma_f32_16x16x32_bf16 v[36:39], v[132:135], v[160:163], v[36:39]\n\t" \
  "ds_read_b64_tr_b16 v[116:117], %[ad1] offset:36864\n\t" \
  "ds_read_b64_tr_b16 v[118:119], %[ad1] offset:37376\n\t" \
  "v_mfma_f32_16x16x32_bf16 v[68:71], v[132:135], v[216:219], v[68:71]\n\t" \
  "v_mfma_f32_16x16x32_bf16 v[40:43], v[136:139], v[160:163], v[40:43]\n\t" \
  "ds_read_b64_tr_b16 v[120:121], %[ad2] offset:36864\n\t" \
  "ds_read_b64_tr_b16 v[122:123], %[ad2] offset:37376\n\t" \
  "v_mfma_f32_16x16x32_bf16 v[72:75], v[136:139], v[216:219], v[72:75]\n\t" \
  "v_mfma_f32_16x16x32_bf16 v[44:47], v[140:143], v[160:163], v[44:47]\n\t" \
  "ds_read_b64_tr_b16 v[124:125], %[ad3] offset:36864\n\t" \
  "ds_read_b64_tr_b16 v[126:127], %[ad3] offset:37376\n\t" \
  "v_mfma_f32_16x16x32_bf16 v[76:79], v[140:143], v[216:219], v[76:79]\n\t" \
  "s_waitcnt lgkmcnt(6)\n\t" \
  "v_mfma_f32_16x16x32_bf16 v[48:51], v[96:99], v[160:163], v[48:51]\n\t" \
  "ds_read_b64_tr_b16 v[128:129], %[ad0] offset:45056\n\t" \
  "ds_read_b64_tr_b16 v[130:131], %[ad0] offset:45568\n\t" \
  "v_mfma_f32_16x16x32_bf16 v[80:83], v[96:99], v[216:219], v[80:83]\n\t" \
  "v_mfma_f32_16x16x32_bf16 v[52:55], v[100:103], v[160:163], v[52:55]\n\t" \
  "ds_read_b64_tr_b16 v[132:133], %[ad1] offset:45056\n\t" \
  "ds_read_b64_tr_b16 v[134:135], %[ad1] offset:45568\n\t" \
  "v_mfma_f32_16x16x32_bf16 v[84:87], v[100:103], v[216:219], v[84:87]\n\t" \
  "v_mfma_f32_16x16x32_bf16 v[56:59], v[104:107], v[160:163], v[56:59]\n\t" \
  "ds_read_b64_tr_b16 v[136:137], %[ad2] offset:45056\n\t" \
  "ds_read_b64_tr_b16 v[138:139], %[ad2] offset:45568\n\t" \
  "v_mfma_f32_16x16x32_bf16 v[88:91], v[104:107], v[216:219], v[88:91]\n\t" \
  "s_add_u32 s62, %[xslo], 114688\n\t" \
  "s_addc_u32 s63, %[xshi], 0\n\t" \
  "global_load_dwordx4 v[152:155], %[voff], s[62:63] offset:2048 sc1\n\t" \
  "s_add_u32 s62, %[xslo], 131072\n\t" \
  "s_addc_u32 s63, %[xshi], 0\n\t" \
  "global_load_dwordx4 v[208:211], %[voff], s[62:63] offset:0 sc1\n\t" \
  "v_mfma_f32_16x16x32_bf16 v[60:63], v[108:111], v[160:163], v[60:63]\n\t" \
  "ds_read_b64_tr_b16 v[140:141], %[ad3] offset:45056\n\t" \
  "ds_read_b64_tr_b16 v[142:143], %[ad3] offset:45568\n\t" \
  "v_mfma_f32_16x16x32_bf16 v[92:95], v[108:111], v[216:219], v[92:95]\n\t" \
  "s_add_u32 s62, %[xslo], 114688\n\t" \
  "s_addc_u32 s63, %[xshi], 0\n\t" \
  "global_load_dwordx4 v[156:159], %[voff], s[62:63] offset:3072 sc1\n\t" \
  "s_add_u32 s62, %[xslo], 131072\n\t" \
  "s_addc_u32 s63, %[xshi], 0\n\t" \
  "global_load_dwordx4 v[212:215], %[voff], s[62:63] offset:1024 sc1\n\t" \
  "s_waitcnt lgkmcnt(6)\n\t" \
  "v_mfma_f32_16x16x32_bf16 v[32:35], v[112:115], v[164:167], v[32:35]\n\t" \
  "ds_read_b64_tr_b16 v[96:97], %[ad0] offset:49152\n\t" \
  "ds_read_b64_tr_b16 v[98:99], %[ad0] offset:49664\n\t" \
  "v_mfma_f32_16x16x32_bf16 v[64:67], v[112:115], v[220:223], v[64:67]\n\t" \
  "v_mfma_f32_16x16x32_bf16 v[36:39], v[116:119], v[164:167], v[36:39]\n\t" \
  "ds_read_b64_tr_b16 v[100:101], %[ad1] offset:49152\n\t" \
  "ds_read_b64_tr_b16 v[102:103], %[ad1] offset:49664\n\t" \
  "v_mfma_f32_16x16x32_bf16 v[68:71], v[116:119], v[220:223], v[68:71]\n\t" \
  "v_mfma_f32_16x16x32_bf16 v[40:43], v[120:123], v[164:167], v[40:43]\n\t" \
  "ds_read_b64_tr_b16 v[104:105], %[ad2] offset:49152\n\t" \
  "ds_read_b64_tr_b16 v[106:107], %[ad2] offset:49664\n\t" \
  "v_mfma_f32_16x16x32_bf16 v[72:75], v[120:123], v[220:223], v[72:75]\n\t" \
  "v_mfma_f32_16x16x32_bf16 v[44:47], v[124:127], v[164:167], v[44:47]\n\t" \
  "ds_read_b64_tr_b16 v[108:109], %[ad3] offset:49152\n\t" \
  "ds_read_b64_tr_b16 v[110:111], %[ad3] offset:49664\n\t" \
  "v_mfma_f32_16x16x32_bf16 v[76:79], v[124:127], v[220:223], v[76:79]\n\t" \
  "s_waitcnt lgkmcnt(6)\n\t" \
  "v_mfma_f32_16x16x32_bf16 v[48:51], v[128:131], v[164:167], v[48:51]\n\t" \
  "ds_read_b64_tr_b16 v[112:113], %[ad0] offset:57344\n\t" \
  "ds_read_b64_tr_b16 v[114:115], %[ad0] offset:57856\n\t" \
  "v_mfma_f32_16x16x32_bf16 v[80:83], v[128:131], v[220:223], v[80:83]\n\t" \
  "v_mfma_f32_16x16x32_bf16 v[52:55], v[132:135], v[164:167], v[52:55]\n\t" \
  "ds_read_b64_tr_b16 v[116:117], %[ad1] offset:57344\n\t" \
  "ds_read_b64_tr_b16 v[118:119], %[ad1] offset:57856\n\t" \
  "v_mfma_f32_16x16x32_bf16 v[84:87], v[132:135], v[220:223], v[84:87]\n\t" \
  "v_mfma_f32_16x16x32_bf16 v[56:59], v[136:139], v[164:167], v[56:59]\n\t" \
  "ds_read_b64_tr_b16 v[120:121], %[ad2] offset:57344\n\t" \
  "ds_read_b64_tr_b16 v[122:123], %[ad2] offset:57856\n\t" \
  "v_mfma_f32_16x16x32_bf16 v[88:91], v[136:139], v[220:223], v[88:91]\n\t" \
  "v_mfma_f32_16x16x32_bf16 v[60:63], v[140:143], v[164:167], v[60:63]\n\t" \
  "ds_read_b64_tr_b16 v[124:125], %[ad3] offset:57344\n\t" \
  "ds_read_b64_tr_b16 v[126:127], %[ad3] offset:57856\n\t" \
  "v_mfma_f32_16x16x32_bf16 v[92:95], v[140:143], v[220:223], v[92:95]\n\t" \
  "s_waitcnt vmcnt(20)\n\t" \
  "s_waitcnt lgkmcnt(6)\n\t" \
  "v_mfma_f32_16x16x32_bf16 v[32:35], v[96:99], v[168:171], v[32:35]\n\t" \
  "ds_read_b64_tr_b16 v[128:129], %[ad0] offset:53248\n\t" \
  "ds_read_b64_tr_b16 v[130:131], %[ad0] offset:53760\n\t" \
  "v_mfma_f32_16x16x32_bf16 v[64:67], v[96:99], v[224:227], v[64:67]\n\t" \
  "v_mfma_f32_16x16x32_bf16 v[36:39], v[100:103], v[168:171], v[36:39]\n\t" \
  "ds_read_b64_tr_b16 v[132:133], %[ad1] offset:53248\n\t" \
  "ds_read_b64_tr_b16 v[134:135], %[ad1] offset:53760\n\t" \
  "v_mfma_f32_16x16x32_bf16 v[68:71], v[100:103], v[224:227], v[68:71]\n\t" \
  "v_mfma_f32_16x16x32_bf16 v[40:43], v[104:107], v[168:171], v[40:43]\n\t" \
  "ds_read_b64_tr_b16 v[136:137], %[ad2] offset:53248\n\t" \
  "ds_read_b64_tr_b16 v[138:139], %[ad2] offset:53760\n\t" \
  "v_mfma_f32_16x16x32_bf16 v[72:75], v[104:107], v[224:227], v[72:75]\n\t" \
  "v_mfma_f32_16x16x32_bf16 v[44:47], v[108:111], v[168:171], v[44:47]\n\t" \
  "ds_read_b64_tr_b16 v[140:141], %[ad3] offset:53248\n\t" \
  "ds_read_b64_tr_b16 v[142:143], %[ad3] offset:53760\n\t" \
  "v_mfma_f32_16x16x32_bf16 v[76:79], v[108:111], v[224:227], v[76:79]\n\t" \
  "s_waitcnt lgkmcnt(6)\n\t" \
  "v_mfma_f32_16x16x32_bf16 v[48:51], v[112:115], v[168:171], v[48:51]\n\t" \
  "ds_read_b64_tr_b16 v[96:97], %[ad0] offset:61440\n\t" \
  "ds_read_b64_tr_b16 v[98:99], %[ad0] offset:61952\n\t" \
  "v_mfma_f32_16x16x32_bf16 v[80:83], v[112:115], v[224:227], v[80:83]\n\t" \
  "v_mfma_f32_16x16x32_bf16 v[52:55], v[116:119], v[168:171], v[52:55]\n\t" \
  "ds_read_b64_tr_b16 v[100:101], %[ad1] offset:61440\n\t" \
  "ds_read_b64_tr_b16 v[102:103], %[ad1] offset:61952\n\t" \
  "v_mfma_f32_16x16x32_bf16 v[84:87], v[116:119], v[224:227], v[84:87]\n\t" \
  "v_mfma_f32_16x16x32_bf16 v[56:59], v[120:123], v[168:171], v[56:59]\n\t" \
  "ds_read_b64_tr_b16 v[104:105], %[ad2] offset:61440\n\t" \
  "ds_read_b64_tr_b16 v[106:107], %[ad2] offset:61952\n\t" \
  "v_mfma_f32_16x16x32_bf16 v[88:91], v[120:123], v[224:227], v[88:91]\n\t" \
  "s_add_u32 s62, %[xslo], 118784\n\t" \
  "s_addc_u32 s63, %[xshi], 0\n\t" \
  "global_load_dwordx4 v[160:163], %[voff], s[62:63] offset:0 sc1\n\t" \
  "s_add_u32 s62, %[xslo], 131072\n\t" \
  "s_addc_u32 s63, %[xshi], 0\n\t" \
  "global_load_dwordx4 v[216:219], %[voff], s[62:63] offset:2048 sc1\n\t" \
  "v_mfma_f32_16x16x32_bf16 v[60:63], v[124:127], v[168:171], v[60:63]\n\t" \
  "ds_read_b64_tr_b16 v[108:109], %[ad3] offset:61440\n\t" \
  "ds_read_b64_tr_b16 v[110:111], %[ad3] offset:61952\n\t" \
  "v_mfma_f32_16x16x32_bf16 v[92:95], v[124:127], v[224:227], v[92:95]\n\t" \
  "s_add_u32 s62, %[xslo], 118784\n\t" \
  "s_addc_u32 s63, %[xshi], 0\n\t" \
  "global_load_dwordx4 v[164:167], %[voff], s[62:63] offset:1024 sc1\n\t" \
  "s_add_u32 s62, %[xslo], 131072\n\t" \
  "s_addc_u32 s63, %[xshi], 0\n\t" \
  "global_load_dwordx4 v[220:223], %[voff], s[62:63] offset:3072 sc1\n\t" \
  "s_waitcnt lgkmcnt(6)\n\t" \
  "v_add_u32 %[ad0], 0x10000, %[ad0]\n\t" \
  "v_add_u32 %[ad1], 0x10000, %[ad1]\n\t" \
  "v_add_u32 %[ad2], 0x10000, %[ad2]\n\t" \
  "v_add_u32 %[ad3], 0x10000, %[ad3]\n\t" \
  "v_mfma_f32_16x16x32_bf16 v[32:35], v[128:131], v[172:175], v[32:35]\n\t" \
  "ds_read_b64_tr_b16 v[112:113], %[ad0] offset:0\n\t" \
  "ds_read_b64_tr_b16 v[114:115], %[ad0] offset:512\n\t" \
  "v_mfma_f32_16x16x32_bf16 v[64:67], v[128:131], v[228:231], v[64:67]\n\t" \
  "v_mfma_f32_16x16x32_bf16 v[36:39], v[132:135], v[172:175], v[36:39]\n\t" \
  "ds_read_b64_tr_b16 v[116:117], %[ad1] offset:0\n\t" \
  "ds_read_b64_tr_b16 v[118:119], %[ad1] offset:512\n\t" \
  "v_mfma_f32_16x16x32_bf16 v[68:71], v[132:135], v[228:231], v[68:71]\n\t" \
  "v_mfma_f32_16x16x32_bf16 v[40:43], v[136:139], v[172:175], v[40:43]\n\t" \
  "ds_read_b64_tr_b16 v[120:121], %[ad2] offset:0\n\t" \
  "ds_read_b64_tr_b16 v[122:123], %[ad2] offset:512\n\t" \
  "v_mfma_f32_16x16x32_bf16 v[72:75], v[136:139], v[228:231], v[72:75]\n\t" \
  "v_mfma_f32_16x16x32_bf16 v[44:47], v[140:143], v[172:175], v[44:47]\n\t" \
  "ds_read_b64_tr_b16 v[124:125], %[ad3] offset:0\n\t" \
  "ds_read_b64_tr_b16 v[126:127], %[ad3] offset:512\n\t" \
  "v_mfma_f32_16x16x32_bf16 v[76:79], v[140:143], v[228:231], v[76:79]\n\t" \
  "s_waitcnt lgkmcnt(6)\n\t" \
  "v_mfma_f32_16x16x32_bf16 v[48:51], v[96:99], v[172:175], v[48:51]\n\t" \
  "ds_read_b64_tr_b16 v[128:129], %[ad0] offset:8192\n\t" \
  "ds_read_b64_tr_b16 v[130:131], %[ad0] offset:8704\n\t" \
  "v_mfma_f32_16x16x32_bf16 v[80:83], v[96:99], v[228:231], v[80:83]\n\t" \
  "v_mfma_f32_16x16x32_bf16 v[52:55], v[100:103], v[172:175], v[52:55]\n\t" \
  "ds_read_b64_tr_b16 v[132:133], %[ad1] offset:8192\n\t" \
  "ds_read_b64_tr_b16 v[134:135], %[ad1] offset:8704\n\t" \
  "v_mfma_f32_16x16x32_bf16 v[84:87], v[100:103], v[228:231], v[84:87]\n\t" \
  "v_mfma_f32_16x16x32_bf16 v[56:59], v[104:107], v[172:175], v[56:59]\n\t" \
  "ds_read_b64_tr_b16 v[136:137], %[ad2] offset:8192\n\t" \
  "ds_read_b64_tr_b16 v[138:139], %[ad2] offset:8704\n\t" \
  "v_mfma_f32_16x16x32_bf16 v[88:91], v[104:107], v[228:231], v[88:91]\n\t" \
  "v_mfma_f32_16x16x32_bf16 v[60:63], v[108:111], v[172:175], v[60:63]\n\t" \
  "ds_read_b64_tr_b16 v[140:141], %[ad3] offset:8192\n\t" \
  "ds_read_b64_tr_b16 v[142:143], %[ad3] offset:8704\n\t" \
  "v_mfma_f32_16x16x32_bf16 v[92:95], v[108:111], v[228:231], v[92:95]\n\t" \
  "s_waitcnt vmcnt(20)\n\t" \
  "s_waitcnt lgkmcnt(6)\n\t" \
  "v_mfma_f32_16x16x32_bf16 v[32:35], v[112:115], v[176:179], v[32:35]\n\t" \
  "ds_read_b64_tr_b16 v[96:97], %[ad0] offset:4096\n\t" \
  "ds_read_b64_tr_b16 v[98:99], %[ad0] offset:4608\n\t" \
  "v_mfma_f32_16x16x32_bf16 v[64:67], v[112:115], v[232:235], v[64:67]\n\t" \
  "v_mfma_f32_16x16x32_bf16 v[36:39], v[116:119], v[176:179], v[36:39]\n\t" \
  "ds_read_b64_tr_b16 v[100:101], %[ad1] offset:4096\n\t" \
  "ds_read_b64_tr_b16 v[102:103], %[ad1] offset:4608\n\t" \
  "v_mfma_f32_16x16x32_bf16 v[68:71], v[116:119], v[232:235], v[68:71]\n\t" \
  "v_mfma_f32_16x16x32_bf16 v[40:43], v[120:123], v[176:179], v[40:43]\n\t" \
  "ds_read_b64_tr_b16 v[104:105], %[ad2] offset:4096\n\t" \
  "ds_read_b64_tr_b16 v[106:107], %[ad2] offset:4608\n\t" \
  "v_mfma_f32_16x16x32_bf16 v[72:75], v[120:123], v[232:235], v[72:75]\n\t" \
  "v_mfma_f32_16x16x32_bf16 v[44:47], v[124:127], v[176:179], v[44:47]\n\t" \
  "ds_read_b64_tr_b16 v[108:109], %[ad3] offset:4096\n\t" \
  "ds_read_b64_tr_b16 v[110:111], %[ad3] offset:4608\n\t" \
  "v_mfma_f32_16x16x32_bf16 v[76:79], v[124:127], v[232:235], v[76:79]\n\t" \
  "s_waitcnt lgkmcnt(6)\n\t" \
  "v_mfma_f32_16x16x32_bf16 v[48:51], v[128:131], v[176:179], v[48:51]\n\t" \
  "ds_read_b64_tr_b16 v[112:113], %[ad0] offset:12288\n\t" \
  "ds_read_b64_tr_b16 v[114:115], %[ad0] offset:12800\n\t" \
  "v_mfma_f32_16x16x32_bf16 v[80:83], v[128:131], v[232:235], v[80:83]\n\t" \
  "v_mfma_f32_16x16x32_bf16 v[52:55], v[132:135], v[176:179], v[52:55]\n\t" \
  "ds_read_b64_tr_b16 v[116:117], %[ad1] offset:12288\n\t" \
  "ds_read_b64_tr_b16 v[118:119], %[ad1] offset:12800\n\t" \
  "v_mfma_f32_16x16x32_bf16 v[84:87], v[132:135], v[232:235], v[84:87]\n\t" \
  "v_mfma_f32_16x16x32_bf16 v[56:59], v[136:139], v[176:179], v[56:59]\n\t" \
  "ds_read_b64_tr_b16 v[120:121], %[ad2] offset:12288\n\t" \
  "ds_read_b64_tr_b16 v[122:123], %[ad2] offset:12800\n\t" \
  "v_mfma_f32_16x16x32_bf16 v[88:91], v[136:139], v[232:235], v[88:91]\n\t" \
  "s_add_u32 s62, %[xslo], 118784\n\t" \
  "s_addc_u32 s63, %[xshi], 0\n\t" \
  "global_load_dwordx4 v[168:171], %[voff], s[62:63] offset:2048 sc1\n\t" \
  "s_add_u32 s62, %[xslo], 135168\n\t" \
  "s_addc_u32 s63, %[xshi], 0\n\t" \
  "global_load_dwordx4 v[224:227], %[voff], s[62:63] offset:0 sc1\n\t" \
  "v_mfma_f32_16x16x32_bf16 v[60:63], v[140:143], v[176:179], v[60:63]\n\t" \
  "ds_read_b64_tr_b16 v[124:125], %[ad3] offset:12288\n\t" \
  "ds_read_b64_tr_b16 v[126:127], %[ad3] offset:12800\n\t" \
  "v_mfma_f32_16x16x32_bf16 v[92:95], v[140:143], v[232:235], v[92:95]\n\t" \
  "s_add_u32 s62, %[xslo], 118784\n\t" \
  "s_addc_u32 s63, %[xshi], 0\n\t" \
  "global_load_dwordx4 v[172:175], %[voff], s[62:63] offset:3072 sc1\n\t" \
  "s_add_u32 s62, %[xslo], 135168\n\t" \
  "s_addc_u32 s63, %[xshi], 0\n\t" \
  "global_load_dwordx4 v[228:231], %[voff], s[62:63] offset:1024 sc1\n\t" \
  "s_waitcnt lgkmcnt(6)\n\t" \
  "v_mfma_f32_16x16x32_bf16 v[32:35], v[96:99], v[180:183], v[32:35]\n\t" \
  "ds_read_b64_tr_b16 v[128:129], %[ad0] offset:16384\n\t" \
  "ds_read_b64_tr_b16 v[130:131], %[ad0] offset:16896\n\t" \
  "v_mfma_f32_16x16x32_bf16 v[64:67], v[96:99], v[236:239], v[64:67]\n\t" \
  "v_mfma_f32_16x16x32_bf16 v[36:39], v[100:103], v[180:183], v[36:39]\n\t" \
  "ds_read_b64_tr_b16 v[132:133], %[ad1] offset:16384\n\t" \
  "ds_read_b64_tr_b16 v[134:135], %[ad1] offset:16896\n\t" \
  "v_mfma_f32_16x16x32_bf16 v[68:71], v[100:103], v[236:239], v[68:71]\n\t" \
  "v_mfma_f32_16x16x32_bf16 v[40:43], v[104:107], v[180:183], v[40:43]\n\t" \
  "ds_read_b64_tr_b16 v[136:137], %[ad2] offset:16384\n\t" \
  "ds_read_b64_tr_b16 v[138:139], %[ad2] offset:16896\n\t" \
  "v_mfma_f32_16x16x32_bf16 v[72:75], v[104:107], v[236:239], v[72:75]\n\t" \
  "v_mfma_f32_16x16x32_bf16 v[44:47], v[108:111], v[180:183], v[44:47]\n\t" \
  "ds_read_b64_tr_b16 v[140:141], %[ad3] offset:16384\n\t" \
  "ds_read_b64_tr_b16 v[142:143], %[ad3] offset:16896\n\t" \
  "v_mfma_f32_16x16x32_bf16 v[76:79], v[108:111], v[236:239], v[76:79]\n\t" \
  "s_waitcnt lgkmcnt(6)\n\t" \
  "v_mfma_f32_16x16x32_bf16 v[48:51], v[112:115], v[180:183], v[48:51]\n\t" \
  "ds_read_b64_tr_b16 v[96:97], %[ad0] offset:24576\n\t" \
  "ds_read_b64_tr_b16 v[98:99], %[ad0] offset:25088\n\t" \
  "v_mfma_f32_16x16x32_bf16 v[80:83], v[112:115], v[236:239], v[80:83]\n\t" \
  "v_mfma_f32_16x16x32_bf16 v[52:55], v[116:119], v[180:183], v[52:55]\n\t" \
  "ds_read_b64_tr_b16 v[100:101], %[ad1] offset:24576\n\t" \
  "ds_read_b64_tr_b16 v[102:103], %[ad1] offset:25088\n\t" \
  "v_mfma_f32_16x16x32_bf16 v[84:87], v[116:119], v[236:239], v[84:87]\n\t" \
  "v_mfma_f32_16x16x32_bf16 v[56:59], v[120:123], v[180:183], v[56:59]\n\t" \
  "ds_read_b64_tr_b16 v[104:105], %[ad2] offset:24576\n\t" \
  "ds_read_b64_tr_b16 v[106:107], %[ad2] offset:25088\n\t" \
  "v_mfma_f32_16x16x32_bf16 v[88:91], v[120:123], v[236:239], v[88:91]\n\t" \
  "v_mfma_f32_16x16x32_bf16 v[60:63], v[124:127], v[180:183], v[60:63]\n\t" \
  "ds_read_b64_tr_b16 v[108:109], %[ad3] offset:24576\n\t" \
  "ds_read_b64_tr_b16 v[110:111], %[ad3] offset:25088\n\t" \
  "v_mfma_f32_16x16x32_bf16 v[92:95], v[124:127], v[236:239], v[92:95]\n\t" \
  "s_waitcnt vmcnt(20)\n\t" \
  "s_waitcnt lgkmcnt(6)\n\t" \
  "v_mfma_f32_16x16x32_bf16 v[32:35], v[128:131], v[184:187], v[32:35]\n\t" \
  "ds_read_b64_tr_b16 v[112:113], %[ad0] offset:20480\n\t" \
  "ds_read_b64_tr_b16 v[114:115], %[ad0] offset:20992\n\t" \
  "v_mfma_f32_16x16x32_bf16 v[64:67], v[128:131], v[240:243], v[64:67]\n\t" \
  "v_mfma_f32_16x16x32_bf16 v[36:39], v[132:135], v[184:187], v[36:39]\n\t" \
  "ds_read_b64_tr_b16 v[116:117], %[ad1] offset:20480\n\t" \
  "ds_read_b64_tr_b16 v[118:119], %[ad1] offset:20992\n\t" \
  "v_mfma_f32_16x16x32_bf16 v[68:71], v[132:135], v[240:243], v[68:71]\n\t" \
  "v_mfma_f32_16x16x32_bf16 v[40:43], v[136:139], v[184:187], v[40:43]\n\t" \
  "ds_read_b64_tr_b16 v[120:121], %[ad2] offset:20480\n\t" \
  "ds_read_b64_tr_b16 v[122:123], %[ad2] offset:20992\n\t" \
  "v_mfma_f32_16x16x32_bf16 v[72:75], v[136:139], v[240:243], v[72:75]\n\t" \
  "v_mfma_f32_16x16x32_bf16 v[44:47], v[140:143], v[184:187], v[44:47]\n\t" \
  "ds_read_b64_tr_b16 v[124:125], %[ad3] offset:20480\n\t" \
  "ds_read_b64_tr_b16 v[126:127], %[ad3] offset:20992\n\t" \
  "v_mfma_f32_16x16x32_bf16 v[76:79], v[140:143], v[240:243], v[76:79]\n\t" \
  "s_waitcnt lgkmcnt(6)\n\t" \
  "v_mfma_f32_16x16x32_bf16 v[48:51], v[96:99], v[184:187], v[48:51]\n\t" \
  "ds_read_b64_tr_b16 v[128:129], %[ad0] offset:28672\n\t" \
  "ds_read_b64_tr_b16 v[130:131], %[ad0] offset:29184\n\t" \
  "v_mfma_f32_16x16x32_bf16 v[80:83], v[96:99], v[240:243], v[80:83]\n\t" \
  "v_mfma_f32_16x16x32_bf16 v[52:55], v[100:103], v[184:187], v[52:55]\n\t" \
  "ds_read_b64_tr_b16 v[132:133], %[ad1] offset:28672\n\t" \
  "ds_read_b64_tr_b16 v[134:135], %[ad1] offset:29184\n\t" \
  "v_mfma_f32_16x16x32_bf16 v[84:87], v[100:103], v[240:243], v[84:87]\n\t" \
  "v_mfma_f32_16x16x32_bf16 v[56:59], v[104:107], v[184:187], v[56:59]\n\t" \
  "ds_read_b64_tr_b16 v[136:137], %[ad2] offset:28672\n\t" \
  "ds_read_b64_tr_b16 v[138:139], %[ad2] offset:29184\n\t" \
  "v_mfma_f32_16x16x32_bf16 v[88:91], v[104:107], v[240:243], v[88:91]\n\t" \
  "s_add_u32 s62, %[xslo], 122880\n\t" \
  "s_addc_u32 s63, %[xshi], 0\n\t" \
  "global_load_dwordx4 v[176:179], %[voff], s[62:63] offset:0 sc1\n\t" \
  "s_add_u32 s62, %[xslo], 135168\n\t" \
  "s_addc_u32 s63, %[xshi], 0\n\t" \
  "global_load_dwordx4 v[232:235], %[voff], s[62:63] offset:2048 sc1\n\t" \
  "v_mfma_f32_16x16x32_bf16 v[60:63], v[108:111], v[184:187], v[60:63]\n\t" \
  "ds_read_b64_tr_b16 v[140:141], %[ad3] offset:28672\n\t" \
  "ds_read_b64_tr_b16 v[142:143], %[ad3] offset:29184\n\t" \
  "v_mfma_f32_16x16x32_bf16 v[92:95], v[108:111], v[240:243], v[92:95]\n\t" \
  "s_add_u32 s62, %[xslo], 122880\n\t" \
  "s_addc_u32 s63, %[xshi], 0\n\t" \
  "global_load_dwordx4 v[180:183], %[voff], s[62:63] offset:1024 sc1\n\t" \
  "s_add_u32 s62, %[xslo], 135168\n\t" \
  "s_addc_u32 s63, %[xshi], 0\n\t" \
  "global_load_dwordx4 v[236:239], %[voff], s[62:63] offset:3072 sc1\n\t" \
  "s_waitcnt lgkmcnt(6)\n\t" \
  "v_mfma_f32_16x16x32_bf16 v[32:35], v[112:115], v[188:191], v[32:35]\n\t" \
  "ds_read_b64_tr_b16 v[96:97], %[ad0] offset:32768\n\t" \
  "ds_read_b64_tr_b16 v[98:99], %[ad0] offset:33280\n\t" \
  "v_mfma_f32_16x16x32_bf16 v[64:67], v[112:115], v[244:247], v[64:67]\n\t" \
  "v_mfma_f32_16x16x32_bf16 v[36:39], v[116:119], v[188:191], v[36:39]\n\t" \
  "ds_read_b64_tr_b16 v[100:101], %[ad1] offset:32768\n\t" \
  "ds_read_b64_tr_b16 v[102:103], %[ad1] offset:33280\n\t" \
  "v_mfma_f32_16x16x32_bf16 v[68:71], v[116:119], v[244:247], v[68:71]\n\t" \
  "v_mfma_f32_16x16x32_bf16 v[40:43], v[120:123], v[188:191], v[40:43]\n\t" \
  "ds_read_b64_tr_b16 v[104:105], %[ad2] offset:32768\n\t" \
  "ds_read_b64_tr_b16 v[106:107], %[ad2] offset:33280\n\t" \
  "v_mfma_f32_16x16x32_bf16 v[72:75], v[120:123], v[244:247], v[72:75]\n\t" \
  "v_mfma_f32_16x16x32_bf16 v[44:47], v[124:127], v[188:191], v[44:47]\n\t" \
  "ds_read_b64_tr_b16 v[108:109], %[ad3] offset:32768\n\t" \
  "ds_read_b64_tr_b16 v[110:111], %[ad3] offset:33280\n\t" \
  "v_mfma_f32_16x16x32_bf16 v[76:79], v[124:127], v[244:247], v[76:79]\n\t" \
  "s_waitcnt lgkmcnt(6)\n\t" \
  "v_mfma_f32_16x16x32_bf16 v[48:51], v[128:131], v[188:191], v[48:51]\n\t" \
  "ds_read_b64_tr_b16 v[112:113], %[ad0] offset:40960\n\t" \
  "ds_read_b64_tr_b16 v[114:115], %[ad0] offset:41472\n\t" \
  "v_mfma_f32_16x16x32_bf16 v[80:83], v[128:131], v[244:247], v[80:83]\n\t" \
  "v_mfma_f32_16x16x32_bf16 v[52:55], v[132:135], v[188:191], v[52:55]\n\t" \
  "ds_read_b64_tr_b16 v[116:117], %[ad1] offset:40960\n\t" \
  "ds_read_b64_tr_b16 v[118:119], %[ad1] offset:41472\n\t" \
  "v_mfma_f32_16x16x32_bf16 v[84:87], v[132:135], v[244:247], v[84:87]\n\t" \
  "v_mfma_f32_16x16x32_bf16 v[56:59], v[136:139], v[188:191], v[56:59]\n\t" \
  "ds_read_b64_tr_b16 v[120:121], %[ad2] offset:40960\n\t" \
  "ds_read_b64_tr_b16 v[122:123], %[ad2] offset:41472\n\t" \
  "v_mfma_f32_16x16x32_bf16 v[88:91], v[136:139], v[244:247], v[88:91]\n\t" \
  "v_mfma_f32_16x16x32_bf16 v[60:63], v[140:143], v[188:191], v[60:63]\n\t" \
  "ds_read_b64_tr_b16 v[124:125], %[ad3] offset:40960\n\t" \
  "ds_read_b64_tr_b16 v[126:127], %[ad3] offset:41472\n\t" \
  "v_mfma_f32_16x16x32_bf16 v[92:95], v[140:143], v[244:247], v[92:95]\n\t" \
  "s_waitcnt vmcnt(20)\n\t" \
  "s_waitcnt lgkmcnt(6)\n\t" \
  "v_mfma_f32_16x16x32_bf16 v[32:35], v[96:99], v[192:195], v[32:35]\n\t" \
  "ds_read_b64_tr_b16 v[128:129], %[ad0] offset:36864\n\t" \
  "ds_read_b64_tr_b16 v[130:131], %[ad0] offset:37376\n\t" \
  "v_mfma_f32_16x16x32_bf16 v[64:67], v[96:99], v[248:251], v[64:67]\n\t" \
  "v_mfma_f32_16x16x32_bf16 v[36:39], v[100:103], v[192:195], v[36:39]\n\t" \
  "ds_read_b64_tr_b16 v[132:133], %[ad1] offset:36864\n\t" \
  "ds_read_b64_tr_b16 v[134:135], %[ad1] offset:37376\n\t" \
  "v_mfma_f32_16x16x32_bf16 v[68:71], v[100:103], v[248:251], v[68:71]\n\t" \
  "v_mfma_f32_16x16x32_bf16 v[40:43], v[104:107], v[192:195], v[40:43]\n\t" \
  "ds_read_b64_tr_b16 v[136:137], %[ad2] offset:36864\n\t" \
  "ds_read_b64_tr_b16 v[138:139], %[ad2] offset:37376\n\t" \
  "v_mfma_f32_16x16x32_bf16 v[72:75], v[104:107], v[248:251], v[72:75]\n\t" \
  "v_mfma_f32_16x16x32_bf16 v[44:47], v[108:111], v[192:195], v[44:47]\n\t" \
  "ds_read_b64_tr_b16 v[140:141], %[ad3] offset:36864\n\t" \
  "ds_read_b64_tr_b16 v[142:143], %[ad3] offset:37376\n\t" \
  "v_mfma_f32_16x16x32_bf16 v[76:79], v[108:111], v[248:251], v[76:79]\n\t" \
  "s_waitcnt lgkmcnt(6)\n\t" \
  "v_mfma_f32_16x16x32_bf16 v[48:51], v[112:115], v[192:195], v[48:51]\n\t" \
  "ds_read_b64_tr_b16 v[96:97], %[ad0] offset:45056\n\t" \
  "ds_read_b64_tr_b16 v[98:99], %[ad0] offset:45568\n\t" \
  "v_mfma_f32_16x16x32_bf16 v[80:83], v[112:115], v[248:251], v[80:83]\n\t" \
  "v_mfma_f32_16x16x32_bf16 v[52:55], v[116:119], v[192:195], v[52:55]\n\t" \
  "ds_read_b64_tr_b16 v[100:101], %[ad1] offset:45056\n\t" \
  "ds_read_b64_tr_b16 v[102:103], %[ad1] offset:45568\n\t" \
  "v_mfma_f32_16x16x32_bf16 v[84:87], v[116:119], v[248:251], v[84:87]\n\t" \
  "v_mfma_f32_16x16x32_bf16 v[56:59], v[120:123], v[192:195], v[56:59]\n\t" \
  "ds_read_b64_tr_b16 v[104:105], %[ad2] offset:45056\n\t" \
  "ds_read_b64_tr_b16 v[106:107], %[ad2] offset:45568\n\t" \
  "v_mfma_f32_16x16x32_bf16 v[88:91], v[120:123], v[248:251], v[88:91]\n\t" \
  "s_add_u32 s62, %[xslo], 122880\n\t" \
  "s_addc_u32 s63, %[xshi], 0\n\t" \
  "global_load_dwordx4 v[184:187], %[voff], s[62:63] offset:2048 sc1\n\t" \
  "s_add_u32 s62, %[xslo], 139264\n\t" \
  "s_addc_u32 s63, %[xshi], 0\n\t" \
  "global_load_dwordx4 v[240:243], %[voff], s[62:63] offset:0 sc1\n\t" \
  "v_mfma_f32_16x16x32_bf16 v[60:63], v[124:127], v[192:195], v[60:63]\n\t" \
  "ds_read_b64_tr_b16 v[108:109], %[ad3] offset:45056\n\t" \
  "ds_read_b64_tr_b16 v[110:111], %[ad3] offset:45568\n\t" \
  "v_mfma_f32_16x16x32_bf16 v[92:95], v[124:127], v[248:251], v[92:95]\n\t" \
  "s_add_u32 s62, %[xslo], 122880\n\t" \
  "s_addc_u32 s63, %[xshi], 0\n\t" \
  "global_load_dwordx4 v[188:191], %[voff], s[62:63] offset:3072 sc1\n\t" \
  "s_add_u32 s62, %[xslo], 139264\n\t" \
  "s_addc_u32 s63, %[xshi], 0\n\t" \
  "global_load_dwordx4 v[244:247], %[voff], s[62:63] offset:1024 sc1\n\t" \
  "s_waitcnt lgkmcnt(8)\n\t" \
  "v_mfma_f32_16x16x32_bf16 v[32:35], v[128:131], v[196:199], v[32:35]\n\t" \
  "v_mfma_f32_16x16x32_bf16 v[64:67], v[128:131], v[252:255], v[64:67]\n\t" \
  "v_mfma_f32_16x16x32_bf16 v[36:39], v[132:135], v[196:199], v[36:39]\n\t" \
  "v_mfma_f32_16x16x32_bf16 v[68:71], v[132:135], v[252:255], v[68:71]\n\t" \
  "v_mfma_f32_16x16x32_bf16 v[40:43], v[136:139], v[196:199], v[40:43]\n\t" \
  "v_mfma_f32_16x16x32_bf16 v[72:75], v[136:139], v[252:255], v[72:75]\n\t" \
  "v_mfma_f32_16x16x32_bf16 v[44:47], v[140:143], v[196:199], v[44:47]\n\t" \
  "v_mfma_f32_16x16x32_bf16 v[76:79], v[140:143], v[252:255], v[76:79]\n\t" \
  "s_waitcnt lgkmcnt(0)\n\t" \
  "v_mfma_f32_16x16x32_bf16 v[48:51], v[96:99], v[196:199], v[48:51]\n\t" \
  "v_mfma_f32_16x16x32_bf16 v[80:83], v[96:99], v[252:255], v[80:83]\n\t" \
  "v_mfma_f32_16x16x32_bf16 v[52:55], v[100:103], v[196:199], v[52:55]\n\t" \
  "v_mfma_f32_16x16x32_bf16 v[84:87], v[100:103], v[252:255], v[84:87]\n\t" \
  "v_mfma_f32_16x16x32_bf16 v[56:59], v[104:107], v[196:199], v[56:59]\n\t" \
  "v_mfma_f32_16x16x32_bf16 v[88:91], v[104:107], v[252:255], v[88:91]\n\t" \
  "v_mfma_f32_16x16x32_bf16 v[60:63], v[108:111], v[196:199], v[60:63]\n\t" \
  "v_mfma_f32_16x16x32_bf16 v[92:95], v[108:111], v[252:255], v[92:95]\n\t" \
  "v_subrev_u32 %[ad0], 0x10000, %[ad0]\n\t" \
  "v_subrev_u32 %[ad1], 0x10000, %[ad1]\n\t" \
  "v_subrev_u32 %[ad2], 0x10000, %[ad2]\n\t" \
  "v_subrev_u32 %[ad3], 0x10000, %[ad3]\n\t" \
  "s_nop 7\n\t" \
  "s_nop 7\n\t" \
  "s_add_u32 s62, %[xslo], 126976\n\t" \
  "s_addc_u32 s63, %[xshi], 0\n\t" \
  "global_load_dwordx4 v[192:195], %[voff], s[62:63] offset:0 sc1\n\t" \
  "s_add_u32 s62, %[xslo], 139264\n\t" \
  "s_addc_u32 s63, %[xshi], 0\n\t" \
  "global_load_dwordx4 v[248:251], %[voff], s[62:63] offset:2048 sc1\n\t" \
  "s_add_u32 s62, %[xslo], 126976\n\t" \
  "s_addc_u32 s63, %[xshi], 0\n\t" \
  "global_load_dwordx4 v[196:199], %[voff], s[62:63] offset:1024 sc1\n\t" \
  "s_add_u32 s62, %[xslo], 139264\n\t" \
  "s_addc_u32 s63, %[xshi], 0\n\t" \
  "global_load_dwordx4 v[252:255], %[voff], s[62:63] offset:3072 sc1\n\t"

#define K12_ROUND1_ASM \
  "ds_read_b64_tr_b16 v[96:97], %[ad0] offset:0\n\t" \
  "ds_read_b64_tr_b16 v[98:99], %[ad0] offset:512\n\t" \
  "ds_read_b64_tr_b16 v[100:101], %[ad1] offset:0\n\t" \
  "ds_read_b64_tr_b16 v[102:103], %[ad1] offset:512\n\t" \
  "ds_read_b64_tr_b16 v[104:105], %[ad2] offset:0\n\t" \
  "ds_read_b64_tr_b16 v[106:107], %[ad2] offset:512\n\t" \
  "ds_read_b64_tr_b16 v[108:109], %[ad3] offset:0\n\t" \
  "ds_read_b64_tr_b16 v[110:111], %[ad3] offset:512\n\t" \
  "s_waitcnt lgkmcnt(4)\n\t" \
  "ds_read_b64_tr_b16 v[112:113], %[ad0] offset:8192\n\t" \
  "ds_read_b64_tr_b16 v[114:115], %[ad0] offset:8704\n\t" \
  "ds_read_b64_tr_b16 v[116:117], %[ad1] offset:8192\n\t" \
  "ds_read_b64_tr_b16 v[118:119], %[ad1] offset:8704\n\t" \
  "ds_read_b64_tr_b16 v[120:121], %[ad2] offset:8192\n\t" \
  "ds_read_b64_tr_b16 v[122:123], %[ad2] offset:8704\n\t" \
  "ds_read_b64_tr_b16 v[124:125], %[ad3] offset:8192\n\t" \
  "ds_read_b64_tr_b16 v[126:127], %[ad3] offset:8704\n\t" \
  "s_waitcnt lgkmcnt(6)\n\t" \
  "v_mfma_f32_16x16x32_bf16 v[32:35], v[96:99], v[144:147], 0\n\t" \
  "ds_read_b64_tr_b16 v[128:129], %[ad0] offset:4096\n\t" \
  "ds_read_b64_tr_b16 v[130:131], %[ad0] offset:4608\n\t" \
  "v_mfma_f32_16x16x32_bf16 v[64:67], v[96:99], v[200:203], 0\n\t" \
  "v_mfma_f32_16x16x32_bf16 v[36:39], v[100:103], v[144:147], 0\n\t" \
  "ds_read_b64_tr_b16 v[132:133], %[ad1] offset:4096\n\t" \
  "ds_read_b64_tr_b16 v[134:135], %[ad1] offset:4608\n\t" \
  "v_mfma_f32_16x16x32_bf16 v[68:71], v[100:103], v[200:203], 0\n\t" \
  "v_mfma_f32_16x16x32_bf16 v[40:43], v[104:107], v[144:147], 0\n\t" \
  "ds_read_b64_tr_b16 v[136:137], %[ad2] offset:4096\n\t" \
  "ds_read_b64_tr_b16 v[138:139], %[ad2] offset:4608\n\t" \
  "v_mfma_f32_16x16x32_bf16 v[72:75], v[104:107], v[200:203], 0\n\t" \
  "v_mfma_f32_16x16x32_bf16 v[44:47], v[108:111], v[144:147], 0\n\t" \
  "ds_read_b64_tr_b16 v[140:141], %[ad3] offset:4096\n\t" \
  "ds_read_b64_tr_b16 v[142:143], %[ad3] offset:4608\n\t" \
  "v_mfma_f32_16x16x32_bf16 v[76:79], v[108:111], v[200:203], 0\n\t" \
  "s_waitcnt lgkmcnt(6)\n\t" \
  "v_mfma_f32_16x16x32_bf16 v[48:51], v[112:115], v[144:147], 0\n\t" \
  "ds_read_b64_tr_b16 v[96:97], %[ad0] offset:12288\n\t" \
  "ds_read_b64_tr_b16 v[98:99], %[ad0] offset:12800\n\t" \
  "v_mfma_f32_16x16x32_bf16 v[80:83], v[112:115], v[200:203], 0\n\t" \
  "v_mfma_f32_16x16x32_bf16 v[52:55], v[116:119], v[144:147], 0\n\t" \
  "ds_read_b64_tr_b16 v[100:101], %[ad1] offset:12288\n\t" \
  "ds_read_b64_tr_b16 v[102:103], %[ad1] offset:12800\n\t" \
  "v_mfma_f32_16x16x32_bf16 v[84:87], v[116:119], v[200:203], 0\n\t" \
  "v_mfma_f32_16x16x32_bf16 v[56:59], v[120:123], v[144:147], 0\n\t" \
  "ds_read_b64_tr_b16 v[104:105], %[ad2] offset:12288\n\t" \
  "ds_read_b64_tr_b16 v[106:107], %[ad2] offset:12800\n\t" \
  "v_mfma_f32_16x16x32_bf16 v[88:91], v[120:123], v[200:203], 0\n\t" \
  "v_mfma_f32_16x16x32_bf16 v[60:63], v[124:127], v[144:147], 0\n\t" \
  "ds_read_b64_tr_b16 v[108:109], %[ad3] offset:12288\n\t" \
  "ds_read_b64_tr_b16 v[110:111], %[ad3] offset:12800\n\t" \
  "v_mfma_f32_16x16x32_bf16 v[92:95], v[124:127], v[200:203], 0\n\t" \
  "s_waitcnt lgkmcnt(6)\n\t" \
  "v_mfma_f32_16x16x32_bf16 v[32:35], v[128:131], v[148:151], v[32:35]\n\t" \
  "ds_read_b64_tr_b16 v[112:113], %[ad0] offset:16384\n\t" \
  "ds_read_b64_tr_b16 v[114:115], %[ad0] offset:16896\n\t" \
  "v_mfma_f32_16x16x32_bf16 v[64:67], v[128:131], v[204:207], v[64:67]\n\t" \
  "v_mfma_f32_16x16x32_bf16 v[36:39], v[132:135], v[148:151], v[36:39]\n\t" \
  "ds_read_b64_tr_b16 v[116:117], %[ad1] offset:16384\n\t" \
  "ds_read_b64_tr_b16 v[118:119], %[ad1] offset:16896\n\t" \
  "v_mfma_f32_16x16x32_bf16 v[68:71], v[132:135], v[204:207], v[68:71]\n\t" \
  "v_mfma_f32_16x16x32_bf16 v[40:43], v[136:139], v[148:151], v[40:43]\n\t" \
  "ds_read_b64_tr_b16 v[120:121], %[ad2] offset:16384\n\t" \
  "ds_read_b64_tr_b16 v[122:123], %[ad2] offset:16896\n\t" \
  "v_mfma_f32_16x16x32_bf16 v[72:75], v[136:139], v[204:207], v[72:75]\n\t" \
  "v_mfma_f32_16x16x32_bf16 v[44:47], v[140:143], v[148:151], v[44:47]\n\t" \
  "ds_read_b64_tr_b16 v[124:125], %[ad3] offset:16384\n\t" \
  "ds_read_b64_tr_b16 v[126:127], %[ad3] offset:16896\n\t" \
  "v_mfma_f32_16x16x32_bf16 v[76:79], v[140:143], v[204:207], v[76:79]\n\t" \
  "s_waitcnt lgkmcnt(6)\n\t" \
  "v_mfma_f32_16x16x32_bf16 v[48:51], v[96:99], v[148:151], v[48:51]\n\t" \
  "ds_read_b64_tr_b16 v[128:129], %[ad0] offset:24576\n\t" \
  "ds_read_b64_tr_b16 v[130:131], %[ad0] offset:25088\n\t" \
  "v_mfma_f32_16x16x32_bf16 v[80:83], v[96:99], v[204:207], v[80:83]\n\t" \
  "v_mfma_f32_16x16x32_bf16 v[52:55], v[100:103], v[148:151], v[52:55]\n\t" \
  "ds_read_b64_tr_b16 v[132:133], %[ad1] offset:24576\n\t" \
  "ds_read_b64_tr_b16 v[134:135], %[ad1] offset:25088\n\t" \
  "v_mfma_f32_16x16x32_bf16 v[84:87], v[100:103], v[204:207], v[84:87]\n\t" \
  "v_mfma_f32_16x16x32_bf16 v[56:59], v[104:107], v[148:151], v[56:59]\n\t" \
  "ds_read_b64_tr_b16 v[136:137], %[ad2] offset:24576\n\t" \
  "ds_read_b64_tr_b16 v[138:139], %[ad2] offset:25088\n\t" \
  "v_mfma_f32_16x16x32_bf16 v[88:91], v[104:107], v[204:207], v[88:91]\n\t" \
  "v_mfma_f32_16x16x32_bf16 v[60:63], v[108:111], v[148:151], v[60:63]\n\t" \
  "ds_read_b64_tr_b16 v[140:141], %[ad3] offset:24576\n\t" \
  "ds_read_b64_tr_b16 v[142:143], %[ad3] offset:25088\n\t" \
  "v_mfma_f32_16x16x32_bf16 v[92:95], v[108:111], v[204:207], v[92:95]\n\t" \
  "s_waitcnt lgkmcnt(6)\n\t" \
  "v_mfma_f32_16x16x32_bf16 v[32:35], v[112:115], v[152:155], v[32:35]\n\t" \
  "ds_read_b64_tr_b16 v[96:97], %[ad0] offset:20480\n\t" \
  "ds_read_b64_tr_b16 v[98:99], %[ad0] offset:20992\n\t" \
  "v_mfma_f32_16x16x32_bf16 v[64:67], v[112:115], v[208:211], v[64:67]\n\t" \
  "v_mfma_f32_16x16x32_bf16 v[36:39], v[116:119], v[152:155], v[36:39]\n\t" \
  "ds_read_b64_tr_b16 v[100:101], %[ad1] offset:20480\n\t" \
  "ds_read_b64_tr_b16 v[102:103], %[ad1] offset:20992\n\t" \
  "v_mfma_f32_16x16x32_bf16 v[68:71], v[116:119], v[208:211], v[68:71]\n\t" \
  "v_mfma_f32_16x16x32_bf16 v[40:43], v[120:123], v[152:155], v[40:43]\n\t" \
  "ds_read_b64_tr_b16 v[104:105], %[ad2] offset:20480\n\t" \
  "ds_read_b64_tr_b16 v[106:107], %[ad2] offset:20992\n\t" \
  "v_mfma_f32_16x16x32_bf16 v[72:75], v[120:123], v[208:211], v[72:75]\n\t" \
  "v_mfma_f32_16x16x32_bf16 v[44:47], v[124:127], v[152:155], v[44:47]\n\t" \
  "ds_read_b64_tr_b16 v[108:109], %[ad3] offset:20480\n\t" \
  "ds_read_b64_tr_b16 v[110:111], %[ad3] offset:20992\n\t" \
  "v_mfma_f32_16x16x32_bf16 v[76:79], v[124:127], v[208:211], v[76:79]\n\t" \
  "s_waitcnt lgkmcnt(6)\n\t" \
  "v_mfma_f32_16x16x32_bf16 v[48:51], v[128:131], v[152:155], v[48:51]\n\t" \
  "ds_read_b64_tr_b16 v[112:113], %[ad0] offset:28672\n\t" \
  "ds_read_b64_tr_b16 v[114:115], %[ad0] offset:29184\n\t" \
  "v_mfma_f32_16x16x32_bf16 v[80:83], v[128:131], v[208:211], v[80:83]\n\t" \
  "v_mfma_f32_16x16x32_bf16 v[52:55], v[132:135], v[152:155], v[52:55]\n\t" \
  "ds_read_b64_tr_b16 v[116:117], %[ad1] offset:28672\n\t" \
  "ds_read_b64_tr_b16 v[118:119], %[ad1] offset:29184\n\t" \
  "v_mfma_f32_16x16x32_bf16 v[84:87], v[132:135], v[208:211], v[84:87]\n\t" \
  "v_mfma_f32_16x16x32_bf16 v[56:59], v[136:139], v[152:155], v[56:59]\n\t" \
  "ds_read_b64_tr_b16 v[120:121], %[ad2] offset:28672\n\t" \
  "ds_read_b64_tr_b16 v[122:123], %[ad2] offset:29184\n\t" \
  "v_mfma_f32_16x16x32_bf16 v[88:91], v[136:139], v[208:211], v[88:91]\n\t" \
  "v_mfma_f32_16x16x32_bf16 v[60:63], v[140:143], v[152:155], v[60:63]\n\t" \
  "ds_read_b64_tr_b16 v[124:125], %[ad3] offset:28672\n\t" \
  "ds_read_b64_tr_b16 v[126:127], %[ad3] offset:29184\n\t" \
  "v_mfma_f32_16x16x32_bf16 v[92:95], v[140:143], v[208:211], v[92:95]\n\t" \
  "s_waitcnt lgkmcnt(6)\n\t" \
  "v_mfma_f32_16x16x32_bf16 v[32:35], v[96:99], v[156:159], v[32:35]\n\t" \
  "ds_read_b64_tr_b16 v[128:129], %[ad0] offset:32768\n\t" \
  "ds_read_b64_tr_b16 v[130:131], %[ad0] offset:33280\n\t" \
  "v_mfma_f32_16x16x32_bf16 v[64:67], v[96:99], v[212:215], v[64:67]\n\t" \
  "v_mfma_f32_16x16x32_bf16 v[36:39], v[100:103], v[156:159], v[36:39]\n\t" \
  "ds_read_b64_tr_b16 v[132:133], %[ad1] offset:32768\n\t" \
  "ds_read_b64_tr_b16 v[134:135], %[ad1] offset:33280\n\t" \
  "v_mfma_f32_16x16x32_bf16 v[68:71], v[100:103], v[212:215], v[68:71]\n\t" \
  "v_mfma_f32_16x16x32_bf16 v[40:43], v[104:107], v[156:159], v[40:43]\n\t" \
  "ds_read_b64_tr_b16 v[136:137], %[ad2] offset:32768\n\t" \
  "ds_read_b64_tr_b16 v[138:139], %[ad2] offset:33280\n\t" \
  "v_mfma_f32_16x16x32_bf16 v[72:75], v[104:107], v[212:215], v[72:75]\n\t" \
  "v_mfma_f32_16x16x32_bf16 v[44:47], v[108:111], v[156:159], v[44:47]\n\t" \
  "ds_read_b64_tr_b16 v[140:141], %[ad3] offset:32768\n\t" \
  "ds_read_b64_tr_b16 v[142:143], %[ad3] offset:33280\n\t" \
  "v_mfma_f32_16x16x32_bf16 v[76:79], v[108:111], v[212:215], v[76:79]\n\t" \
  "s_waitcnt lgkmcnt(6)\n\t" \
  "v_mfma_f32_16x16x32_bf16 v[48:51], v[112:115], v[156:159], v[48:51]\n\t" \
  "ds_read_b64_tr_b16 v[96:97], %[ad0] offset:40960\n\t" \
  "ds_read_b64_tr_b16 v[98:99], %[ad0] offset:41472\n\t" \
  "v_mfma_f32_16x16x32_bf16 v[80:83], v[112:115], v[212:215], v[80:83]\n\t" \
  "v_mfma_f32_16x16x32_bf16 v[52:55], v[116:119], v[156:159], v[52:55]\n\t" \
  "ds_read_b64_tr_b16 v[100:101], %[ad1] offset:40960\n\t" \
  "ds_read_b64_tr_b16 v[102:103], %[ad1] offset:41472\n\t" \
  "v_mfma_f32_16x16x32_bf16 v[84:87], v[116:119], v[212:215], v[84:87]\n\t" \
  "v_mfma_f32_16x16x32_bf16 v[56:59], v[120:123], v[156:159], v[56:59]\n\t" \
  "ds_read_b64_tr_b16 v[104:105], %[ad2] offset:40960\n\t" \
  "ds_read_b64_tr_b16 v[106:107], %[ad2] offset:41472\n\t" \
  "v_mfma_f32_16x16x32_bf16 v[88:91], v[120:123], v[212:215], v[88:91]\n\t" \
  "v_mfma_f32_16x16x32_bf16 v[60:63], v[124:127], v[156:159], v[60:63]\n\t" \
  "ds_read_b64_tr_b16 v[108:109], %[ad3] offset:40960\n\t" \
  "ds_read_b64_tr_b16 v[110:111], %[ad3] offset:41472\n\t" \
  "v_mfma_f32_16x16x32_bf16 v[92:95], v[124:127], v[212:215], v[92:95]\n\t" \
  "s_waitcnt lgkmcnt(6)\n\t" \
  "v_mfma_f32_16x16x32_bf16 v[32:35], v[128:131], v[160:163], v[32:35]\n\t" \
  "ds_read_b64_tr_b16 v[112:113], %[ad0] offset:36864\n\t" \
  "ds_read_b64_tr_b16 v[114:115], %[ad0] offset:37376\n\t" \
  "v_mfma_f32_16x16x32_bf16 v[64:67], v[128:131], v[216:219], v[64:67]\n\t" \
  "v_mfma_f32_16x16x32_bf16 v[36:39], v[132:135], v[160:163], v[36:39]\n\t" \
  "ds_read_b64_tr_b16 v[116:117], %[ad1] offset:36864\n\t" \
  "ds_read_b64_tr_b16 v[118:119], %[ad1] offset:37376\n\t" \
  "v_mfma_f32_16x16x32_bf16 v[68:71], v[132:135], v[216:219], v[68:71]\n\t" \
  "v_mfma_f32_16x16x32_bf16 v[40:43], v[136:139], v[160:163], v[40:43]\n\t" \
  "ds_read_b64_tr_b16 v[120:121], %[ad2] offset:36864\n\t" \
  "ds_read_b64_tr_b16 v[122:123], %[ad2] offset:37376\n\t" \
  "v_mfma_f32_16x16x32_bf16 v[72:75], v[136:139], v[216:219], v[72:75]\n\t" \
  "v_mfma_f32_16x16x32_bf16 v[44:47], v[140:143], v[160:163], v[44:47]\n\t" \
  "ds_read_b64_tr_b16 v[124:125], %[ad3] offset:36864\n\t" \
  "ds_read_b64_tr_b16 v[126:127], %[ad3] offset:37376\n\t" \
  "v_mfma_f32_16x16x32_bf16 v[76:79], v[140:143], v[216:219], v[76:79]\n\t" \
  "s_waitcnt lgkmcnt(6)\n\t" \
  "v_mfma_f32_16x16x32_bf16 v[48:51], v[96:99], v[160:163], v[48:51]\n\t" \
  "ds_read_b64_tr_b16 v[128:129], %[ad0] offset:45056\n\t" \
  "ds_read_b64_tr_b16 v[130:131], %[ad0] offset:45568\n\t" \
  "v_mfma_f32_16x16x32_bf16 v[80:83], v[96:99], v[216:219], v[80:83]\n\t" \
  "v_mfma_f32_16x16x32_bf16 v[52:55], v[100:103], v[160:163], v[52:55]\n\t" \
  "ds_read_b64_tr_b16 v[132:133], %[ad1] offset:45056\n\t" \
  "ds_read_b64_tr_b16 v[134:135], %[ad1] offset:45568\n\t" \
  "v_mfma_f32_16x16x32_bf16 v[84:87], v[100:103], v[216:219], v[84:87]\n\t" \
  "v_mfma_f32_16x16x32_bf16 v[56:59], v[104:107], v[160:163], v[56:59]\n\t" \
  "ds_read_b64_tr_b16 v[136:137], %[ad2] offset:45056\n\t" \
  "ds_read_b64_tr_b16 v[138:139], %[ad2] offset:45568\n\t" \
  "v_mfma_f32_16x16x32_bf16 v[88:91], v[104:107], v[216:219], v[88:91]\n\t" \
  "v_mfma_f32_16x16x32_bf16 v[60:63], v[108:111], v[160:163], v[60:63]\n\t" \
  "ds_read_b64_tr_b16 v[140:141], %[ad3] offset:45056\n\t" \
  "ds_read_b64_tr_b16 v[142:143], %[ad3] offset:45568\n\t" \
  "v_mfma_f32_16x16x32_bf16 v[92:95], v[108:111], v[216:219], v[92:95]\n\t" \
  "s_waitcnt lgkmcnt(6)\n\t" \
  "v_mfma_f32_16x16x32_bf16 v[32:35], v[112:115], v[164:167], v[32:35]\n\t" \
  "ds_read_b64_tr_b16 v[96:97], %[ad0] offset:49152\n\t" \
  "ds_read_b64_tr_b16 v[98:99], %[ad0] offset:49664\n\t" \
  "v_mfma_f32_16x16x32_bf16 v[64:67], v[112:115], v[220:223], v[64:67]\n\t" \
  "v_mfma_f32_16x16x32_bf16 v[36:39], v[116:119], v[164:167], v[36:39]\n\t" \
  "ds_read_b64_tr_b16 v[100:101], %[ad1] offset:49152\n\t" \
  "ds_read_b64_tr_b16 v[102:103], %[ad1] offset:49664\n\t" \
  "v_mfma_f32_16x16x32_bf16 v[68:71], v[116:119], v[220:223], v[68:71]\n\t" \
  "v_mfma_f32_16x16x32_bf16 v[40:43], v[120:123], v[164:167], v[40:43]\n\t" \
  "ds_read_b64_tr_b16 v[104:105], %[ad2] offset:49152\n\t" \
  "ds_read_b64_tr_b16 v[106:107], %[ad2] offset:49664\n\t" \
  "v_mfma_f32_16x16x32_bf16 v[72:75], v[120:123], v[220:223], v[72:75]\n\t" \
  "v_mfma_f32_16x16x32_bf16 v[44:47], v[124:127], v[164:167], v[44:47]\n\t" \
  "ds_read_b64_tr_b16 v[108:109], %[ad3] offset:49152\n\t" \
  "ds_read_b64_tr_b16 v[110:111], %[ad3] offset:49664\n\t" \
  "v_mfma_f32_16x16x32_bf16 v[76:79], v[124:127], v[220:223], v[76:79]\n\t" \
  "s_waitcnt lgkmcnt(6)\n\t" \
  "v_mfma_f32_16x16x32_bf16 v[48:51], v[128:131], v[164:167], v[48:51]\n\t" \
  "ds_read_b64_tr_b16 v[112:113], %[ad0] offset:57344\n\t" \
  "ds_read_b64_tr_b16 v[114:115], %[ad0] offset:57856\n\t" \
  "v_mfma_f32_16x16x32_bf16 v[80:83], v[128:131], v[220:223], v[80:83]\n\t" \
  "v_mfma_f32_16x16x32_bf16 v[52:55], v[132:135], v[164:167], v[52:55]\n\t" \
  "ds_read_b64_tr_b16 v[116:117], %[ad1] offset:57344\n\t" \
  "ds_read_b64_tr_b16 v[118:119], %[ad1] offset:57856\n\t" \
  "v_mfma_f32_16x16x32_bf16 v[84:87], v[132:135], v[220:223], v[84:87]\n\t" \
  "v_mfma_f32_16x16x32_bf16 v[56:59], v[136:139], v[164:167], v[56:59]\n\t" \
  "ds_read_b64_tr_b16 v[120:121], %[ad2] offset:57344\n\t" \
  "ds_read_b64_tr_b16 v[122:123], %[ad2] offset:57856\n\t" \
  "v_mfma_f32_16x16x32_bf16 v[88:91], v[136:139], v[220:223], v[88:91]\n\t" \
  "v_mfma_f32_16x16x32_bf16 v[60:63], v[140:143], v[164:167], v[60:63]\n\t" \
  "ds_read_b64_tr_b16 v[124:125], %[ad3] offset:57344\n\t" \
  "ds_read_b64_tr_b16 v[126:127], %[ad3] offset:57856\n\t" \
  "v_mfma_f32_16x16x32_bf16 v[92:95], v[140:143], v[220:223], v[92:95]\n\t" \
  "s_waitcnt lgkmcnt(6)\n\t" \
  "v_mfma_f32_16x16x32_bf16 v[32:35], v[96:99], v[168:171], v[32:35]\n\t" \
  "ds_read_b64_tr_b16 v[128:129], %[ad0] offset:53248\n\t" \
  "ds_read_b64_tr_b16 v[130:131], %[ad0] offset:53760\n\t" \
  "v_mfma_f32_16x16x32_bf16 v[64:67], v[96:99], v[224:227], v[64:67]\n\t" \
  "v_mfma_f32_16x16x32_bf16 v[36:39], v[100:103], v[168:171], v[36:39]\n\t" \
  "ds_read_b64_tr_b16 v[132:133], %[ad1] offset:53248\n\t" \
  "ds_read_b64_tr_b16 v[134:135], %[ad1] offset:53760\n\t" \
  "v_mfma_f32_16x16x32_bf16 v[68:71], v[100:103], v[224:227], v[68:71]\n\t" \
  "v_mfma_f32_16x16x32_bf16 v[40:43], v[104:107], v[168:171], v[40:43]\n\t" \
  "ds_read_b64_tr_b16 v[136:137], %[ad2] offset:53248\n\t" \
  "ds_read_b64_tr_b16 v[138:139], %[ad2] offset:53760\n\t" \
  "v_mfma_f32_16x16x32_bf16 v[72:75], v[104:107], v[224:227], v[72:75]\n\t" \
  "v_mfma_f32_16x16x32_bf16 v[44:47], v[108:111], v[168:171], v[44:47]\n\t" \
  "ds_read_b64_tr_b16 v[140:141], %[ad3] offset:53248\n\t" \
  "ds_read_b64_tr_b16 v[142:143], %[ad3] offset:53760\n\t" \
  "v_mfma_f32_16x16x32_bf16 v[76:79], v[108:111], v[224:227], v[76:79]\n\t" \
  "s_waitcnt lgkmcnt(6)\n\t" \
  "v_mfma_f32_16x16x32_bf16 v[48:51], v[112:115], v[168:171], v[48:51]\n\t" \
  "ds_read_b64_tr_b16 v[96:97], %[ad0] offset:61440\n\t" \
  "ds_read_b64_tr_b16 v[98:99], %[ad0] offset:61952\n\t" \
  "v_mfma_f32_16x16x32_bf16 v[80:83], v[112:115], v[224:227], v[80:83]\n\t" \
  "v_mfma_f32_16x16x32_bf16 v[52:55], v[116:119], v[168:171], v[52:55]\n\t" \
  "ds_read_b64_tr_b16 v[100:101], %[ad1] offset:61440\n\t" \
  "ds_read_b64_tr_b16 v[102:103], %[ad1] offset:61952\n\t" \
  "v_mfma_f32_16x16x32_bf16 v[84:87], v[116:119], v[224:227], v[84:87]\n\t" \
  "v_mfma_f32_16x16x32_bf16 v[56:59], v[120:123], v[168:171], v[56:59]\n\t" \
  "ds_read_b64_tr_b16 v[104:105], %[ad2] offset:61440\n\t" \
  "ds_read_b64_tr_b16 v[106:107], %[ad2] offset:61952\n\t" \
  "v_mfma_f32_16x16x32_bf16 v[88:91], v[120:123], v[224:227], v[88:91]\n\t" \
  "v_mfma_f32_16x16x32_bf16 v[60:63], v[124:127], v[168:171], v[60:63]\n\t" \
  "ds_read_b64_tr_b16 v[108:109], %[ad3] offset:61440\n\t" \
  "ds_read_b64_tr_b16 v[110:111], %[ad3] offset:61952\n\t" \
  "v_mfma_f32_16x16x32_bf16 v[92:95], v[124:127], v[224:227], v[92:95]\n\t" \
  "s_waitcnt lgkmcnt(6)\n\t" \
  "v_add_u32 %[ad0], 0x10000, %[ad0]\n\t" \
  "v_add_u32 %[ad1], 0x10000, %[ad1]\n\t" \
  "v_add_u32 %[ad2], 0x10000, %[ad2]\n\t" \
  "v_add_u32 %[ad3], 0x10000, %[ad3]\n\t" \
  "v_mfma_f32_16x16x32_bf16 v[32:35], v[128:131], v[172:175], v[32:35]\n\t" \
  "ds_read_b64_tr_b16 v[112:113], %[ad0] offset:0\n\t" \
  "ds_read_b64_tr_b16 v[114:115], %[ad0] offset:512\n\t" \
  "v_mfma_f32_16x16x32_bf16 v[64:67], v[128:131], v[228:231], v[64:67]\n\t" \
  "v_mfma_f32_16x16x32_bf16 v[36:39], v[132:135], v[172:175], v[36:39]\n\t" \
  "ds_read_b64_tr_b16 v[116:117], %[ad1] offset:0\n\t" \
  "ds_read_b64_tr_b16 v[118:119], %[ad1] offset:512\n\t" \
  "v_mfma_f32_16x16x32_bf16 v[68:71], v[132:135], v[228:231], v[68:71]\n\t" \
  "v_mfma_f32_16x16x32_bf16 v[40:43], v[136:139], v[172:175], v[40:43]\n\t" \
  "ds_read_b64_tr_b16 v[120:121], %[ad2] offset:0\n\t" \
  "ds_read_b64_tr_b16 v[122:123], %[ad2] offset:512\n\t" \
  "v_mfma_f32_16x16x32_bf16 v[72:75], v[136:139], v[228:231], v[72:75]\n\t" \
  "v_mfma_f32_16x16x32_bf16 v[44:47], v[140:143], v[172:175], v[44:47]\n\t" \
  "ds_read_b64_tr_b16 v[124:125], %[ad3] offset:0\n\t" \
  "ds_read_b64_tr_b16 v[126:127], %[ad3] offset:512\n\t" \
  "v_mfma_f32_16x16x32_bf16 v[76:79], v[140:143], v[228:231], v[76:79]\n\t" \
  "s_waitcnt lgkmcnt(6)\n\t" \
  "v_mfma_f32_16x16x32_bf16 v[48:51], v[96:99], v[172:175], v[48:51]\n\t" \
  "ds_read_b64_tr_b16 v[128:129], %[ad0] offset:8192\n\t" \
  "ds_read_b64_tr_b16 v[130:131], %[ad0] offset:8704\n\t" \
  "v_mfma_f32_16x16x32_bf16 v[80:83], v[96:99], v[228:231], v[80:83]\n\t" \
  "v_mfma_f32_16x16x32_bf16 v[52:55], v[100:103], v[172:175], v[52:55]\n\t" \
  "ds_read_b64_tr_b16 v[132:133], %[ad1] offset:8192\n\t" \
  "ds_read_b64_tr_b16 v[134:135], %[ad1] offset:8704\n\t" \
  "v_mfma_f32_16x16x32_bf16 v[84:87], v[100:103], v[228:231], v[84:87]\n\t" \
  "v_mfma_f32_16x16x32_bf16 v[56:59], v[104:107], v[172:175], v[56:59]\n\t" \
  "ds_read_b64_tr_b16 v[136:137], %[ad2] offset:8192\n\t" \
  "ds_read_b64_tr_b16 v[138:139], %[ad2] offset:8704\n\t" \
  "v_mfma_f32_16x16x32_bf16 v[88:91], v[104:107], v[228:231], v[88:91]\n\t" \
  "v_mfma_f32_16x16x32_bf16 v[60:63], v[108:111], v[172:175], v[60:63]\n\t" \
  "ds_read_b64_tr_b16 v[140:141], %[ad3] offset:8192\n\t" \
  "ds_read_b64_tr_b16 v[142:143], %[ad3] offset:8704\n\t" \
  "v_mfma_f32_16x16x32_bf16 v[92:95], v[108:111], v[228:231], v[92:95]\n\t" \
  "s_waitcnt lgkmcnt(6)\n\t" \
  "v_mfma_f32_16x16x32_bf16 v[32:35], v[112:115], v[176:179], v[32:35]\n\t" \
  "ds_read_b64_tr_b16 v[96:97], %[ad0] offset:4096\n\t" \
  "ds_read_b64_tr_b16 v[98:99], %[ad0] offset:4608\n\t" \
  "v_mfma_f32_16x16x32_bf16 v[64:67], v[112:115], v[232:235], v[64:67]\n\t" \
  "v_mfma_f32_16x16x32_bf16 v[36:39], v[116:119], v[176:179], v[36:39]\n\t" \
  "ds_read_b64_tr_b16 v[100:101], %[ad1] offset:4096\n\t" \
  "ds_read_b64_tr_b16 v[102:103], %[ad1] offset:4608\n\t" \
  "v_mfma_f32_16x16x32_bf16 v[68:71], v[116:119], v[232:235], v[68:71]\n\t" \
  "v_mfma_f32_16x16x32_bf16 v[40:43], v[120:123], v[176:179], v[40:43]\n\t" \
  "ds_read_b64_tr_b16 v[104:105], %[ad2] offset:4096\n\t" \
  "ds_read_b64_tr_b16 v[106:107], %[ad2] offset:4608\n\t" \
  "v_mfma_f32_16x16x32_bf16 v[72:75], v[120:123], v[232:235], v[72:75]\n\t" \
  "v_mfma_f32_16x16x32_bf16 v[44:47], v[124:127], v[176:179], v[44:47]\n\t" \
  "ds_read_b64_tr_b16 v[108:109], %[ad3] offset:4096\n\t" \
  "ds_read_b64_tr_b16 v[110:111], %[ad3] offset:4608\n\t" \
  "v_mfma_f32_16x16x32_bf16 v[76:79], v[124:127], v[232:235], v[76:79]\n\t" \
  "s_waitcnt lgkmcnt(6)\n\t" \
  "v_mfma_f32_16x16x32_bf16 v[48:51], v[128:131], v[176:179], v[48:51]\n\t" \
  "ds_read_b64_tr_b16 v[112:113], %[ad0] offset:12288\n\t" \
  "ds_read_b64_tr_b16 v[114:115], %[ad0] offset:12800\n\t" \
  "v_mfma_f32_16x16x32_bf16 v[80:83], v[128:131], v[232:235], v[80:83]\n\t" \
  "v_mfma_f32_16x16x32_bf16 v[52:55], v[132:135], v[176:179], v[52:55]\n\t" \
  "ds_read_b64_tr_b16 v[116:117], %[ad1] offset:12288\n\t" \
  "ds_read_b64_tr_b16 v[118:119], %[ad1] offset:12800\n\t" \
  "v_mfma_f32_16x16x32_bf16 v[84:87], v[132:135], v[232:235], v[84:87]\n\t" \
  "v_mfma_f32_16x16x32_bf16 v[56:59], v[136:139], v[176:179], v[56:59]\n\t" \
  "ds_read_b64_tr_b16 v[120:121], %[ad2] offset:12288\n\t" \
  "ds_read_b64_tr_b16 v[122:123], %[ad2] offset:12800\n\t" \
  "v_mfma_f32_16x16x32_bf16 v[88:91], v[136:139], v[232:235], v[88:91]\n\t" \
  "v_mfma_f32_16x16x32_bf16 v[60:63], v[140:143], v[176:179], v[60:63]\n\t" \
  "ds_read_b64_tr_b16 v[124:125], %[ad3] offset:12288\n\t" \
  "ds_read_b64_tr_b16 v[126:127], %[ad3] offset:12800\n\t" \
  "v_mfma_f32_16x16x32_bf16 v[92:95], v[140:143], v[232:235], v[92:95]\n\t" \
  "s_waitcnt lgkmcnt(6)\n\t" \
  "v_mfma_f32_16x16x32_bf16 v[32:35], v[96:99], v[180:183], v[32:35]\n\t" \
  "ds_read_b64_tr_b16 v[128:129], %[ad0] offset:16384\n\t" \
  "ds_read_b64_tr_b16 v[130:131], %[ad0] offset:16896\n\t" \
  "v_mfma_f32_16x16x32_bf16 v[64:67], v[96:99], v[236:239], v[64:67]\n\t" \
  "v_mfma_f32_16x16x32_bf16 v[36:39], v[100:103], v[180:183], v[36:39]\n\t" \
  "ds_read_b64_tr_b16 v[132:133], %[ad1] offset:16384\n\t" \
  "ds_read_b64_tr_b16 v[134:135], %[ad1] offset:16896\n\t" \
  "v_mfma_f32_16x16x32_bf16 v[68:71], v[100:103], v[236:239], v[68:71]\n\t" \
  "v_mfma_f32_16x16x32_bf16 v[40:43], v[104:107], v[180:183], v[40:43]\n\t" \
  "ds_read_b64_tr_b16 v[136:137], %[ad2] offset:16384\n\t" \
  "ds_read_b64_tr_b16 v[138:139], %[ad2] offset:16896\n\t" \
  "v_mfma_f32_16x16x32_bf16 v[72:75], v[104:107], v[236:239], v[72:75]\n\t" \
  "v_mfma_f32_16x16x32_bf16 v[44:47], v[108:111], v[180:183], v[44:47]\n\t" \
  "ds_read_b64_tr_b16 v[140:141], %[ad3] offset:16384\n\t" \
  "ds_read_b64_tr_b16 v[142:143], %[ad3] offset:16896\n\t" \
  "v_mfma_f32_16x16x32_bf16 v[76:79], v[108:111], v[236:239], v[76:79]\n\t" \
  "s_waitcnt lgkmcnt(6)\n\t" \
  "v_mfma_f32_16x16x32_bf16 v[48:51], v[112:115], v[180:183], v[48:51]\n\t" \
  "ds_read_b64_tr_b16 v[96:97], %[ad0] offset:24576\n\t" \
  "ds_read_b64_tr_b16 v[98:99], %[ad0] offset:25088\n\t" \
  "v_mfma_f32_16x16x32_bf16 v[80:83], v[112:115], v[236:239], v[80:83]\n\t" \
  "v_mfma_f32_16x16x32_bf16 v[52:55], v[116:119], v[180:183], v[52:55]\n\t" \
  "ds_read_b64_tr_b16 v[100:101], %[ad1] offset:24576\n\t" \
  "ds_read_b64_tr_b16 v[102:103], %[ad1] offset:25088\n\t" \
  "v_mfma_f32_16x16x32_bf16 v[84:87], v[116:119], v[236:239], v[84:87]\n\t" \
  "v_mfma_f32_16x16x32_bf16 v[56:59], v[120:123], v[180:183], v[56:59]\n\t" \
  "ds_read_b64_tr_b16 v[104:105], %[ad2] offset:24576\n\t" \
  "ds_read_b64_tr_b16 v[106:107], %[ad2] offset:25088\n\t" \
  "v_mfma_f32_16x16x32_bf16 v[88:91], v[120:123], v[236:239], v[88:91]\n\t" \
  "v_mfma_f32_16x16x32_bf16 v[60:63], v[124:127], v[180:183], v[60:63]\n\t" \
  "ds_read_b64_tr_b16 v[108:109], %[ad3] offset:24576\n\t" \
  "ds_read_b64_tr_b16 v[110:111], %[ad3] offset:25088\n\t" \
  "v_mfma_f32_16x16x32_bf16 v[92:95], v[124:127], v[236:239], v[92:95]\n\t" \
  "s_waitcnt lgkmcnt(6)\n\t" \
  "v_mfma_f32_16x16x32_bf16 v[32:35], v[128:131], v[184:187], v[32:35]\n\t" \
  "ds_read_b64_tr_b16 v[112:113], %[ad0] offset:20480\n\t" \
  "ds_read_b64_tr_b16 v[114:115], %[ad0] offset:20992\n\t" \
  "v_mfma_f32_16x16x32_bf16 v[64:67], v[128:131], v[240:243], v[64:67]\n\t" \
  "v_mfma_f32_16x16x32_bf16 v[36:39], v[132:135], v[184:187], v[36:39]\n\t" \
  "ds_read_b64_tr_b16 v[116:117], %[ad1] offset:20480\n\t" \
  "ds_read_b64_tr_b16 v[118:119], %[ad1] offset:20992\n\t" \
  "v_mfma_f32_16x16x32_bf16 v[68:71], v[132:135], v[240:243], v[68:71]\n\t" \
  "v_mfma_f32_16x16x32_bf16 v[40:43], v[136:139], v[184:187], v[40:43]\n\t" \
  "ds_read_b64_tr_b16 v[120:121], %[ad2] offset:20480\n\t" \
  "ds_read_b64_tr_b16 v[122:123], %[ad2] offset:20992\n\t" \
  "v_mfma_f32_16x16x32_bf16 v[72:75], v[136:139], v[240:243], v[72:75]\n\t" \
  "v_mfma_f32_16x16x32_bf16 v[44:47], v[140:143], v[184:187], v[44:47]\n\t" \
  "ds_read_b64_tr_b16 v[124:125], %[ad3] offset:20480\n\t" \
  "ds_read_b64_tr_b16 v[126:127], %[ad3] offset:20992\n\t" \
  "v_mfma_f32_16x16x32_bf16 v[76:79], v[140:143], v[240:243], v[76:79]\n\t" \
  "s_waitcnt lgkmcnt(6)\n\t" \
  "v_mfma_f32_16x16x32_bf16 v[48:51], v[96:99], v[184:187], v[48:51]\n\t" \
  "ds_read_b64_tr_b16 v[128:129], %[ad0] offset:28672\n\t" \
  "ds_read_b64_tr_b16 v[130:131], %[ad0] offset:29184\n\t" \
  "v_mfma_f32_16x16x32_bf16 v[80:83], v[96:99], v[240:243], v[80:83]\n\t" \
  "v_mfma_f32_16x16x32_bf16 v[52:55], v[100:103], v[184:187], v[52:55]\n\t" \
  "ds_read_b64_tr_b16 v[132:133], %[ad1] offset:28672\n\t" \
  "ds_read_b64_tr_b16 v[134:135], %[ad1] offset:29184\n\t" \
  "v_mfma_f32_16x16x32_bf16 v[84:87], v[100:103], v[240:243], v[84:87]\n\t" \
  "v_mfma_f32_16x16x32_bf16 v[56:59], v[104:107], v[184:187], v[56:59]\n\t" \
  "ds_read_b64_tr_b16 v[136:137], %[ad2] offset:28672\n\t" \
  "ds_read_b64_tr_b16 v[138:139], %[ad2] offset:29184\n\t" \
  "v_mfma_f32_16x16x32_bf16 v[88:91], v[104:107], v[240:243], v[88:91]\n\t" \
  "v_mfma_f32_16x16x32_bf16 v[60:63], v[108:111], v[184:187], v[60:63]\n\t" \
  "ds_read_b64_tr_b16 v[140:141], %[ad3] offset:28672\n\t" \
  "ds_read_b64_tr_b16 v[142:143], %[ad3] offset:29184\n\t" \
  "v_mfma_f32_16x16x32_bf16 v[92:95], v[108:111], v[240:243], v[92:95]\n\t" \
  "s_waitcnt lgkmcnt(6)\n\t" \
  "v_mfma_f32_16x16x32_bf16 v[32:35], v[112:115], v[188:191], v[32:35]\n\t" \
  "ds_read_b64_tr_b16 v[96:97], %[ad0] offset:32768\n\t" \
  "ds_read_b64_tr_b16 v[98:99], %[ad0] offset:33280\n\t" \
  "v_mfma_f32_16x16x32_bf16 v[64:67], v[112:115], v[244:247], v[64:67]\n\t" \
  "v_mfma_f32_16x16x32_bf16 v[36:39], v[116:119], v[188:191], v[36:39]\n\t" \
  "ds_read_b64_tr_b16 v[100:101], %[ad1] offset:32768\n\t" \
  "ds_read_b64_tr_b16 v[102:103], %[ad1] offset:33280\n\t" \
  "v_mfma_f32_16x16x32_bf16 v[68:71], v[116:119], v[244:247], v[68:71]\n\t" \
  "v_mfma_f32_16x16x32_bf16 v[40:43], v[120:123], v[188:191], v[40:43]\n\t" \
  "ds_read_b64_tr_b16 v[104:105], %[ad2] offset:32768\n\t" \
  "ds_read_b64_tr_b16 v[106:107], %[ad2] offset:33280\n\t" \
  "v_mfma_f32_16x16x32_bf16 v[72:75], v[120:123], v[244:247], v[72:75]\n\t" \
  "v_mfma_f32_16x16x32_bf16 v[44:47], v[124:127], v[188:191], v[44:47]\n\t" \
  "ds_read_b64_tr_b16 v[108:109], %[ad3] offset:32768\n\t" \
  "ds_read_b64_tr_b16 v[110:111], %[ad3] offset:33280\n\t" \
  "v_mfma_f32_16x16x32_bf16 v[76:79], v[124:127], v[244:247], v[76:79]\n\t" \
  "s_waitcnt lgkmcnt(6)\n\t" \
  "v_mfma_f32_16x16x32_bf16 v[48:51], v[128:131], v[188:191], v[48:51]\n\t" \
  "ds_read_b64_tr_b16 v[112:113], %[ad0] offset:40960\n\t" \
  "ds_read_b64_tr_b16 v[114:115], %[ad0] offset:41472\n\t" \
  "v_mfma_f32_16x16x32_bf16 v[80:83], v[128:131], v[244:247], v[80:83]\n\t" \
  "v_mfma_f32_16x16x32_bf16 v[52:55], v[132:135], v[188:191], v[52:55]\n\t" \
  "ds_read_b64_tr_b16 v[116:117], %[ad1] offset:40960\n\t" \
  "ds_read_b64_tr_b16 v[118:119], %[ad1] offset:41472\n\t" \
  "v_mfma_f32_16x16x32_bf16 v[84:87], v[132:135], v[244:247], v[84:87]\n\t" \
  "v_mfma_f32_16x16x32_bf16 v[56:59], v[136:139], v[188:191], v[56:59]\n\t" \
  "ds_read_b64_tr_b16 v[120:121], %[ad2] offset:40960\n\t" \
  "ds_read_b64_tr_b16 v[122:123], %[ad2] offset:41472\n\t" \
  "v_mfma_f32_16x16x32_bf16 v[88:91], v[136:139], v[244:247], v[88:91]\n\t" \
  "v_mfma_f32_16x16x32_bf16 v[60:63], v[140:143], v[188:191], v[60:63]\n\t" \
  "ds_read_b64_tr_b16 v[124:125], %[ad3] offset:40960\n\t" \
  "ds_read_b64_tr_b16 v[126:127], %[ad3] offset:41472\n\t" \
  "v_mfma_f32_16x16x32_bf16 v[92:95], v[140:143], v[244:247], v[92:95]\n\t" \
  "s_waitcnt lgkmcnt(6)\n\t" \
  "v_mfma_f32_16x16x32_bf16 v[32:35], v[96:99], v[192:195], v[32:35]\n\t" \
  "ds_read_b64_tr_b16 v[128:129], %[ad0] offset:36864\n\t" \
  "ds_read_b64_tr_b16 v[130:131], %[ad0] offset:37376\n\t" \
  "v_mfma_f32_16x16x32_bf16 v[64:67], v[96:99], v[248:251], v[64:67]\n\t" \
  "v_mfma_f32_16x16x32_bf16 v[36:39], v[100:103], v[192:195], v[36:39]\n\t" \
  "ds_read_b64_tr_b16 v[132:133], %[ad1] offset:36864\n\t" \
  "ds_read_b64_tr_b16 v[134:135], %[ad1] offset:37376\n\t" \
  "v_mfma_f32_16x16x32_bf16 v[68:71], v[100:103], v[248:251], v[68:71]\n\t" \
  "v_mfma_f32_16x16x32_bf16 v[40:43], v[104:107], v[192:195], v[40:43]\n\t" \
  "ds_read_b64_tr_b16 v[136:137], %[ad2] offset:36864\n\t" \
  "ds_read_b64_tr_b16 v[138:139], %[ad2] offset:37376\n\t" \
  "v_mfma_f32_16x16x32_bf16 v[72:75], v[104:107], v[248:251], v[72:75]\n\t" \
  "v_mfma_f32_16x16x32_bf16 v[44:47], v[108:111], v[192:195], v[44:47]\n\t" \
  "ds_read_b64_tr_b16 v[140:141], %[ad3] offset:36864\n\t" \
  "ds_read_b64_tr_b16 v[142:143], %[ad3] offset:37376\n\t" \
  "v_mfma_f32_16x16x32_bf16 v[76:79], v[108:111], v[248:251], v[76:79]\n\t" \
  "s_waitcnt lgkmcnt(6)\n\t" \
  "v_mfma_f32_16x16x32_bf16 v[48:51], v[112:115], v[192:195], v[48:51]\n\t" \
  "ds_read_b64_tr_b16 v[96:97], %[ad0] offset:45056\n\t" \
  "ds_read_b64_tr_b16 v[98:99], %[ad0] offset:45568\n\t" \
  "v_mfma_f32_16x16x32_bf16 v[80:83], v[112:115], v[248:251], v[80:83]\n\t" \
  "v_mfma_f32_16x16x32_bf16 v[52:55], v[116:119], v[192:195], v[52:55]\n\t" \
  "ds_read_b64_tr_b16 v[100:101], %[ad1] offset:45056\n\t" \
  "ds_read_b64_tr_b16 v[102:103], %[ad1] offset:45568\n\t" \
  "v_mfma_f32_16x16x32_bf16 v[84:87], v[116:119], v[248:251], v[84:87]\n\t" \
  "v_mfma_f32_16x16x32_bf16 v[56:59], v[120:123], v[192:195], v[56:59]\n\t" \
  "ds_read_b64_tr_b16 v[104:105], %[ad2] offset:45056\n\t" \
  "ds_read_b64_tr_b16 v[106:107], %[ad2] offset:45568\n\t" \
  "v_mfma_f32_16x16x32_bf16 v[88:91], v[120:123], v[248:251], v[88:91]\n\t" \
  "v_mfma_f32_16x16x32_bf16 v[60:63], v[124:127], v[192:195], v[60:63]\n\t" \
  "ds_read_b64_tr_b16 v[108:109], %[ad3] offset:45056\n\t" \
  "ds_read_b64_tr_b16 v[110:111], %[ad3] offset:45568\n\t" \
  "v_mfma_f32_16x16x32_bf16 v[92:95], v[124:127], v[248:251], v[92:95]\n\t" \
  "s_waitcnt lgkmcnt(8)\n\t" \
  "v_mfma_f32_16x16x32_bf16 v[32:35], v[128:131], v[196:199], v[32:35]\n\t" \
  "v_mfma_f32_16x16x32_bf16 v[64:67], v[128:131], v[252:255], v[64:67]\n\t" \
  "v_mfma_f32_16x16x32_bf16 v[36:39], v[132:135], v[196:199], v[36:39]\n\t" \
  "v_mfma_f32_16x16x32_bf16 v[68:71], v[132:135], v[252:255], v[68:71]\n\t" \
  "v_mfma_f32_16x16x32_bf16 v[40:43], v[136:139], v[196:199], v[40:43]\n\t" \
  "v_mfma_f32_16x16x32_bf16 v[72:75], v[136:139], v[252:255], v[72:75]\n\t" \
  "v_mfma_f32_16x16x32_bf16 v[44:47], v[140:143], v[196:199], v[44:47]\n\t" \
  "v_mfma_f32_16x16x32_bf16 v[76:79], v[140:143], v[252:255], v[76:79]\n\t" \
  "s_waitcnt lgkmcnt(0)\n\t" \
  "v_mfma_f32_16x16x32_bf16 v[48:51], v[96:99], v[196:199], v[48:51]\n\t" \
  "v_mfma_f32_16x16x32_bf16 v[80:83], v[96:99], v[252:255], v[80:83]\n\t" \
  "v_mfma_f32_16x16x32_bf16 v[52:55], v[100:103], v[196:199], v[52:55]\n\t" \
  "v_mfma_f32_16x16x32_bf16 v[84:87], v[100:103], v[252:255], v[84:87]\n\t" \
  "v_mfma_f32_16x16x32_bf16 v[56:59], v[104:107], v[196:199], v[56:59]\n\t" \
  "v_mfma_f32_16x16x32_bf16 v[88:91], v[104:107], v[252:255], v[88:91]\n\t" \
  "v_mfma_f32_16x16x32_bf16 v[60:63], v[108:111], v[196:199], v[60:63]\n\t" \
  "v_mfma_f32_16x16x32_bf16 v[92:95], v[108:111], v[252:255], v[92:95]\n\t" \
  "v_subrev_u32 %[ad0], 0x10000, %[ad0]\n\t" \
  "v_subrev_u32 %[ad1], 0x10000, %[ad1]\n\t" \
  "v_subrev_u32 %[ad2], 0x10000, %[ad2]\n\t" \
  "v_subrev_u32 %[ad3], 0x10000, %[ad3]\n\t" \
  "s_nop 7\n\t" \
  "s_nop 7\n\t"

#define K12_WISSUE_ASM \
  "s_add_u32 s60, %[gblo], 0\n\t" \
  "s_addc_u32 s61, %[gbhi], 0\n\t" \
  "s_add_u32 m0, %[ldo], 0\n\t" \
  "s_nop 0\n\t" \
  "global_load_lds_dwordx4 %[lo0], s[60:61]\n\t" \
  "s_add_u32 s60, %[gblo], 28672\n\t" \
  "s_addc_u32 s61, %[gbhi], 0\n\t" \
  "s_add_u32 m0, %[ldo], 1024\n\t" \
  "s_nop 0\n\t" \
  "global_load_lds_dwordx4 %[lo1], s[60:61]\n\t" \
  "s_add_u32 s60, %[ublo], 0\n\t" \
  "s_addc_u32 s61, %[ubhi], 0\n\t" \
  "s_add_u32 m0, %[ldo], 8192\n\t" \
  "s_nop 0\n\t" \
  "global_load_lds_dwordx4 %[lo0], s[60:61]\n\t" \
  "s_add_u32 s60, %[ublo], 28672\n\t" \
  "s_addc_u32 s61, %[ubhi], 0\n\t" \
  "s_add_u32 m0, %[ldo], 9216\n\t" \
  "s_nop 0\n\t" \
  "global_load_lds_dwordx4 %[lo1], s[60:61]\n\t" \
  "s_waitcnt vmcnt(4)\n\t" \
  "s_add_u32 s60, %[gblo], 229376\n\t" \
  "s_addc_u32 s61, %[gbhi], 0\n\t" \
  "s_add_u32 m0, %[ldo], 16384\n\t" \
  "s_nop 0\n\t" \
  "global_load_lds_dwordx4 %[lo0], s[60:61]\n\t" \
  "s_add_u32 s60, %[gblo], 258048\n\t" \
  "s_addc_u32 s61, %[gbhi], 0\n\t" \
  "s_add_u32 m0, %[ldo], 17408\n\t" \
  "s_nop 0\n\t" \
  "global_load_lds_dwordx4 %[lo1], s[60:61]\n\t" \
  "s_add_u32 s60, %[ublo], 229376\n\t" \
  "s_addc_u32 s61, %[ubhi], 0\n\t" \
  "s_add_u32 m0, %[ldo], 24576\n\t" \
  "s_nop 0\n\t" \
  "global_load_lds_dwordx4 %[lo0], s[60:61]\n\t" \
  "s_add_u32 s60, %[ublo], 258048\n\t" \
  "s_addc_u32 s61, %[ubhi], 0\n\t" \
  "s_add_u32 m0, %[ldo], 25600\n\t" \
  "s_nop 0\n\t" \
  "global_load_lds_dwordx4 %[lo1], s[60:61]\n\t" \
  "s_waitcnt vmcnt(4)\n\t" \
  "s_add_u32 s60, %[gblo], 458752\n\t" \
  "s_addc_u32 s61, %[gbhi], 0\n\t" \
  "s_add_u32 m0, %[ldo], 32768\n\t" \
  "s_nop 0\n\t" \
  "global_load_lds_dwordx4 %[lo0], s[60:61]\n\t" \
  "s_add_u32 s60, %[gblo], 487424\n\t" \
  "s_addc_u32 s61, %[gbhi], 0\n\t" \
  "s_add_u32 m0, %[ldo], 33792\n\t" \
  "s_nop 0\n\t" \
  "global_load_lds_dwordx4 %[lo1], s[60:61]\n\t" \
  "s_add_u32 s60, %[ublo], 458752\n\t" \
  "s_addc_u32 s61, %[ubhi], 0\n\t" \
  "s_add_u32 m0, %[ldo], 40960\n\t" \
  "s_nop 0\n\t" \
  "global_load_lds_dwordx4 %[lo0], s[60:61]\n\t" \
  "s_add_u32 s60, %[ublo], 487424\n\t" \
  "s_addc_u32 s61, %[ubhi], 0\n\t" \
  "s_add_u32 m0, %[ldo], 41984\n\t" \
  "s_nop 0\n\t" \
  "global_load_lds_dwordx4 %[lo1], s[60:61]\n\t" \
  "s_waitcnt vmcnt(4)\n\t" \
  "s_add_u32 s60, %[gblo], 688128\n\t" \
  "s_addc_u32 s61, %[gbhi], 0\n\t" \
  "s_add_u32 m0, %[ldo], 49152\n\t" \
  "s_nop 0\n\t" \
  "global_load_lds_dwordx4 %[lo0], s[60:61]\n\t" \
  "s_add_u32 s60, %[gblo], 716800\n\t" \
  "s_addc_u32 s61, %[gbhi], 0\n\t" \
  "s_add_u32 m0, %[ldo], 50176\n\t" \
  "s_nop 0\n\t" \
  "global_load_lds_dwordx4 %[lo1], s[60:61]\n\t" \
  "s_add_u32 s60, %[ublo], 688128\n\t" \
  "s_addc_u32 s61, %[ubhi], 0\n\t" \
  "s_add_u32 m0, %[ldo], 57344\n\t" \
  "s_nop 0\n\t" \
  "global_load_lds_dwordx4 %[lo0], s[60:61]\n\t" \
  "s_add_u32 s60, %[ublo], 716800\n\t" \
  "s_addc_u32 s61, %[ubhi], 0\n\t" \
  "s_add_u32 m0, %[ldo], 58368\n\t" \
  "s_nop 0\n\t" \
  "global_load_lds_dwordx4 %[lo1], s[60:61]\n\t" \
  "s_waitcnt vmcnt(4)\n\t" \
  "s_add_u32 s60, %[gblo], 917504\n\t" \
  "s_addc_u32 s61, %[gbhi], 0\n\t" \
  "s_add_u32 m0, %[ldo], 65536\n\t" \
  "s_nop 0\n\t" \
  "global_load_lds_dwordx4 %[lo0], s[60:61]\n\t" \
  "s_add_u32 s60, %[gblo], 946176\n\t" \
  "s_addc_u32 s61, %[gbhi], 0\n\t" \
  "s_add_u32 m0, %[ldo], 66560\n\t" \
  "s_nop 0\n\t" \
  "global_load_lds_dwordx4 %[lo1], s[60:61]\n\t" \
  "s_add_u32 s60, %[ublo], 917504\n\t" \
  "s_addc_u32 s61, %[ubhi], 0\n\t" \
  "s_add_u32 m0, %[ldo], 73728\n\t" \
  "s_nop 0\n\t" \
  "global_load_lds_dwordx4 %[lo0], s[60:61]\n\t" \
  "s_add_u32 s60, %[ublo], 946176\n\t" \
  "s_addc_u32 s61, %[ubhi], 0\n\t" \
  "s_add_u32 m0, %[ldo], 74752\n\t" \
  "s_nop 0\n\t" \
  "global_load_lds_dwordx4 %[lo1], s[60:61]\n\t" \
  "s_waitcnt vmcnt(4)\n\t" \
  "s_add_u32 s60, %[gblo], 1146880\n\t" \
  "s_addc_u32 s61, %[gbhi], 0\n\t" \
  "s_add_u32 m0, %[ldo], 81920\n\t" \
  "s_nop 0\n\t" \
  "global_load_lds_dwordx4 %[lo0], s[60:61]\n\t" \
  "s_add_u32 s60, %[gblo], 1175552\n\t" \
  "s_addc_u32 s61, %[gbhi], 0\n\t" \
  "s_add_u32 m0, %[ldo], 82944\n\t" \
  "s_nop 0\n\t" \
  "global_load_lds_dwordx4 %[lo1], s[60:61]\n\t" \
  "s_add_u32 s60, %[ublo], 1146880\n\t" \
  "s_addc_u32 s61, %[ubhi], 0\n\t" \
  "s_add_u32 m0, %[ldo], 90112\n\t" \
  "s_nop 0\n\t" \
  "global_load_lds_dwordx4 %[lo0], s[60:61]\n\t" \
  "s_add_u32 s60, %[ublo], 1175552\n\t" \
  "s_addc_u32 s61, %[ubhi], 0\n\t" \
  "s_add_u32 m0, %[ldo], 91136\n\t" \
  "s_nop 0\n\t" \
  "global_load_lds_dwordx4 %[lo1], s[60:61]\n\t" \
  "s_waitcnt vmcnt(4)\n\t" \
  "s_add_u32 s60, %[gblo], 1376256\n\t" \
  "s_addc_u32 s61, %[gbhi], 0\n\t" \
  "s_add_u32 m0, %[ldo], 98304\n\t" \
  "s_nop 0\n\t" \
  "global_load_lds_dwordx4 %[lo0], s[60:61]\n\t" \
  "s_add_u32 s60, %[gblo], 1404928\n\t" \
  "s_addc_u32 s61, %[gbhi], 0\n\t" \
  "s_add_u32 m0, %[ldo], 99328\n\t" \
  "s_nop 0\n\t" \
  "global_load_lds_dwordx4 %[lo1], s[60:61]\n\t" \
  "s_add_u32 s60, %[ublo], 1376256\n\t" \
  "s_addc_u32 s61, %[ubhi], 0\n\t" \
  "s_add_u32 m0, %[ldo], 106496\n\t" \
  "s_nop 0\n\t" \
  "global_load_lds_dwordx4 %[lo0], s[60:61]\n\t" \
  "s_add_u32 s60, %[ublo], 1404928\n\t" \
  "s_addc_u32 s61, %[ubhi], 0\n\t" \
  "s_add_u32 m0, %[ldo], 107520\n\t" \
  "s_nop 0\n\t" \
  "global_load_lds_dwordx4 %[lo1], s[60:61]\n\t" \
  "s_waitcnt vmcnt(4)\n\t"

#define K2_ROUND0B_ASM \
  "s_add_u32 s60, %[gblo], 0\n\t" \
  "s_addc_u32 s61, %[gbhi], 0\n\t" \
  "s_add_u32 m0, %[ldo], 0\n\t" \
  "s_nop 0\n\t" \
  "global_load_lds_dwordx4 %[lo0], s[60:61]\n\t" \
  "s_add_u32 s60, %[gblo], 28672\n\t" \
  "s_addc_u32 s61, %[gbhi], 0\n\t" \
  "s_add_u32 m0, %[ldo], 1024\n\t" \
  "s_nop 0\n\t" \
  "global_load_lds_dwordx4 %[lo1], s[60:61]\n\t" \
  "s_add_u32 s60, %[ublo], 0\n\t" \
  "s_addc_u32 s61, %[ubhi], 0\n\t" \
  "s_add_u32 m0, %[ldo], 8192\n\t" \
  "s_nop 0\n\t" \
  "global_load_lds_dwordx4 %[lo0], s[60:61]\n\t" \
  "s_add_u32 s60, %[ublo], 28672\n\t" \
  "s_addc_u32 s61, %[ubhi], 0\n\t" \
  "s_add_u32 m0, %[ldo], 9216\n\t" \
  "s_nop 0\n\t" \
  "global_load_lds_dwordx4 %[lo1], s[60:61]\n\t" \
  "s_add_u32 s62, %[xslo], 0\n\t" \
  "s_addc_u32 s63, %[xshi], 0\n\t" \
  "global_load_dwordx4 v[144:147], %[voff], s[62:63] offset:0\n\t" \
  "s_add_u32 s62, %[xslo], 12288\n\t" \
  "s_addc_u32 s63, %[xshi], 0\n\t" \
  "global_load_dwordx4 v[200:203], %[voff], s[62:63] offset:2048\n\t" \
  "s_add_u32 s62, %[xslo], 0\n\t" \
  "s_addc_u32 s63, %[xshi], 0\n\t" \
  "global_load_dwordx4 v[148:151], %[voff], s[62:63] offset:1024\n\t" \
  "s_add_u32 s62, %[xslo], 12288\n\t" \
  "s_addc_u32 s63, %[xshi], 0\n\t" \
  "global_load_dwordx4 v[204:207], %[voff], s[62:63] offset:3072\n\t" \
  "s_add_u32 s60, %[gblo], 229376\n\t" \
  "s_addc_u32 s61, %[gbhi], 0\n\t" \
  "s_add_u32 m0, %[ldo], 16384\n\t" \
  "s_nop 0\n\t" \
  "global_load_lds_dwordx4 %[lo0], s[60:61]\n\t" \
  "s_add_u32 s60, %[gblo], 258048\n\t" \
  "s_addc_u32 s61, %[gbhi], 0\n\t" \
  "s_add_u32 m0, %[ldo], 17408\n\t" \
  "s_nop 0\n\t" \
  "global_load_lds_dwordx4 %[lo1], s[60:61]\n\t" \
  "s_add_u32 s60, %[ublo], 229376\n\t" \
  "s_addc_u32 s61, %[ubhi], 0\n\t" \
  "s_add_u32 m0, %[ldo], 24576\n\t" \
  "s_nop 0\n\t" \
  "global_load_lds_dwordx4 %[lo0], s[60:61]\n\t" \
  "s_add_u32 s60, %[ublo], 258048\n\t" \
  "s_addc_u32 s61, %[ubhi], 0\n\t" \
  "s_add_u32 m0, %[ldo], 25600\n\t" \
  "s_nop 0\n\t" \
  "global_load_lds_dwordx4 %[lo1], s[60:61]\n\t" \
  "s_add_u32 s62, %[xslo], 0\n\t" \
  "s_addc_u32 s63, %[xshi], 0\n\t" \
  "global_load_dwordx4 v[152:155], %[voff], s[62:63] offset:2048\n\t" \
  "s_add_u32 s62, %[xslo], 16384\n\t" \
  "s_addc_u32 s63, %[xshi], 0\n\t" \
  "global_load_dwordx4 v[208:211], %[voff], s[62:63] offset:0\n\t" \
  "s_add_u32 s62, %[xslo], 0\n\t" \
  "s_addc_u32 s63, %[xshi], 0\n\t" \
  "global_load_dwordx4 v[156:159], %[voff], s[62:63] offset:3072\n\t" \
  "s_add_u32 s62, %[xslo], 16384\n\t" \
  "s_addc_u32 s63, %[xshi], 0\n\t" \
  "global_load_dwordx4 v[212:215], %[voff], s[62:63] offset:1024\n\t" \
  "s_add_u32 s60, %[gblo], 458752\n\t" \
  "s_addc_u32 s61, %[gbhi], 0\n\t" \
  "s_add_u32 m0, %[ldo], 32768\n\t" \
  "s_nop 0\n\t" \
  "global_load_lds_dwordx4 %[lo0], s[60:61]\n\t" \
  "s_add_u32 s60, %[gblo], 487424\n\t" \
  "s_addc_u32 s61, %[gbhi], 0\n\t" \
  "s_add_u32 m0, %[ldo], 33792\n\t" \
  "s_nop 0\n\t" \
  "global_load_lds_dwordx4 %[lo1], s[60:61]\n\t" \
  "s_add_u32 s60, %[ublo], 458752\n\t" \
  "s_addc_u32 s61, %[ubhi], 0\n\t" \
  "s_add_u32 m0, %[ldo], 40960\n\t" \
  "s_nop 0\n\t" \
  "global_load_lds_dwordx4 %[lo0], s[60:61]\n\t" \
  "s_add_u32 s60, %[ublo], 487424\n\t" \
  "s_addc_u32 s61, %[ubhi], 0\n\t" \
  "s_add_u32 m0, %[ldo], 41984\n\t" \
  "s_nop 0\n\t" \
  "global_load_lds_dwordx4 %[lo1], s[60:61]\n\t" \
  "s_add_u32 s62, %[xslo], 4096\n\t" \
  "s_addc_u32 s63, %[xshi], 0\n\t" \
  "global_load_dwordx4 v[160:163], %[voff], s[62:63] offset:0\n\t" \
  "s_add_u32 s62, %[xslo], 16384\n\t" \
  "s_addc_u32 s63, %[xshi], 0\n\t" \
  "global_load_dwordx4 v[216:219], %[voff], s[62:63] offset:2048\n\t" \
  "s_add_u32 s62, %[xslo], 4096\n\t" \
  "s_addc_u32 s63, %[xshi], 0\n\t" \
  "global_load_dwordx4 v[164:167], %[voff], s[62:63] offset:1024\n\t" \
  "s_add_u32 s62, %[xslo], 16384\n\t" \
  "s_addc_u32 s63, %[xshi], 0\n\t" \
  "global_load_dwordx4 v[220:223], %[voff], s[62:63] offset:3072\n\t" \
  "s_waitcnt vmcnt(16)\n\t" \
  "s_waitcnt lgkmcnt(0)\n\t" \
  "s_mov_b64 s[64:65], exec\n\t" \
  "s_mov_b64 exec, 1\n\t" \
  "ds_add_u32 %[cnt], %[one]\n\t" \
  "s_mov_b64 exec, s[64:65]\n\t" \
  "s_mov_b32 s67, 0x400000\n\t" \
  "1:\n\t" \
  "ds_read_b32 %[vt], %[cnt]\n\t" \
  "s_waitcnt lgkmcnt(0)\n\t" \
  "v_readfirstlane_b32 s66, %[vt]\n\t" \
  "s_nop 3\n\t" \
  "s_cmp_ge_u32 s66, 4\n\t" \
  "s_cbranch_scc1 2f\n\t" \
  "s_sub_u32 s67, s67, 1\n\t" \
  "s_cbranch_scc1 2f\n\t" \
  "s_sleep 1\n\t" \
  "s_branch 1b\n\t" \
  "2:\n\t" \
  "ds_read_b64_tr_b16 v[96:97], %[ad0] offset:0\n\t" \
  "ds_read_b64_tr_b16 v[98:99], %[ad0] offset:512\n\t" \
  "ds_read_b64_tr_b16 v[100:101], %[ad1] offset:0\n\t" \
  "ds_read_b64_tr_b16 v[102:103], %[ad1] offset:512\n\t" \
  "ds_read_b64_tr_b16 v[104:105], %[ad2] offset:0\n\t" \
  "ds_read_b64_tr_b16 v[106:107], %[ad2] offset:512\n\t" \
  "ds_read_b64_tr_b16 v[108:109], %[ad3] offset:0\n\t" \
  "ds_read_b64_tr_b16 v[110:111], %[ad3] offset:512\n\t" \
  "s_waitcnt lgkmcnt(4)\n\t" \
  "ds_read_b64_tr_b16 v[112:113], %[ad0] offset:8192\n\t" \
  "ds_read_b64_tr_b16 v[114:115], %[ad0] offset:8704\n\t" \
  "ds_read_b64_tr_b16 v[116:117], %[ad1] offset:8192\n\t" \
  "ds_read_b64_tr_b16 v[118:119], %[ad1] offset:8704\n\t" \
  "ds_read_b64_tr_b16 v[120:121], %[ad2] offset:8192\n\t" \
  "ds_read_b64_tr_b16 v[122:123], %[ad2] offset:8704\n\t" \
  "ds_read_b64_tr_b16 v[124:125], %[ad3] offset:8192\n\t" \
  "ds_read_b64_tr_b16 v[126:127], %[ad3] offset:8704\n\t" \
  "s_waitcnt lgkmcnt(6)\n\t" \
  "v_mfma_f32_16x16x32_bf16 v[32:35], v[96:99], v[144:147], 0\n\t" \
  "ds_read_b64_tr_b16 v[128:129], %[ad0] offset:4096\n\t" \
  "ds_read_b64_tr_b16 v[130:131], %[ad0] offset:4608\n\t" \
  "v_mfma_f32_16x16x32_bf16 v[64:67], v[96:99], v[200:203], 0\n\t" \
  "s_add_u32 s60, %[gblo], 688128\n\t" \
  "s_addc_u32 s61, %[gbhi], 0\n\t" \
  "s_add_u32 m0, %[ldo], 49152\n\t" \
  "s_nop 0\n\t" \
  "global_load_lds_dwordx4 %[lo0], s[60:61]\n\t" \
  "s_add_u32 s60, %[gblo], 716800\n\t" \
  "s_addc_u32 s61, %[gbhi], 0\n\t" \
  "s_add_u32 m0, %[ldo], 50176\n\t" \
  "s_nop 0\n\t" \
  "global_load_lds_dwordx4 %[lo1], s[60:61]\n\t" \
  "v_mfma_f32_16x16x32_bf16 v[36:39], v[100:103], v[144:147], 0\n\t" \
  "ds_read_b64_tr_b16 v[132:133], %[ad1] offset:4096\n\t" \
  "ds_read_b64_tr_b16 v[134:135], %[ad1] offset:4608\n\t" \
  "v_mfma_f32_16x16x32_bf16 v[68:71], v[100:103], v[200:203], 0\n\t" \
  "s_add_u32 s60, %[ublo], 688128\n\t" \
  "s_addc_u32 s61, %[ubhi], 0\n\t" \
  "s_add_u32 m0, %[ldo], 57344\n\t" \
  "s_nop 0\n\t" \
  "global_load_lds_dwordx4 %[lo0], s[60:61]\n\t" \
  "s_add_u32 s60, %[ublo], 716800\n\t" \
  "s_addc_u32 s61, %[ubhi], 0\n\t" \
  "s_add_u32 m0, %[ldo], 58368\n\t" \
  "s_nop 0\n\t" \
  "global_load_lds_dwordx4 %[lo1], s[60:61]\n\t" \
  "v_mfma_f32_16x16x32_bf16 v[40:43], v[104:107], v[144:147], 0\n\t" \
  "ds_read_b64_tr_b16 v[136:137], %[ad2] offset:4096\n\t" \
  "ds_read_b64_tr_b16 v[138:139], %[ad2] offset:4608\n\t" \
  "v_mfma_f32_16x16x32_bf16 v[72:75], v[104:107], v[200:203], 0\n\t" \
  "s_add_u32 s62, %[xslo], 4096\n\t" \
  "s_addc_u32 s63, %[xshi], 0\n\t" \
  "global_load_dwordx4 v[168:171], %[voff], s[62:63] offset:2048\n\t" \
  "s_add_u32 s62, %[xslo], 20480\n\t" \
  "s_addc_u32 s63, %[xshi], 0\n\t" \
  "global_load_dwordx4 v[224:227], %[voff], s[62:63] offset:0\n\t" \
  "v_mfma_f32_16x16x32_bf16 v[44:47], v[108:111], v[144:147], 0\n\t" \
  "ds_read_b64_tr_b16 v[140:141], %[ad3] offset:4096\n\t" \
  "ds_read_b64_tr_b16 v[142:143], %[ad3] offset:4608\n\t" \
  "v_mfma_f32_16x16x32_bf16 v[76:79], v[108:111], v[200:203], 0\n\t" \
  "s_add_u32 s62, %[xslo], 4096\n\t" \
  "s_addc_u32 s63, %[xshi], 0\n\t" \
  "global_load_dwordx4 v[172:175], %[voff], s[62:63] offset:3072\n\t" \
  "s_add_u32 s62, %[xslo], 20480\n\t" \
  "s_addc_u32 s63, %[xshi], 0\n\t" \
  "global_load_dwordx4 v[228:231], %[voff], s[62:63] offset:1024\n\t" \
  "s_waitcnt lgkmcnt(6)\n\t" \
  "v_mfma_f32_16x16x32_bf16 v[48:51], v[112:115], v[144:147], 0\n\t" \
  "ds_read_b64_tr_b16 v[96:97], %[ad0] offset:12288\n\t" \
  "ds_read_b64_tr_b16 v[98:99], %[ad0] offset:12800\n\t" \
  "v_mfma_f32_16x16x32_bf16 v[80:83], v[112:115], v[200:203], 0\n\t" \
  "v_mfma_f32_16x16x32_bf16 v[52:55], v[116:119], v[144:147], 0\n\t" \
  "ds_read_b64_tr_b16 v[100:101], %[ad1] offset:12288\n\t" \
  "ds_read_b64_tr_b16 v[102:103], %[ad1] offset:12800\n\t" \
  "v_mfma_f32_16x16x32_bf16 v[84:87], v[116:119], v[200:203], 0\n\t" \
  "v_mfma_f32_16x16x32_bf16 v[56:59], v[120:123], v[144:147], 0\n\t" \
  "ds_read_b64_tr_b16 v[104:105], %[ad2] offset:12288\n\t" \
  "ds_read_b64_tr_b16 v[106:107], %[ad2] offset:12800\n\t" \
  "v_mfma_f32_16x16x32_bf16 v[88:91], v[120:123], v[200:203], 0\n\t" \
  "v_mfma_f32_16x16x32_bf16 v[60:63], v[124:127], v[144:147], 0\n\t" \
  "ds_read_b64_tr_b16 v[108:109], %[ad3] offset:12288\n\t" \
  "ds_read_b64_tr_b16 v[110:111], %[ad3] offset:12800\n\t" \
  "v_mfma_f32_16x16x32_bf16 v[92:95], v[124:127], v[200:203], 0\n\t" \
  "s_waitcnt vmcnt(16)\n\t" \
  "s_waitcnt lgkmcnt(0)\n\t" \
  "s_mov_b64 s[64:65], exec\n\t" \
  "s_mov_b64 exec, 1\n\t" \
  "ds_add_u32 %[cnt], %[one]\n\t" \
  "s_mov_b64 exec, s[64:65]\n\t" \
  "s_mov_b32 s67, 0x400000\n\t" \
  "1:\n\t" \
  "ds_read_b32 %[vt], %[cnt]\n\t" \
  "s_waitcnt lgkmcnt(0)\n\t" \
  "v_readfirstlane_b32 s66, %[vt]\n\t" \
  "s_nop 3\n\t" \
  "s_cmp_ge_u32 s66, 8\n\t" \
  "s_cbranch_scc1 2f\n\t" \
  "s_sub_u32 s67, s67, 1\n\t" \
  "s_cbranch_scc1 2f\n\t" \
  "s_sleep 1\n\t" \
  "s_branch 1b\n\t" \
  "2:\n\t" \
  "s_waitcnt lgkmcnt(6)\n\t" \
  "v_mfma_f32_16x16x32_bf16 v[32:35], v[128:131], v[148:151], v[32:35]\n\t" \
  "ds_read_b64_tr_b16 v[112:113], %[ad0] offset:16384\n\t" \
  "ds_read_b64_tr_b16 v[114:115], %[ad0] offset:16896\n\t" \
  "v_mfma_f32_16x16x32_bf16 v[64:67], v[128:131], v[204:207], v[64:67]\n\t" \
  "v_mfma_f32_16x16x32_bf16 v[36:39], v[132:135], v[148:151], v[36:39]\n\t" \
  "ds_read_b64_tr_b16 v[116:117], %[ad1] offset:16384\n\t" \
  "ds_read_b64_tr_b16 v[118:119], %[ad1] offset:16896\n\t" \
  "v_mfma_f32_16x16x32_bf16 v[68:71], v[132:135], v[204:207], v[68:71]\n\t" \
  "v_mfma_f32_16x16x32_bf16 v[40:43], v[136:139], v[148:151], v[40:43]\n\t" \
  "ds_read_b64_tr_b16 v[120:121], %[ad2] offset:16384\n\t" \
  "ds_read_b64_tr_b16 v[122:123], %[ad2] offset:16896\n\t" \
  "v_mfma_f32_16x16x32_bf16 v[72:75], v[136:139], v[204:207], v[72:75]\n\t" \
  "v_mfma_f32_16x16x32_bf16 v[44:47], v[140:143], v[148:151], v[44:47]\n\t" \
  "ds_read_b64_tr_b16 v[124:125], %[ad3] offset:16384\n\t" \
  "ds_read_b64_tr_b16 v[126:127], %[ad3] offset:16896\n\t" \
  "v_mfma_f32_16x16x32_bf16 v[76:79], v[140:143], v[204:207], v[76:79]\n\t" \
  "s_waitcnt lgkmcnt(6)\n\t" \
  "v_mfma_f32_16x16x32_bf16 v[48:51], v[96:99], v[148:151], v[48:51]\n\t" \
  "ds_read_b64_tr_b16 v[128:129], %[ad0] offset:24576\n\t" \
  "ds_read_b64_tr_b16 v[130:131], %[ad0] offset:25088\n\t" \
  "v_mfma_f32_16x16x32_bf16 v[80:83], v[96:99], v[204:207], v[80:83]\n\t" \
  "v_mfma_f32_16x16x32_bf16 v[52:55], v[100:103], v[148:151], v[52:55]\n\t" \
  "ds_read_b64_tr_b16 v[132:133], %[ad1] offset:24576\n\t" \
  "ds_read_b64_tr_b16 v[134:135], %[ad1] offset:25088\n\t" \
  "v_mfma_f32_16x16x32_bf16 v[84:87], v[100:103], v[204:207], v[84:87]\n\t" \
  "v_mfma_f32_16x16x32_bf16 v[56:59], v[104:107], v[148:151], v[56:59]\n\t" \
  "ds_read_b64_tr_b16 v[136:137], %[ad2] offset:24576\n\t" \
  "ds_read_b64_tr_b16 v[138:139], %[ad2] offset:25088\n\t" \
  "v_mfma_f32_16x16x32_bf16 v[88:91], v[104:107], v[204:207], v[88:91]\n\t" \
  "v_mfma_f32_16x16x32_bf16 v[60:63], v[108:111], v[148:151], v[60:63]\n\t" \
  "ds_read_b64_tr_b16 v[140:141], %[ad3] offset:24576\n\t" \
  "ds_read_b64_tr_b16 v[142:143], %[ad3] offset:25088\n\t" \
  "v_mfma_f32_16x16x32_bf16 v[92:95], v[108:111], v[204:207], v[92:95]\n\t" \
  "s_waitcnt lgkmcnt(6)\n\t" \
  "v_mfma_f32_16x16x32_bf16 v[32:35], v[112:115], v[152:155], v[32:35]\n\t" \
  "ds_read_b64_tr_b16 v[96:97], %[ad0] offset:20480\n\t" \
  "ds_read_b64_tr_b16 v[98:99], %[ad0] offset:20992\n\t" \
  "v_mfma_f32_16x16x32_bf16 v[64:67], v[112:115], v[208:211], v[64:67]\n\t" \
  "s_add_u32 s60, %[gblo], 917504\n\t" \
  "s_addc_u32 s61, %[gbhi], 0\n\t" \
  "s_add_u32 m0, %[ldo], 65536\n\t" \
  "s_nop 0\n\t" \
  "global_load_lds_dwordx4 %[lo0], s[60:61]\n\t" \
  "s_add_u32 s60, %[gblo], 946176\n\t" \
  "s_addc_u32 s61, %[gbhi], 0\n\t" \
  "s_add_u32 m0, %[ldo], 66560\n\t" \
  "s_nop 0\n\t" \
  "global_load_lds_dwordx4 %[lo1], s[60:61]\n\t" \
  "v_mfma_f32_16x16x32_bf16 v[36:39], v[116:119], v[152:155], v[36:39]\n\t" \
  "ds_read_b64_tr_b16 v[100:101], %[ad1] offset:20480\n\t" \
  "ds_read_b64_tr_b16 v[102:103], %[ad1] offset:20992\n\t" \
  "v_mfma_f32_16x16x32_bf16 v[68:71], v[116:119], v[208:211], v[68:71]\n\t" \
  "s_add_u32 s60, %[ublo], 917504\n\t" \
  "s_addc_u32 s61, %[ubhi], 0\n\t" \
  "s_add_u32 m0, %[ldo], 73728\n\t" \
  "s_nop 0\n\t" \
  "global_load_lds_dwordx4 %[lo0], s[60:61]\n\t" \
  "s_add_u32 s60, %[ublo], 946176\n\t" \
  "s_addc_u32 s61, %[ubhi], 0\n\t" \
  "s_add_u32 m0, %[ldo], 74752\n\t" \
  "s_nop 0\n\t" \
  "global_load_lds_dwordx4 %[lo1], s[60:61]\n\t" \
  "v_mfma_f32_16x16x32_bf16 v[40:43], v[120:123], v[152:155], v[40:43]\n\t" \
  "ds_read_b64_tr_b16 v[104:105], %[ad2] offset:20480\n\t" \
  "ds_read_b64_tr_b16 v[106:107], %[ad2] offset:20992\n\t" \
  "v_mfma_f32_16x16x32_bf16 v[72:75], v[120:123], v[208:211], v[72:75]\n\t" \
  "s_add_u32 s62, %[xslo], 8192\n\t" \
  "s_addc_u32 s63, %[xshi], 0\n\t" \
  "global_load_dwordx4 v[176:179], %[voff], s[62:63] offset:0\n\t" \
  "s_add_u32 s62, %[xslo], 20480\n\t" \
  "s_addc_u32 s63, %[xshi], 0\n\t" \
  "global_load_dwordx4 v[232:235], %[voff], s[62:63] offset:2048\n\t" \
  "v_mfma_f32_16x16x32_bf16 v[44:47], v[124:127], v[152:155], v[44:47]\n\t" \
  "ds_read_b64_tr_b16 v[108:109], %[ad3] offset:20480\n\t" \
  "ds_read_b64_tr_b16 v[110:111], %[ad3] offset:20992\n\t" \
  "v_mfma_f32_16x16x32_bf16 v[76:79], v[124:127], v[208:211], v[76:79]\n\t" \
  "s_add_u32 s62, %[xslo], 8192\n\t" \
  "s_addc_u32 s63, %[xshi], 0\n\t" \
  "global_load_dwordx4 v[180:183], %[voff], s[62:63] offset:1024\n\t" \
  "s_add_u32 s62, %[xslo], 20480\n\t" \
  "s_addc_u32 s63, %[xshi], 0\n\t" \
  "global_load_dwordx4 v[236:239], %[voff], s[62:63] offset:3072\n\t" \
  "s_waitcnt lgkmcnt(6)\n\t" \
  "v_mfma_f32_16x16x32_bf16 v[48:51], v[128:131], v[152:155], v[48:51]\n\t" \
  "ds_read_b64_tr_b16 v[112:113], %[ad0] offset:28672\n\t" \
  "ds_read_b64_tr_b16 v[114:115], %[ad0] offset:29184\n\t" \
  "v_mfma_f32_16x16x32_bf16 v[80:83], v[128:131], v[208:211], v[80:83]\n\t" \
  "v_mfma_f32_16x16x32_bf16 v[52:55], v[132:135], v[152:155], v[52:55]\n\t" \
  "ds_read_b64_tr_b16 v[116:117], %[ad1] offset:28672\n\t" \
  "ds_read_b64_tr_b16 v[118:119], %[ad1] offset:29184\n\t" \
  "v_mfma_f32_16x16x32_bf16 v[84:87], v[132:135], v[208:211], v[84:87]\n\t" \
  "v_mfma_f32_16x16x32_bf16 v[56:59], v[136:139], v[152:155], v[56:59]\n\t" \
  "ds_read_b64_tr_b16 v[120:121], %[ad2] offset:28672\n\t" \
  "ds_read_b64_tr_b16 v[122:123], %[ad2] offset:29184\n\t" \
  "v_mfma_f32_16x16x32_bf16 v[88:91], v[136:139], v[208:211], v[88:91]\n\t" \
  "v_mfma_f32_16x16x32_bf16 v[60:63], v[140:143], v[152:155], v[60:63]\n\t" \
  "ds_read_b64_tr_b16 v[124:125], %[ad3] offset:28672\n\t" \
  "ds_read_b64_tr_b16 v[126:127], %[ad3] offset:29184\n\t" \
  "v_mfma_f32_16x16x32_bf16 v[92:95], v[140:143], v[208:211], v[92:95]\n\t" \
  "s_waitcnt vmcnt(16)\n\t" \
  "s_waitcnt lgkmcnt(0)\n\t" \
  "s_mov_b64 s[64:65], exec\n\t" \
  "s_mov_b64 exec, 1\n\t" \
  "ds_add_u32 %[cnt], %[one]\n\t" \
  "s_mov_b64 exec, s[64:65]\n\t" \
  "s_mov_b32 s67, 0x400000\n\t" \
  "1:\n\t" \
  "ds_read_b32 %[vt], %[cnt]\n\t" \
  "s_waitcnt lgkmcnt(0)\n\t" \
  "v_readfirstlane_b32 s66, %[vt]\n\t" \
  "s_nop 3\n\t" \
  "s_cmp_ge_u32 s66, 12\n\t" \
  "s_cbranch_scc1 2f\n\t" \
  "s_sub_u32 s67, s67, 1\n\t" \
  "s_cbranch_scc1 2f\n\t" \
  "s_sleep 1\n\t" \
  "s_branch 1b\n\t" \
  "2:\n\t" \
  "s_waitcnt lgkmcnt(6)\n\t" \
  "v_mfma_f32_16x16x32_bf16 v[32:35], v[96:99], v[156:159], v[32:35]\n\t" \
  "ds_read_b64_tr_b16 v[128:129], %[ad0] offset:32768\n\t" \
  "ds_read_b64_tr_b16 v[130:131], %[ad0] offset:33280\n\t" \
  "v_mfma_f32_16x16x32_bf16 v[64:67], v[96:99], v[212:215], v[64:67]\n\t" \
  "v_mfma_f32_16x16x32_bf16 v[36:39], v[100:103], v[156:159], v[36:39]\n\t" \
  "ds_read_b64_tr_b16 v[132:133], %[ad1] offset:32768\n\t" \
  "ds_read_b64_tr_b16 v[134:135], %[ad1] offset:33280\n\t" \
  "v_mfma_f32_16x16x32_bf16 v[68:71], v[100:103], v[212:215], v[68:71]\n\t" \
  "v_mfma_f32_16x16x32_bf16 v[40:43], v[104:107], v[156:159], v[40:43]\n\t" \
  "ds_read_b64_tr_b16 v[136:137], %[ad2] offset:32768\n\t" \
  "ds_read_b64_tr_b16 v[138:139], %[ad2] offset:33280\n\t" \
  "v_mfma_f32_16x16x32_bf16 v[72:75], v[104:107], v[212:215], v[72:75]\n\t" \
  "v_mfma_f32_16x16x32_bf16 v[44:47], v[108:111], v[156:159], v[44:47]\n\t" \
  "ds_read_b64_tr_b16 v[140:141], %[ad3] offset:32768\n\t" \
  "ds_read_b64_tr_b16 v[142:143], %[ad3] offset:33280\n\t" \
  "v_mfma_f32_16x16x32_bf16 v[76:79], v[108:111], v[212:215], v[76:79]\n\t" \
  "s_waitcnt lgkmcnt(6)\n\t" \
  "v_mfma_f32_16x16x32_bf16 v[48:51], v[112:115], v[156:159], v[48:51]\n\t" \
  "ds_read_b64_tr_b16 v[96:97], %[ad0] offset:40960\n\t" \
  "ds_read_b64_tr_b16 v[98:99], %[ad0] offset:41472\n\t" \
  "v_mfma_f32_16x16x32_bf16 v[80:83], v[112:115], v[212:215], v[80:83]\n\t" \
  "v_mfma_f32_16x16x32_bf16 v[52:55], v[116:119], v[156:159], v[52:55]\n\t" \
  "ds_read_b64_tr_b16 v[100:101], %[ad1] offset:40960\n\t" \
  "ds_read_b64_tr_b16 v[102:103], %[ad1] offset:41472\n\t" \
  "v_mfma_f32_16x16x32_bf16 v[84:87], v[116:119], v[212:215], v[84:87]\n\t" \
  "v_mfma_f32_16x16x32_bf16 v[56:59], v[120:123], v[156:159], v[56:59]\n\t" \
  "ds_read_b64_tr_b16 v[104:105], %[ad2] offset:40960\n\t" \
  "ds_read_b64_tr_b16 v[106:107], %[ad2] offset:41472\n\t" \
  "v_mfma_f32_16x16x32_bf16 v[88:91], v[120:123], v[212:215], v[88:91]\n\t" \
  "v_mfma_f32_16x16x32_bf16 v[60:63], v[124:127], v[156:159], v[60:63]\n\t" \
  "ds_read_b64_tr_b16 v[108:109], %[ad3] offset:40960\n\t" \
  "ds_read_b64_tr_b16 v[110:111], %[ad3] offset:41472\n\t" \
  "v_mfma_f32_16x16x32_bf16 v[92:95], v[124:127], v[212:215], v[92:95]\n\t" \
  "s_waitcnt lgkmcnt(6)\n\t" \
  "v_mfma_f32_16x16x32_bf16 v[32:35], v[128:131], v[160:163], v[32:35]\n\t" \
  "ds_read_b64_tr_b16 v[112:113], %[ad0] offset:36864\n\t" \
  "ds_read_b64_tr_b16 v[114:115], %[ad0] offset:37376\n\t" \
  "v_mfma_f32_16x16x32_bf16 v[64:67], v[128:131], v[216:219], v[64:67]\n\t" \
  "s_add_u32 s60, %[gblo], 1146880\n\t" \
  "s_addc_u32 s61, %[gbhi], 0\n\t" \
  "s_add_u32 m0, %[ldo], 81920\n\t" \
  "s_nop 0\n\t" \
  "global_load_lds_dwordx4 %[lo0], s[60:61]\n\t" \
  "s_add_u32 s60, %[gblo], 1175552\n\t" \
  "s_addc_u32 s61, %[gbhi], 0\n\t" \
  "s_add_u32 m0, %[ldo], 82944\n\t" \
  "s_nop 0\n\t" \
  "global_load_lds_dwordx4 %[lo1], s[60:61]\n\t" \
  "v_mfma_f32_16x16x32_bf16 v[36:39], v[132:135], v[160:163], v[36:39]\n\t" \
  "ds_read_b64_tr_b16 v[116:117], %[ad1] offset:36864\n\t" \
  "ds_read_b64_tr_b16 v[118:119], %[ad1] offset:37376\n\t" \
  "v_mfma_f32_16x16x32_bf16 v[68:71], v[132:135], v[216:219], v[68:71]\n\t" \
  "s_add_u32 s60, %[ublo], 1146880\n\t" \
  "s_addc_u32 s61, %[ubhi], 0\n\t" \
  "s_add_u32 m0, %[ldo], 90112\n\t" \
  "s_nop 0\n\t" \
  "global_load_lds_dwordx4 %[lo0], s[60:61]\n\t" \
  "s_add_u32 s60, %[ublo], 1175552\n\t" \
  "s_addc_u32 s61, %[ubhi], 0\n\t" \
  "s_add_u32 m0, %[ldo], 91136\n\t" \
  "s_nop 0\n\t" \
  "global_load_lds_dwordx4 %[lo1], s[60:61]\n\t" \
  "v_mfma_f32_16x16x32_bf16 v[40:43], v[136:139], v[160:163], v[40:43]\n\t" \
  "ds_read_b64_tr_b16 v[120:121], %[ad2] offset:36864\n\t" \
  "ds_read_b64_tr_b16 v[122:123], %[ad2] offset:37376\n\t" \
  "v_mfma_f32_16x16x32_bf16 v[72:75], v[136:139], v[216:219], v[72:75]\n\t" \
  "s_add_u32 s62, %[xslo], 8192\n\t" \
  "s_addc_u32 s63, %[xshi], 0\n\t" \
  "global_load_dwordx4 v[184:187], %[voff], s[62:63] offset:2048\n\t" \
  "s_add_u32 s62, %[xslo], 24576\n\t" \
  "s_addc_u32 s63, %[xshi], 0\n\t" \
  "global_load_dwordx4 v[240:243], %[voff], s[62:63] offset:0\n\t" \
  "v_mfma_f32_16x16x32_bf16 v[44:47], v[140:143], v[160:163], v[44:47]\n\t" \
  "ds_read_b64_tr_b16 v[124:125], %[ad3] offset:36864\n\t" \
  "ds_read_b64_tr_b16 v[126:127], %[ad3] offset:37376\n\t" \
  "v_mfma_f32_16x16x32_bf16 v[76:79], v[140:143], v[216:219], v[76:79]\n\t" \
  "s_add_u32 s62, %[xslo], 8192\n\t" \
  "s_addc_u32 s63, %[xshi], 0\n\t" \
  "global_load_dwordx4 v[188:191], %[voff], s[62:63] offset:3072\n\t" \
  "s_add_u32 s62, %[xslo], 24576\n\t" \
  "s_addc_u32 s63, %[xshi], 0\n\t" \
  "global_load_dwordx4 v[244:247], %[voff], s[62:63] offset:1024\n\t" \
  "s_waitcnt lgkmcnt(6)\n\t" \
  "v_mfma_f32_16x16x32_bf16 v[48:51], v[96:99], v[160:163], v[48:51]\n\t" \
  "ds_read_b64_tr_b16 v[128:129], %[ad0] offset:45056\n\t" \
  "ds_read_b64_tr_b16 v[130:131], %[ad0] offset:45568\n\t" \
  "v_mfma_f32_16x16x32_bf16 v[80:83], v[96:99], v[216:219], v[80:83]\n\t" \
  "v_mfma_f32_16x16x32_bf16 v[52:55], v[100:103], v[160:163], v[52:55]\n\t" \
  "ds_read_b64_tr_b16 v[132:133], %[ad1] offset:45056\n\t" \
  "ds_read_b64_tr_b16 v[134:135], %[ad1] offset:45568\n\t" \
  "v_mfma_f32_16x16x32_bf16 v[84:87], v[100:103], v[216:219], v[84:87]\n\t" \
  "v_mfma_f32_16x16x32_bf16 v[56:59], v[104:107], v[160:163], v[56:59]\n\t" \
  "ds_read_b64_tr_b16 v[136:137], %[ad2] offset:45056\n\t" \
  "ds_read_b64_tr_b16 v[138:139], %[ad2] offset:45568\n\t" \
  "v_mfma_f32_16x16x32_bf16 v[88:91], v[104:107], v[216:219], v[88:91]\n\t" \
  "v_mfma_f32_16x16x32_bf16 v[60:63], v[108:111], v[160:163], v[60:63]\n\t" \
  "ds_read_b64_tr_b16 v[140:141], %[ad3] offset:45056\n\t" \
  "ds_read_b64_tr_b16 v[142:143], %[ad3] offset:45568\n\t" \
  "v_mfma_f32_16x16x32_bf16 v[92:95], v[108:111], v[216:219], v[92:95]\n\t" \
  "s_waitcnt vmcnt(16)\n\t" \
  "s_waitcnt lgkmcnt(0)\n\t" \
  "s_mov_b64 s[64:65], exec\n\t" \
  "s_mov_b64 exec, 1\n\t" \
  "ds_add_u32 %[cnt], %[one]\n\t" \
  "s_mov_b64 exec, s[64:65]\n\t" \
  "s_mov_b32 s67, 0x400000\n\t" \
  "1:\n\t" \
  "ds_read_b32 %[vt], %[cnt]\n\t" \
  "s_waitcnt lgkmcnt(0)\n\t" \
  "v_readfirstlane_b32 s66, %[vt]\n\t" \
  "s_nop 3\n\t" \
  "s_cmp_ge_u32 s66, 16\n\t" \
  "s_cbranch_scc1 2f\n\t" \
  "s_sub_u32 s67, s67, 1\n\t" \
  "s_cbranch_scc1 2f\n\t" \
  "s_sleep 1\n\t" \
  "s_branch 1b\n\t" \
  "2:\n\t" \
  "s_waitcnt lgkmcnt(6)\n\t" \
  "v_mfma_f32_16x16x32_bf16 v[32:35], v[112:115], v[164:167], v[32:35]\n\t" \
  "ds_read_b64_tr_b16 v[96:97], %[ad0] offset:49152\n\t" \
  "ds_read_b64_tr_b16 v[98:99], %[ad0] offset:49664\n\t" \
  "v_mfma_f32_16x16x32_bf16 v[64:67], v[112:115], v[220:223], v[64:67]\n\t" \
  "v_mfma_f32_16x16x32_bf16 v[36:39], v[116:119], v[164:167], v[36:39]\n\t" \
  "ds_read_b64_tr_b16 v[100:101], %[ad1] offset:49152\n\t" \
  "ds_read_b64_tr_b16 v[102:103], %[ad1] offset:49664\n\t" \
  "v_mfma_f32_16x16x32_bf16 v[68:71], v[116:119], v[220:223], v[68:71]\n\t" \
  "v_mfma_f32_16x16x32_bf16 v[40:43], v[120:123], v[164:167], v[40:43]\n\t" \
  "ds_read_b64_tr_b16 v[104:105], %[ad2] offset:49152\n\t" \
  "ds_read_b64_tr_b16 v[106:107], %[ad2] offset:49664\n\t" \
  "v_mfma_f32_16x16x32_bf16 v[72:75], v[120:123], v[220:223], v[72:75]\n\t" \
  "v_mfma_f32_16x16x32_bf16 v[44:47], v[124:127], v[164:167], v[44:47]\n\t" \
  "ds_read_b64_tr_b16 v[108:109], %[ad3] offset:49152\n\t" \
  "ds_read_b64_tr_b16 v[110:111], %[ad3] offset:49664\n\t" \
  "v_mfma_f32_16x16x32_bf16 v[76:79], v[124:127], v[220:223], v[76:79]\n\t" \
  "s_waitcnt lgkmcnt(6)\n\t" \
  "v_mfma_f32_16x16x32_bf16 v[48:51], v[128:131], v[164:167], v[48:51]\n\t" \
  "ds_read_b64_tr_b16 v[112:113], %[ad0] offset:57344\n\t" \
  "ds_read_b64_tr_b16 v[114:115], %[ad0] offset:57856\n\t" \
  "v_mfma_f32_16x16x32_bf16 v[80:83], v[128:131], v[220:223], v[80:83]\n\t" \
  "v_mfma_f32_16x16x32_bf16 v[52:55], v[132:135], v[164:167], v[52:55]\n\t" \
  "ds_read_b64_tr_b16 v[116:117], %[ad1] offset:57344\n\t" \
  "ds_read_b64_tr_b16 v[118:119], %[ad1] offset:57856\n\t" \
  "v_mfma_f32_16x16x32_bf16 v[84:87], v[132:135], v[220:223], v[84:87]\n\t" \
  "v_mfma_f32_16x16x32_bf16 v[56:59], v[136:139], v[164:167], v[56:59]\n\t" \
  "ds_read_b64_tr_b16 v[120:121], %[ad2] offset:57344\n\t" \
  "ds_read_b64_tr_b16 v[122:123], %[ad2] offset:57856\n\t" \
  "v_mfma_f32_16x16x32_bf16 v[88:91], v[136:139], v[220:223], v[88:91]\n\t" \
  "v_mfma_f32_16x16x32_bf16 v[60:63], v[140:143], v[164:167], v[60:63]\n\t" \
  "ds_read_b64_tr_b16 v[124:125], %[ad3] offset:57344\n\t" \
  "ds_read_b64_tr_b16 v[126:127], %[ad3] offset:57856\n\t" \
  "v_mfma_f32_16x16x32_bf16 v[92:95], v[140:143], v[220:223], v[92:95]\n\t" \
  "s_waitcnt lgkmcnt(6)\n\t" \
  "v_mfma_f32_16x16x32_bf16 v[32:35], v[96:99], v[168:171], v[32:35]\n\t" \
  "ds_read_b64_tr_b16 v[128:129], %[ad0] offset:53248\n\t" \
  "ds_read_b64_tr_b16 v[130:131], %[ad0] offset:53760\n\t" \
  "v_mfma_f32_16x16x32_bf16 v[64:67], v[96:99], v[224:227], v[64:67]\n\t" \
  "s_add_u32 s60, %[gblo], 1376256\n\t" \
  "s_addc_u32 s61, %[gbhi], 0\n\t" \
  "s_add_u32 m0, %[ldo], 98304\n\t" \
  "s_nop 0\n\t" \
  "global_load_lds_dwordx4 %[lo0], s[60:61]\n\t" \
  "s_add_u32 s60, %[gblo], 1404928\n\t" \
  "s_addc_u32 s61, %[gbhi], 0\n\t" \
  "s_add_u32 m0, %[ldo], 99328\n\t" \
  "s_nop 0\n\t" \
  "global_load_lds_dwordx4 %[lo1], s[60:61]\n\t" \
  "v_mfma_f32_16x16x32_bf16 v[36:39], v[100:103], v[168:171], v[36:39]\n\t" \
  "ds_read_b64_tr_b16 v[132:133], %[ad1] offset:53248\n\t" \
  "ds_read_b64_tr_b16 v[134:135], %[ad1] offset:53760\n\t" \
  "v_mfma_f32_16x16x32_bf16 v[68:71], v[100:103], v[224:227], v[68:71]\n\t" \
  "s_add_u32 s60, %[ublo], 1376256\n\t" \
  "s_addc_u32 s61, %[ubhi], 0\n\t" \
  "s_add_u32 m0, %[ldo], 106496\n\t" \
  "s_nop 0\n\t" \
  "global_load_lds_dwordx4 %[lo0], s[60:61]\n\t" \
  "s_add_u32 s60, %[ublo], 1404928\n\t" \
  "s_addc_u32 s61, %[ubhi], 0\n\t" \
  "s_add_u32 m0, %[ldo], 107520\n\t" \
  "s_nop 0\n\t" \
  "global_load_lds_dwordx4 %[lo1], s[60:61]\n\t" \
  "v_mfma_f32_16x16x32_bf16 v[40:43], v[104:107], v[168:171], v[40:43]\n\t" \
  "ds_read_b64_tr_b16 v[136:137], %[ad2] offset:53248\n\t" \
  "ds_read_b64_tr_b16 v[138:139], %[ad2] offset:53760\n\t" \
  "v_mfma_f32_16x16x32_bf16 v[72:75], v[104:107], v[224:227], v[72:75]\n\t" \
  "s_add_u32 s62, %[xslo], 12288\n\t" \
  "s_addc_u32 s63, %[xshi], 0\n\t" \
  "global_load_dwordx4 v[192:195], %[voff], s[62:63] offset:0\n\t" \
  "s_add_u32 s62, %[xslo], 24576\n\t" \
  "s_addc_u32 s63, %[xshi], 0\n\t" \
  "global_load_dwordx4 v[248:251], %[voff], s[62:63] offset:2048\n\t" \
  "v_mfma_f32_16x16x32_bf16 v[44:47], v[108:111], v[168:171], v[44:47]\n\t" \
  "ds_read_b64_tr_b16 v[140:141], %[ad3] offset:53248\n\t" \
  "ds_read_b64_tr_b16 v[142:143], %[ad3] offset:53760\n\t" \
  "v_mfma_f32_16x16x32_bf16 v[76:79], v[108:111], v[224:227], v[76:79]\n\t" \
  "s_add_u32 s62, %[xslo], 12288\n\t" \
  "s_addc_u32 s63, %[xshi], 0\n\t" \
  "global_load_dwordx4 v[196:199], %[voff], s[62:63] offset:1024\n\t" \
  "s_add_u32 s62, %[xslo], 24576\n\t" \
  "s_addc_u32 s63, %[xshi], 0\n\t" \
  "global_load_dwordx4 v[252:255], %[voff], s[62:63] offset:3072\n\t" \
  "s_waitcnt lgkmcnt(6)\n\t" \
  "v_mfma_f32_16x16x32_bf16 v[48:51], v[112:115], v[168:171], v[48:51]\n\t" \
  "ds_read_b64_tr_b16 v[96:97], %[ad0] offset:61440\n\t" \
  "ds_read_b64_tr_b16 v[98:99], %[ad0] offset:61952\n\t" \
  "v_mfma_f32_16x16x32_bf16 v[80:83], v[112:115], v[224:227], v[80:83]\n\t" \
  "v_mfma_f32_16x16x32_bf16 v[52:55], v[116:119], v[168:171], v[52:55]\n\t" \
  "ds_read_b64_tr_b16 v[100:101], %[ad1] offset:61440\n\t" \
  "ds_read_b64_tr_b16 v[102:103], %[ad1] offset:61952\n\t" \
  "v_mfma_f32_16x16x32_bf16 v[84:87], v[116:119], v[224:227], v[84:87]\n\t" \
  "v_mfma_f32_16x16x32_bf16 v[56:59], v[120:123], v[168:171], v[56:59]\n\t" \
  "ds_read_b64_tr_b16 v[104:105], %[ad2] offset:61440\n\t" \
  "ds_read_b64_tr_b16 v[106:107], %[ad2] offset:61952\n\t" \
  "v_mfma_f32_16x16x32_bf16 v[88:91], v[120:123], v[224:227], v[88:91]\n\t" \
  "v_mfma_f32_16x16x32_bf16 v[60:63], v[124:127], v[168:171], v[60:63]\n\t" \
  "ds_read_b64_tr_b16 v[108:109], %[ad3] offset:61440\n\t" \
  "ds_read_b64_tr_b16 v[110:111], %[ad3] offset:61952\n\t" \
  "v_mfma_f32_16x16x32_bf16 v[92:95], v[124:127], v[224:227], v[92:95]\n\t" \
  "s_waitcnt vmcnt(16)\n\t" \
  "s_waitcnt lgkmcnt(0)\n\t" \
  "s_mov_b64 s[64:65], exec\n\t" \
  "s_mov_b64 exec, 1\n\t" \
  "ds_add_u32 %[cnt], %[one]\n\t" \
  "s_mov_b64 exec, s[64:65]\n\t" \
  "s_mov_b32 s67, 0x400000\n\t" \
  "1:\n\t" \
  "ds_read_b32 %[vt], %[cnt]\n\t" \
  "s_waitcnt lgkmcnt(0)\n\t" \
  "v_readfirstlane_b32 s66, %[vt]\n\t" \
  "s_nop 3\n\t" \
  "s_cmp_ge_u32 s66, 20\n\t" \
  "s_cbranch_scc1 2f\n\t" \
  "s_sub_u32 s67, s67, 1\n\t" \
  "s_cbranch_scc1 2f\n\t" \
  "s_sleep 1\n\t" \
  "s_branch 1b\n\t" \
  "2:\n\t" \
  "s_waitcnt lgkmcnt(6)\n\t" \
  "v_add_u32 %[ad0], 0x10000, %[ad0]\n\t" \
  "v_add_u32 %[ad1], 0x10000, %[ad1]\n\t" \
  "v_add_u32 %[ad2], 0x10000, %[ad2]\n\t" \
  "v_add_u32 %[ad3], 0x10000, %[ad3]\n\t" \
  "v_mfma_f32_16x16x32_bf16 v[32:35], v[128:131], v[172:175], v[32:35]\n\t" \
  "ds_read_b64_tr_b16 v[112:113], %[ad0] offset:0\n\t" \
  "ds_read_b64_tr_b16 v[114:115], %[ad0] offset:512\n\t" \
  "v_mfma_f32_16x16x32_bf16 v[64:67], v[128:131], v[228:231], v[64:67]\n\t" \
  "v_mfma_f32_16x16x32_bf16 v[36:39], v[132:135], v[172:175], v[36:39]\n\t" \
  "ds_read_b64_tr_b16 v[116:117], %[ad1] offset:0\n\t" \
  "ds_read_b64_tr_b16 v[118:119], %[ad1] offset:512\n\t" \
  "v_mfma_f32_16x16x32_bf16 v[68:71], v[132:135], v[228:231], v[68:71]\n\t" \
  "v_mfma_f32_16x16x32_bf16 v[40:43], v[136:139], v[172:175], v[40:43]\n\t" \
  "ds_read_b64_tr_b16 v[120:121], %[ad2] offset:0\n\t" \
  "ds_read_b64_tr_b16 v[122:123], %[ad2] offset:512\n\t" \
  "v_mfma_f32_16x16x32_bf16 v[72:75], v[136:139], v[228:231], v[72:75]\n\t" \
  "v_mfma_f32_16x16x32_bf16 v[44:47], v[140:143], v[172:175], v[44:47]\n\t" \
  "ds_read_b64_tr_b16 v[124:125], %[ad3] offset:0\n\t" \
  "ds_read_b64_tr_b16 v[126:127], %[ad3] offset:512\n\t" \
  "v_mfma_f32_16x16x32_bf16 v[76:79], v[140:143], v[228:231], v[76:79]\n\t" \
  "s_waitcnt lgkmcnt(6)\n\t" \
  "v_mfma_f32_16x16x32_bf16 v[48:51], v[96:99], v[172:175], v[48:51]\n\t" \
  "ds_read_b64_tr_b16 v[128:129], %[ad0] offset:8192\n\t" \
  "ds_read_b64_tr_b16 v[130:131], %[ad0] offset:8704\n\t" \
  "v_mfma_f32_16x16x32_bf16 v[80:83], v[96:99], v[228:231], v[80:83]\n\t" \
  "v_mfma_f32_16x16x32_bf16 v[52:55], v[100:103], v[172:175], v[52:55]\n\t" \
  "ds_read_b64_tr_b16 v[132:133], %[ad1] offset:8192\n\t" \
  "ds_read_b64_tr_b16 v[134:135], %[ad1] offset:8704\n\t" \
  "v_mfma_f32_16x16x32_bf16 v[84:87], v[100:103], v[228:231], v[84:87]\n\t" \
  "v_mfma_f32_16x16x32_bf16 v[56:59], v[104:107], v[172:175], v[56:59]\n\t" \
  "ds_read_b64_tr_b16 v[136:137], %[ad2] offset:8192\n\t" \
  "ds_read_b64_tr_b16 v[138:139], %[ad2] offset:8704\n\t" \
  "v_mfma_f32_16x16x32_bf16 v[88:91], v[104:107], v[228:231], v[88:91]\n\t" \
  "v_mfma_f32_16x16x32_bf16 v[60:63], v[108:111], v[172:175], v[60:63]\n\t" \
  "ds_read_b64_tr_b16 v[140:141], %[ad3] offset:8192\n\t" \
  "ds_read_b64_tr_b16 v[142:143], %[ad3] offset:8704\n\t" \
  "v_mfma_f32_16x16x32_bf16 v[92:95], v[108:111], v[228:231], v[92:95]\n\t" \
  "s_waitcnt lgkmcnt(6)\n\t" \
  "v_mfma_f32_16x16x32_bf16 v[32:35], v[112:115], v[176:179], v[32:35]\n\t" \
  "ds_read_b64_tr_b16 v[96:97], %[ad0] offset:4096\n\t" \
  "ds_read_b64_tr_b16 v[98:99], %[ad0] offset:4608\n\t" \
  "v_mfma_f32_16x16x32_bf16 v[64:67], v[112:115], v[232:235], v[64:67]\n\t" \
  "v_mfma_f32_16x16x32_bf16 v[36:39], v[116:119], v[176:179], v[36:39]\n\t" \
  "ds_read_b64_tr_b16 v[100:101], %[ad1] offset:4096\n\t" \
  "ds_read_b64_tr_b16 v[102:103], %[ad1] offset:4608\n\t" \
  "v_mfma_f32_16x16x32_bf16 v[68:71], v[116:119], v[232:235], v[68:71]\n\t" \
  "v_mfma_f32_16x16x32_bf16 v[40:43], v[120:123], v[176:179], v[40:43]\n\t" \
  "ds_read_b64_tr_b16 v[104:105], %[ad2] offset:4096\n\t" \
  "ds_read_b64_tr_b16 v[106:107], %[ad2] offset:4608\n\t" \
  "v_mfma_f32_16x16x32_bf16 v[72:75], v[120:123], v[232:235], v[72:75]\n\t" \
  "v_mfma_f32_16x16x32_bf16 v[44:47], v[124:127], v[176:179], v[44:47]\n\t" \
  "ds_read_b64_tr_b16 v[108:109], %[ad3] offset:4096\n\t" \
  "ds_read_b64_tr_b16 v[110:111], %[ad3] offset:4608\n\t" \
  "v_mfma_f32_16x16x32_bf16 v[76:79], v[124:127], v[232:235], v[76:79]\n\t" \
  "s_waitcnt lgkmcnt(6)\n\t" \
  "v_mfma_f32_16x16x32_bf16 v[48:51], v[128:131], v[176:179], v[48:51]\n\t" \
  "ds_read_b64_tr_b16 v[112:113], %[ad0] offset:12288\n\t" \
  "ds_read_b64_tr_b16 v[114:115], %[ad0] offset:12800\n\t" \
  "v_mfma_f32_16x16x32_bf16 v[80:83], v[128:131], v[232:235], v[80:83]\n\t" \
  "v_mfma_f32_16x16x32_bf16 v[52:55], v[132:135], v[176:179], v[52:55]\n\t" \
  "ds_read_b64_tr_b16 v[116:117], %[ad1] offset:12288\n\t" \
  "ds_read_b64_tr_b16 v[118:119], %[ad1] offset:12800\n\t" \
  "v_mfma_f32_16x16x32_bf16 v[84:87], v[132:135], v[232:235], v[84:87]\n\t" \
  "v_mfma_f32_16x16x32_bf16 v[56:59], v[136:139], v[176:179], v[56:59]\n\t" \
  "ds_read_b64_tr_b16 v[120:121], %[ad2] offset:12288\n\t" \
  "ds_read_b64_tr_b16 v[122:123], %[ad2] offset:12800\n\t" \
  "v_mfma_f32_16x16x32_bf16 v[88:91], v[136:139], v[232:235], v[88:91]\n\t" \
  "v_mfma_f32_16x16x32_bf16 v[60:63], v[140:143], v[176:179], v[60:63]\n\t" \
  "ds_read_b64_tr_b16 v[124:125], %[ad3] offset:12288\n\t" \
  "ds_read_b64_tr_b16 v[126:127], %[ad3] offset:12800\n\t" \
  "v_mfma_f32_16x16x32_bf16 v[92:95], v[140:143], v[232:235], v[92:95]\n\t" \
  "s_waitcnt vmcnt(8)\n\t" \
  "s_waitcnt lgkmcnt(0)\n\t" \
  "s_mov_b64 s[64:65], exec\n\t" \
  "s_mov_b64 exec, 1\n\t" \
  "ds_add_u32 %[cnt], %[one]\n\t" \
  "s_mov_b64 exec, s[64:65]\n\t" \
  "s_mov_b32 s67, 0x400000\n\t" \
  "1:\n\t" \
  "ds_read_b32 %[vt], %[cnt]\n\t" \
  "s_waitcnt lgkmcnt(0)\n\t" \
  "v_readfirstlane_b32 s66, %[vt]\n\t" \
  "s_nop 3\n\t" \
  "s_cmp_ge_u32 s66, 24\n\t" \
  "s_cbranch_scc1 2f\n\t" \
  "s_sub_u32 s67, s67, 1\n\t" \
  "s_cbranch_scc1 2f\n\t" \
  "s_sleep 1\n\t" \
  "s_branch 1b\n\t" \
  "2:\n\t" \
  "s_waitcnt lgkmcnt(6)\n\t" \
  "v_mfma_f32_16x16x32_bf16 v[32:35], v[96:99], v[180:183], v[32:35]\n\t" \
  "ds_read_b64_tr_b16 v[128:129], %[ad0] offset:16384\n\t" \
  "ds_read_b64_tr_b16 v[130:131], %[ad0] offset:16896\n\t" \
  "v_mfma_f32_16x16x32_bf16 v[64:67], v[96:99], v[236:239], v[64:67]\n\t" \
  "v_mfma_f32_16x16x32_bf16 v[36:39], v[100:103], v[180:183], v[36:39]\n\t" \
  "ds_read_b64_tr_b16 v[132:133], %[ad1] offset:16384\n\t" \
  "ds_read_b64_tr_b16 v[134:135], %[ad1] offset:16896\n\t" \
  "v_mfma_f32_16x16x32_bf16 v[68:71], v[100:103], v[236:239], v[68:71]\n\t" \
  "v_mfma_f32_16x16x32_bf16 v[40:43], v[104:107], v[180:183], v[40:43]\n\t" \
  "ds_read_b64_tr_b16 v[136:137], %[ad2] offset:16384\n\t" \
  "ds_read_b64_tr_b16 v[138:139], %[ad2] offset:16896\n\t" \
  "v_mfma_f32_16x16x32_bf16 v[72:75], v[104:107], v[236:239], v[72:75]\n\t" \
  "v_mfma_f32_16x16x32_bf16 v[44:47], v[108:111], v[180:183], v[44:47]\n\t" \
  "ds_read_b64_tr_b16 v[140:141], %[ad3] offset:16384\n\t" \
  "ds_read_b64_tr_b16 v[142:143], %[ad3] offset:16896\n\t" \
  "v_mfma_f32_16x16x32_bf16 v[76:79], v[108:111], v[236:239], v[76:79]\n\t" \
  "s_waitcnt lgkmcnt(6)\n\t" \
  "v_mfma_f32_16x16x32_bf16 v[48:51], v[112:115], v[180:183], v[48:51]\n\t" \
  "ds_read_b64_tr_b16 v[96:97], %[ad0] offset:24576\n\t" \
  "ds_read_b64_tr_b16 v[98:99], %[ad0] offset:25088\n\t" \
  "v_mfma_f32_16x16x32_bf16 v[80:83], v[112:115], v[236:239], v[80:83]\n\t" \
  "v_mfma_f32_16x16x32_bf16 v[52:55], v[116:119], v[180:183], v[52:55]\n\t" \
  "ds_read_b64_tr_b16 v[100:101], %[ad1] offset:24576\n\t" \
  "ds_read_b64_tr_b16 v[102:103], %[ad1] offset:25088\n\t" \
  "v_mfma_f32_16x16x32_bf16 v[84:87], v[116:119], v[236:239], v[84:87]\n\t" \
  "v_mfma_f32_16x16x32_bf16 v[56:59], v[120:123], v[180:183], v[56:59]\n\t" \
  "ds_read_b64_tr_b16 v[104:105], %[ad2] offset:24576\n\t" \
  "ds_read_b64_tr_b16 v[106:107], %[ad2] offset:25088\n\t" \
  "v_mfma_f32_16x16x32_bf16 v[88:91], v[120:123], v[236:239], v[88:91]\n\t" \
  "v_mfma_f32_16x16x32_bf16 v[60:63], v[124:127], v[180:183], v[60:63]\n\t" \
  "ds_read_b64_tr_b16 v[108:109], %[ad3] offset:24576\n\t" \
  "ds_read_b64_tr_b16 v[110:111], %[ad3] offset:25088\n\t" \
  "v_mfma_f32_16x16x32_bf16 v[92:95], v[124:127], v[236:239], v[92:95]\n\t" \
  "s_waitcnt lgkmcnt(6)\n\t" \
  "v_mfma_f32_16x16x32_bf16 v[32:35], v[128:131], v[184:187], v[32:35]\n\t" \
  "ds_read_b64_tr_b16 v[112:113], %[ad0] offset:20480\n\t" \
  "ds_read_b64_tr_b16 v[114:115], %[ad0] offset:20992\n\t" \
  "v_mfma_f32_16x16x32_bf16 v[64:67], v[128:131], v[240:243], v[64:67]\n\t" \
  "v_mfma_f32_16x16x32_bf16 v[36:39], v[132:135], v[184:187], v[36:39]\n\t" \
  "ds_read_b64_tr_b16 v[116:117], %[ad1] offset:20480\n\t" \
  "ds_read_b64_tr_b16 v[118:119], %[ad1] offset:20992\n\t" \
  "v_mfma_f32_16x16x32_bf16 v[68:71], v[132:135], v[240:243], v[68:71]\n\t" \
  "v_mfma_f32_16x16x32_bf16 v[40:43], v[136:139], v[184:187], v[40:43]\n\t" \
  "ds_read_b64_tr_b16 v[120:121], %[ad2] offset:20480\n\t" \
  "ds_read_b64_tr_b16 v[122:123], %[ad2] offset:20992\n\t" \
  "v_mfma_f32_16x16x32_bf16 v[72:75], v[136:139], v[240:243], v[72:75]\n\t" \
  "v_mfma_f32_16x16x32_bf16 v[44:47], v[140:143], v[184:187], v[44:47]\n\t" \
  "ds_read_b64_tr_b16 v[124:125], %[ad3] offset:20480\n\t" \
  "ds_read_b64_tr_b16 v[126:127], %[ad3] offset:20992\n\t" \
  "v_mfma_f32_16x16x32_bf16 v[76:79], v[140:143], v[240:243], v[76:79]\n\t" \
  "s_waitcnt lgkmcnt(6)\n\t" \
  "v_mfma_f32_16x16x32_bf16 v[48:51], v[96:99], v[184:187], v[48:51]\n\t" \
  "ds_read_b64_tr_b16 v[128:129], %[ad0] offset:28672\n\t" \
  "ds_read_b64_tr_b16 v[130:131], %[ad0] offset:29184\n\t" \
  "v_mfma_f32_16x16x32_bf16 v[80:83], v[96:99], v[240:243], v[80:83]\n\t" \
  "v_mfma_f32_16x16x32_bf16 v[52:55], v[100:103], v[184:187], v[52:55]\n\t" \
  "ds_read_b64_tr_b16 v[132:133], %[ad1] offset:28672\n\t" \
  "ds_read_b64_tr_b16 v[134:135], %[ad1] offset:29184\n\t" \
  "v_mfma_f32_16x16x32_bf16 v[84:87], v[100:103], v[240:243], v[84:87]\n\t" \
  "v_mfma_f32_16x16x32_bf16 v[56:59], v[104:107], v[184:187], v[56:59]\n\t" \
  "ds_read_b64_tr_b16 v[136:137], %[ad2] offset:28672\n\t" \
  "ds_read_b64_tr_b16 v[138:139], %[ad2] offset:29184\n\t" \
  "v_mfma_f32_16x16x32_bf16 v[88:91], v[104:107], v[240:243], v[88:91]\n\t" \
  "v_mfma_f32_16x16x32_bf16 v[60:63], v[108:111], v[184:187], v[60:63]\n\t" \
  "ds_read_b64_tr_b16 v[140:141], %[ad3] offset:28672\n\t" \
  "ds_read_b64_tr_b16 v[142:143], %[ad3] offset:29184\n\t" \
  "v_mfma_f32_16x16x32_bf16 v[92:95], v[108:111], v[240:243], v[92:95]\n\t" \
  "s_waitcnt vmcnt(0)\n\t" \
  "s_waitcnt lgkmcnt(0)\n\t" \
  "s_mov_b64 s[64:65], exec\n\t" \
  "s_mov_b64 exec, 1\n\t" \
  "ds_add_u32 %[cnt], %[one]\n\t" \
  "s_mov_b64 exec, s[64:65]\n\t" \
  "s_mov_b32 s67, 0x400000\n\t" \
  "1:\n\t" \
  "ds_read_b32 %[vt], %[cnt]\n\t" \
  "s_waitcnt lgkmcnt(0)\n\t" \
  "v_readfirstlane_b32 s66, %[vt]\n\t" \
  "s_nop 3\n\t" \
  "s_cmp_ge_u32 s66, 28\n\t" \
  "s_cbranch_scc1 2f\n\t" \
  "s_sub_u32 s67, s67, 1\n\t" \
  "s_cbranch_scc1 2f\n\t" \
  "s_sleep 1\n\t" \
  "s_branch 1b\n\t" \
  "2:\n\t" \
  "s_waitcnt lgkmcnt(6)\n\t" \
  "v_mfma_f32_16x16x32_bf16 v[32:35], v[112:115], v[188:191], v[32:35]\n\t" \
  "ds_read_b64_tr_b16 v[96:97], %[ad0] offset:32768\n\t" \
  "ds_read_b64_tr_b16 v[98:99], %[ad0] offset:33280\n\t" \
  "v_mfma_f32_16x16x32_bf16 v[64:67], v[112:115], v[244:247], v[64:67]\n\t" \
  "v_mfma_f32_16x16x32_bf16 v[36:39], v[116:119], v[188:191], v[36:39]\n\t" \
  "ds_read_b64_tr_b16 v[100:101], %[ad1] offset:32768\n\t" \
  "ds_read_b64_tr_b16 v[102:103], %[ad1] offset:33280\n\t" \
  "v_mfma_f32_16x16x32_bf16 v[68:71], v[116:119], v[244:247], v[68:71]\n\t" \
  "v_mfma_f32_16x16x32_bf16 v[40:43], v[120:123], v[188:191], v[40:43]\n\t" \
  "ds_read_b64_tr_b16 v[104:105], %[ad2] offset:32768\n\t" \
  "ds_read_b64_tr_b16 v[106:107], %[ad2] offset:33280\n\t" \
  "v_mfma_f32_16x16x32_bf16 v[72:75], v[120:123], v[244:247], v[72:75]\n\t" \
  "v_mfma_f32_16x16x32_bf16 v[44:47], v[124:127], v[188:191], v[44:47]\n\t" \
  "ds_read_b64_tr_b16 v[108:109], %[ad3] offset:32768\n\t" \
  "ds_read_b64_tr_b16 v[110:111], %[ad3] offset:33280\n\t" \
  "v_mfma_f32_16x16x32_bf16 v[76:79], v[124:127], v[244:247], v[76:79]\n\t" \
  "s_waitcnt lgkmcnt(6)\n\t" \
  "v_mfma_f32_16x16x32_bf16 v[48:51], v[128:131], v[188:191], v[48:51]\n\t" \
  "ds_read_b64_tr_b16 v[112:113], %[ad0] offset:40960\n\t" \
  "ds_read_b64_tr_b16 v[114:115], %[ad0] offset:41472\n\t" \
  "v_mfma_f32_16x16x32_bf16 v[80:83], v[128:131], v[244:247], v[80:83]\n\t" \
  "v_mfma_f32_16x16x32_bf16 v[52:55], v[132:135], v[188:191], v[52:55]\n\t" \
  "ds_read_b64_tr_b16 v[116:117], %[ad1] offset:40960\n\t" \
  "ds_read_b64_tr_b16 v[118:119], %[ad1] offset:41472\n\t" \
  "v_mfma_f32_16x16x32_bf16 v[84:87], v[132:135], v[244:247], v[84:87]\n\t" \
  "v_mfma_f32_16x16x32_bf16 v[56:59], v[136:139], v[188:191], v[56:59]\n\t" \
  "ds_read_b64_tr_b16 v[120:121], %[ad2] offset:40960\n\t" \
  "ds_read_b64_tr_b16 v[122:123], %[ad2] offset:41472\n\t" \
  "v_mfma_f32_16x16x32_bf16 v[88:91], v[136:139], v[244:247], v[88:91]\n\t" \
  "v_mfma_f32_16x16x32_bf16 v[60:63], v[140:143], v[188:191], v[60:63]\n\t" \
  "ds_read_b64_tr_b16 v[124:125], %[ad3] offset:40960\n\t" \
  "ds_read_b64_tr_b16 v[126:127], %[ad3] offset:41472\n\t" \
  "v_mfma_f32_16x16x32_bf16 v[92:95], v[140:143], v[244:247], v[92:95]\n\t" \
  "s_waitcnt lgkmcnt(6)\n\t" \
  "v_mfma_f32_16x16x32_bf16 v[32:35], v[96:99], v[192:195], v[32:35]\n\t" \
  "ds_read_b64_tr_b16 v[128:129], %[ad0] offset:36864\n\t" \
  "ds_read_b64_tr_b16 v[130:131], %[ad0] offset:37376\n\t" \
  "v_mfma_f32_16x16x32_bf16 v[64:67], v[96:99], v[248:251], v[64:67]\n\t" \
  "v_mfma_f32_16x16x32_bf16 v[36:39], v[100:103], v[192:195], v[36:39]\n\t" \
  "ds_read_b64_tr_b16 v[132:133], %[ad1] offset:36864\n\t" \
  "ds_read_b64_tr_b16 v[134:135], %[ad1] offset:37376\n\t" \
  "v_mfma_f32_16x16x32_bf16 v[68:71], v[100:103], v[248:251], v[68:71]\n\t" \
  "v_mfma_f32_16x16x32_bf16 v[40:43], v[104:107], v[192:195], v[40:43]\n\t" \
  "ds_read_b64_tr_b16 v[136:137], %[ad2] offset:36864\n\t" \
  "ds_read_b64_tr_b16 v[138:139], %[ad2] offset:37376\n\t" \
  "v_mfma_f32_16x16x32_bf16 v[72:75], v[104:107], v[248:251], v[72:75]\n\t" \
  "v_mfma_f32_16x16x32_bf16 v[44:47], v[108:111], v[192:195], v[44:47]\n\t" \
  "ds_read_b64_tr_b16 v[140:141], %[ad3] offset:36864\n\t" \
  "ds_read_b64_tr_b16 v[142:143], %[ad3] offset:37376\n\t" \
  "v_mfma_f32_16x16x32_bf16 v[76:79], v[108:111], v[248:251], v[76:79]\n\t" \
  "s_waitcnt lgkmcnt(6)\n\t" \
  "v_mfma_f32_16x16x32_bf16 v[48:51], v[112:115], v[192:195], v[48:51]\n\t" \
  "ds_read_b64_tr_b16 v[96:97], %[ad0] offset:45056\n\t" \
  "ds_read_b64_tr_b16 v[98:99], %[ad0] offset:45568\n\t" \
  "v_mfma_f32_16x16x32_bf16 v[80:83], v[112:115], v[248:251], v[80:83]\n\t" \
  "v_mfma_f32_16x16x32_bf16 v[52:55], v[116:119], v[192:195], v[52:55]\n\t" \
  "ds_read_b64_tr_b16 v[100:101], %[ad1] offset:45056\n\t" \
  "ds_read_b64_tr_b16 v[102:103], %[ad1] offset:45568\n\t" \
  "v_mfma_f32_16x16x32_bf16 v[84:87], v[116:119], v[248:251], v[84:87]\n\t" \
  "v_mfma_f32_16x16x32_bf16 v[56:59], v[120:123], v[192:195], v[56:59]\n\t" \
  "ds_read_b64_tr_b16 v[104:105], %[ad2] offset:45056\n\t" \
  "ds_read_b64_tr_b16 v[106:107], %[ad2] offset:45568\n\t" \
  "v_mfma_f32_16x16x32_bf16 v[88:91], v[120:123], v[248:251], v[88:91]\n\t" \
  "v_mfma_f32_16x16x32_bf16 v[60:63], v[124:127], v[192:195], v[60:63]\n\t" \
  "ds_read_b64_tr_b16 v[108:109], %[ad3] offset:45056\n\t" \
  "ds_read_b64_tr_b16 v[110:111], %[ad3] offset:45568\n\t" \
  "v_mfma_f32_16x16x32_bf16 v[92:95], v[124:127], v[248:251], v[92:95]\n\t" \
  "s_waitcnt lgkmcnt(8)\n\t" \
  "v_mfma_f32_16x16x32_bf16 v[32:35], v[128:131], v[196:199], v[32:35]\n\t" \
  "v_mfma_f32_16x16x32_bf16 v[64:67], v[128:131], v[252:255], v[64:67]\n\t" \
  "v_mfma_f32_16x16x32_bf16 v[36:39], v[132:135], v[196:199], v[36:39]\n\t" \
  "v_mfma_f32_16x16x32_bf16 v[68:71], v[132:135], v[252:255], v[68:71]\n\t" \
  "v_mfma_f32_16x16x32_bf16 v[40:43], v[136:139], v[196:199], v[40:43]\n\t" \
  "v_mfma_f32_16x16x32_bf16 v[72:75], v[136:139], v[252:255], v[72:75]\n\t" \
  "v_mfma_f32_16x16x32_bf16 v[44:47], v[140:143], v[196:199], v[44:47]\n\t" \
  "v_mfma_f32_16x16x32_bf16 v[76:79], v[140:143], v[252:255], v[76:79]\n\t" \
  "s_waitcnt lgkmcnt(0)\n\t" \
  "v_mfma_f32_16x16x32_bf16 v[48:51], v[96:99], v[196:199], v[48:51]\n\t" \
  "v_mfma_f32_16x16x32_bf16 v[80:83], v[96:99], v[252:255], v[80:83]\n\t" \
  "v_mfma_f32_16x16x32_bf16 v[52:55], v[100:103], v[196:199], v[52:55]\n\t" \
  "v_mfma_f32_16x16x32_bf16 v[84:87], v[100:103], v[252:255], v[84:87]\n\t" \
  "v_mfma_f32_16x16x32_bf16 v[56:59], v[104:107], v[196:199], v[56:59]\n\t" \
  "v_mfma_f32_16x16x32_bf16 v[88:91], v[104:107], v[252:255], v[88:91]\n\t" \
  "v_mfma_f32_16x16x32_bf16 v[60:63], v[108:111], v[196:199], v[60:63]\n\t" \
  "v_mfma_f32_16x16x32_bf16 v[92:95], v[108:111], v[252:255], v[92:95]\n\t" \
  "v_subrev_u32 %[ad0], 0x10000, %[ad0]\n\t" \
  "v_subrev_u32 %[ad1], 0x10000, %[ad1]\n\t" \
  "v_subrev_u32 %[ad2], 0x10000, %[ad2]\n\t" \
  "v_subrev_u32 %[ad3], 0x10000, %[ad3]\n\t" \
  "s_nop 7\n\t" \
  "s_nop 7\n\t"

#define K2_XISSUE1_ASM \
  "s_add_u32 s62, %[xslo], 114688\n\t" \
  "s_addc_u32 s63, %[xshi], 0\n\t" \
  "global_load_dwordx4 v[144:147], %[voff], s[62:63] offset:0\n\t" \
  "s_add_u32 s62, %[xslo], 126976\n\t" \
  "s_addc_u32 s63, %[xshi], 0\n\t" \
  "global_load_dwordx4 v[200:203], %[voff], s[62:63] offset:2048\n\t" \
  "s_add_u32 s62, %[xslo], 114688\n\t" \
  "s_addc_u32 s63, %[xshi], 0\n\t" \
  "global_load_dwordx4 v[148:151], %[voff], s[62:63] offset:1024\n\t" \
  "s_add_u32 s62, %[xslo], 126976\n\t" \
  "s_addc_u32 s63, %[xshi], 0\n\t" \
  "global_load_dwordx4 v[204:207], %[voff], s[62:63] offset:3072\n\t" \
  "s_add_u32 s62, %[xslo], 114688\n\t" \
  "s_addc_u32 s63, %[xshi], 0\n\t" \
  "global_load_dwordx4 v[152:155], %[voff], s[62:63] offset:2048\n\t" \
  "s_add_u32 s62, %[xslo], 131072\n\t" \
  "s_addc_u32 s63, %[xshi], 0\n\t" \
  "global_load_dwordx4 v[208:211], %[voff], s[62:63] offset:0\n\t" \
  "s_add_u32 s62, %[xslo], 114688\n\t" \
  "s_addc_u32 s63, %[xshi], 0\n\t" \
  "global_load_dwordx4 v[156:159], %[voff], s[62:63] offset:3072\n\t" \
  "s_add_u32 s62, %[xslo], 131072\n\t" \
  "s_addc_u32 s63, %[xshi], 0\n\t" \
  "global_load_dwordx4 v[212:215], %[voff], s[62:63] offset:1024\n\t" \
  "s_add_u32 s62, %[xslo], 118784\n\t" \
  "s_addc_u32 s63, %[xshi], 0\n\t" \
  "global_load_dwordx4 v[160:163], %[voff], s[62:63] offset:0\n\t" \
  "s_add_u32 s62, %[xslo], 131072\n\t" \
  "s_addc_u32 s63, %[xshi], 0\n\t" \
  "global_load_dwordx4 v[216:219], %[voff], s[62:63] offset:2048\n\t" \
  "s_add_u32 s62, %[xslo], 118784\n\t" \
  "s_addc_u32 s63, %[xshi], 0\n\t" \
  "global_load_dwordx4 v[164:167], %[voff], s[62:63] offset:1024\n\t" \
  "s_add_u32 s62, %[xslo], 131072\n\t" \
  "s_addc_u32 s63, %[xshi], 0\n\t" \
  "global_load_dwordx4 v[220:223], %[voff], s[62:63] offset:3072\n\t" \
  "s_add_u32 s62, %[xslo], 118784\n\t" \
  "s_addc_u32 s63, %[xshi], 0\n\t" \
  "global_load_dwordx4 v[168:171], %[voff], s[62:63] offset:2048\n\t" \
  "s_add_u32 s62, %[xslo], 135168\n\t" \
  "s_addc_u32 s63, %[xshi], 0\n\t" \
  "global_load_dwordx4 v[224:227], %[voff], s[62:63] offset:0\n\t" \
  "s_add_u32 s62, %[xslo], 118784\n\t" \
  "s_addc_u32 s63, %[xshi], 0\n\t" \
  "global_load_dwordx4 v[172:175], %[voff], s[62:63] offset:3072\n\t" \
  "s_add_u32 s62, %[xslo], 135168\n\t" \
  "s_addc_u32 s63, %[xshi], 0\n\t" \
  "global_load_dwordx4 v[228:231], %[voff], s[62:63] offset:1024\n\t" \
  "s_add_u32 s62, %[xslo], 122880\n\t" \
  "s_addc_u32 s63, %[xshi], 0\n\t" \
  "global_load_dwordx4 v[176:179], %[voff], s[62:63] offset:0\n\t" \
  "s_add_u32 s62, %[xslo], 135168\n\t" \
  "s_addc_u32 s63, %[xshi], 0\n\t" \
  "global_load_dwordx4 v[232:235], %[voff], s[62:63] offset:2048\n\t" \
  "s_add_u32 s62, %[xslo], 122880\n\t" \
  "s_addc_u32 s63, %[xshi], 0\n\t" \
  "global_load_dwordx4 v[180:183], %[voff], s[62:63] offset:1024\n\t" \
  "s_add_u32 s62, %[xslo], 135168\n\t" \
  "s_addc_u32 s63, %[xshi], 0\n\t" \
  "global_load_dwordx4 v[236:239], %[voff], s[62:63] offset:3072\n\t" \
  "s_add_u32 s62, %[xslo], 122880\n\t" \
  "s_addc_u32 s63, %[xshi], 0\n\t" \
  "global_load_dwordx4 v[184:187], %[voff], s[62:63] offset:2048\n\t" \
  "s_add_u32 s62, %[xslo], 139264\n\t" \
  "s_addc_u32 s63, %[xshi], 0\n\t" \
  "global_load_dwordx4 v[240:243], %[voff], s[62:63] offset:0\n\t" \
  "s_add_u32 s62, %[xslo], 122880\n\t" \
  "s_addc_u32 s63, %[xshi], 0\n\t" \
  "global_load_dwordx4 v[188:191], %[voff], s[62:63] offset:3072\n\t" \
  "s_add_u32 s62, %[xslo], 139264\n\t" \
  "s_addc_u32 s63, %[xshi], 0\n\t" \
  "global_load_dwordx4 v[244:247], %[voff], s[62:63] offset:1024\n\t" \
  "s_add_u32 s62, %[xslo], 126976\n\t" \
  "s_addc_u32 s63, %[xshi], 0\n\t" \
  "global_load_dwordx4 v[192:195], %[voff], s[62:63] offset:0\n\t" \
  "s_add_u32 s62, %[xslo], 139264\n\t" \
  "s_addc_u32 s63, %[xshi], 0\n\t" \
  "global_load_dwordx4 v[248:251], %[voff], s[62:63] offset:2048\n\t" \
  "s_add_u32 s62, %[xslo], 126976\n\t" \
  "s_addc_u32 s63, %[xshi], 0\n\t" \
  "global_load_dwordx4 v[196:199], %[voff], s[62:63] offset:1024\n\t" \
  "s_add_u32 s62, %[xslo], 139264\n\t" \
  "s_addc_u32 s63, %[xshi], 0\n\t" \
  "global_load_dwordx4 v[252:255], %[voff], s[62:63] offset:3072\n\t"

#define K2_ROUND0_ASM \
  "s_add_u32 s60, %[gblo], 0\n\t" \
  "s_addc_u32 s61, %[gbhi], 0\n\t" \
  "s_add_u32 m0, %[ldo], 0\n\t" \
  "s_nop 0\n\t" \
  "global_load_lds_dwordx4 %[lo0], s[60:61]\n\t" \
  "s_add_u32 s60, %[gblo], 28672\n\t" \
  "s_addc_u32 s61, %[gbhi], 0\n\t" \
  "s_add_u32 m0, %[ldo], 1024\n\t" \
  "s_nop 0\n\t" \
  "global_load_lds_dwordx4 %[lo1], s[60:61]\n\t" \
  "s_add_u32 s60, %[ublo], 0\n\t" \
  "s_addc_u32 s61, %[ubhi], 0\n\t" \
  "s_add_u32 m0, %[ldo], 8192\n\t" \
  "s_nop 0\n\t" \
  "global_load_lds_dwordx4 %[lo0], s[60:61]\n\t" \
  "s_add_u32 s60, %[ublo], 28672\n\t" \
  "s_addc_u32 s61, %[ubhi], 0\n\t" \
  "s_add_u32 m0, %[ldo], 9216\n\t" \
  "s_nop 0\n\t" \
  "global_load_lds_dwordx4 %[lo1], s[60:61]\n\t" \
  "s_add_u32 s62, %[xslo], 0\n\t" \
  "s_addc_u32 s63, %[xshi], 0\n\t" \
  "global_load_dwordx4 v[144:147], %[voff], s[62:63] offset:0\n\t" \
  "s_add_u32 s62, %[xslo], 12288\n\t" \
  "s_addc_u32 s63, %[xshi], 0\n\t" \
  "global_load_dwordx4 v[200:203], %[voff], s[62:63] offset:2048\n\t" \
  "s_add_u32 s62, %[xslo], 0\n\t" \
  "s_addc_u32 s63, %[xshi], 0\n\t" \
  "global_load_dwordx4 v[148:151], %[voff], s[62:63] offset:1024\n\t" \
  "s_add_u32 s62, %[xslo], 12288\n\t" \
  "s_addc_u32 s63, %[xshi], 0\n\t" \
  "global_load_dwordx4 v[204:207], %[voff], s[62:63] offset:3072\n\t" \
  "s_add_u32 s60, %[gblo], 229376\n\t" \
  "s_addc_u32 s61, %[gbhi], 0\n\t" \
  "s_add_u32 m0, %[ldo], 16384\n\t" \
  "s_nop 0\n\t" \
  "global_load_lds_dwordx4 %[lo0], s[60:61]\n\t" \
  "s_add_u32 s60, %[gblo], 258048\n\t" \
  "s_addc_u32 s61, %[gbhi], 0\n\t" \
  "s_add_u32 m0, %[ldo], 17408\n\t" \
  "s_nop 0\n\t" \
  "global_load_lds_dwordx4 %[lo1], s[60:61]\n\t" \
  "s_add_u32 s60, %[ublo], 229376\n\t" \
  "s_addc_u32 s61, %[ubhi], 0\n\t" \
  "s_add_u32 m0, %[ldo], 24576\n\t" \
  "s_nop 0\n\t" \
  "global_load_lds_dwordx4 %[lo0], s[60:61]\n\t" \
  "s_add_u32 s60, %[ublo], 258048\n\t" \
  "s_addc_u32 s61, %[ubhi], 0\n\t" \
  "s_add_u32 m0, %[ldo], 25600\n\t" \
  "s_nop 0\n\t" \
  "global_load_lds_dwordx4 %[lo1], s[60:61]\n\t" \
  "s_add_u32 s62, %[xslo], 0\n\t" \
  "s_addc_u32 s63, %[xshi], 0\n\t" \
  "global_load_dwordx4 v[152:155], %[voff], s[62:63] offset:2048\n\t" \
  "s_add_u32 s62, %[xslo], 16384\n\t" \
  "s_addc_u32 s63, %[xshi], 0\n\t" \
  "global_load_dwordx4 v[208:211], %[voff], s[62:63] offset:0\n\t" \
  "s_add_u32 s62, %[xslo], 0\n\t" \
  "s_addc_u32 s63, %[xshi], 0\n\t" \
  "global_load_dwordx4 v[156:159], %[voff], s[62:63] offset:3072\n\t" \
  "s_add_u32 s62, %[xslo], 16384\n\t" \
  "s_addc_u32 s63, %[xshi], 0\n\t" \
  "global_load_dwordx4 v[212:215], %[voff], s[62:63] offset:1024\n\t" \
  "s_add_u32 s60, %[gblo], 458752\n\t" \
  "s_addc_u32 s61, %[gbhi], 0\n\t" \
  "s_add_u32 m0, %[ldo], 32768\n\t" \
  "s_nop 0\n\t" \
  "global_load_lds_dwordx4 %[lo0], s[60:61]\n\t" \
  "s_add_u32 s60, %[gblo], 487424\n\t" \
  "s_addc_u32 s61, %[gbhi], 0\n\t" \
  "s_add_u32 m0, %[ldo], 33792\n\t" \
  "s_nop 0\n\t" \
  "global_load_lds_dwordx4 %[lo1], s[60:61]\n\t" \
  "s_add_u32 s60, %[ublo], 458752\n\t" \
  "s_addc_u32 s61, %[ubhi], 0\n\t" \
  "s_add_u32 m0, %[ldo], 40960\n\t" \
  "s_nop 0\n\t" \
  "global_load_lds_dwordx4 %[lo0], s[60:61]\n\t" \
  "s_add_u32 s60, %[ublo], 487424\n\t" \
  "s_addc_u32 s61, %[ubhi], 0\n\t" \
  "s_add_u32 m0, %[ldo], 41984\n\t" \
  "s_nop 0\n\t" \
  "global_load_lds_dwordx4 %[lo1], s[60:61]\n\t" \
  "s_add_u32 s62, %[xslo], 4096\n\t" \
  "s_addc_u32 s63, %[xshi], 0\n\t" \
  "global_load_dwordx4 v[160:163], %[voff], s[62:63] offset:0\n\t" \
  "s_add_u32 s62, %[xslo], 16384\n\t" \
  "s_addc_u32 s63, %[xshi], 0\n\t" \
  "global_load_dwordx4 v[216:219], %[voff], s[62:63] offset:2048\n\t" \
  "s_add_u32 s62, %[xslo], 4096\n\t" \
  "s_addc_u32 s63, %[xshi], 0\n\t" \
  "global_load_dwordx4 v[164:167], %[voff], s[62:63] offset:1024\n\t" \
  "s_add_u32 s62, %[xslo], 16384\n\t" \
  "s_addc_u32 s63, %[xshi], 0\n\t" \
  "global_load_dwordx4 v[220:223], %[voff], s[62:63] offset:3072\n\t" \
  "s_waitcnt vmcnt(16)\n\t" \
  "s_waitcnt lgkmcnt(0)\n\t" \
  "s_mov_b64 s[64:65], exec\n\t" \
  "s_mov_b64 exec, 1\n\t" \
  "ds_add_u32 %[cnt], %[one]\n\t" \
  "s_mov_b64 exec, s[64:65]\n\t" \
  "s_mov_b32 s67, 0x400000\n\t" \
  "1:\n\t" \
  "ds_read_b32 %[vt], %[cnt]\n\t" \
  "s_waitcnt lgkmcnt(0)\n\t" \
  "v_readfirstlane_b32 s66, %[vt]\n\t" \
  "s_nop 3\n\t" \
  "s_cmp_ge_u32 s66, 4\n\t" \
  "s_cbranch_scc1 2f\n\t" \
  "s_sub_u32 s67, s67, 1\n\t" \
  "s_cbranch_scc1 2f\n\t" \
  "s_sleep 1\n\t" \
  "s_branch 1b\n\t" \
  "2:\n\t" \
  "ds_read_b64_tr_b16 v[96:97], %[ad0] offset:0\n\t" \
  "ds_read_b64_tr_b16 v[98:99], %[ad0] offset:512\n\t" \
  "ds_read_b64_tr_b16 v[100:101], %[ad1] offset:0\n\t" \
  "ds_read_b64_tr_b16 v[102:103], %[ad1] offset:512\n\t" \
  "ds_read_b64_tr_b16 v[104:105], %[ad2] offset:0\n\t" \
  "ds_read_b64_tr_b16 v[106:107], %[ad2] offset:512\n\t" \
  "ds_read_b64_tr_b16 v[108:109], %[ad3] offset:0\n\t" \
  "ds_read_b64_tr_b16 v[110:111], %[ad3] offset:512\n\t" \
  "s_waitcnt lgkmcnt(4)\n\t" \
  "ds_read_b64_tr_b16 v[112:113], %[ad0] offset:8192\n\t" \
  "ds_read_b64_tr_b16 v[114:115], %[ad0] offset:8704\n\t" \
  "ds_read_b64_tr_b16 v[116:117], %[ad1] offset:8192\n\t" \
  "ds_read_b64_tr_b16 v[118:119], %[ad1] offset:8704\n\t" \
  "ds_read_b64_tr_b16 v[120:121], %[ad2] offset:8192\n\t" \
  "ds_read_b64_tr_b16 v[122:123], %[ad2] offset:8704\n\t" \
  "ds_read_b64_tr_b16 v[124:125], %[ad3] offset:8192\n\t" \
  "ds_read_b64_tr_b16 v[126:127], %[ad3] offset:8704\n\t" \
  "s_waitcnt lgkmcnt(6)\n\t" \
  "v_mfma_f32_16x16x32_bf16 v[32:35], v[96:99], v[144:147], 0\n\t" \
  "ds_read_b64_tr_b16 v[128:129], %[ad0] offset:4096\n\t" \
  "ds_read_b64_tr_b16 v[130:131], %[ad0] offset:4608\n\t" \
  "v_mfma_f32_16x16x32_bf16 v[64:67], v[96:99], v[200:203], 0\n\t" \
  "s_add_u32 s60, %[gblo], 688128\n\t" \
  "s_addc_u32 s61, %[gbhi], 0\n\t" \
  "s_add_u32 m0, %[ldo], 49152\n\t" \
  "s_nop 0\n\t" \
  "global_load_lds_dwordx4 %[lo0], s[60:61]\n\t" \
  "s_add_u32 s60, %[gblo], 716800\n\t" \
  "s_addc_u32 s61, %[gbhi], 0\n\t" \
  "s_add_u32 m0, %[ldo], 50176\n\t" \
  "s_nop 0\n\t" \
  "global_load_lds_dwordx4 %[lo1], s[60:61]\n\t" \
  "v_mfma_f32_16x16x32_bf16 v[36:39], v[100:103], v[144:147], 0\n\t" \
  "ds_read_b64_tr_b16 v[132:133], %[ad1] offset:4096\n\t" \
  "ds_read_b64_tr_b16 v[134:135], %[ad1] offset:4608\n\t" \
  "v_mfma_f32_16x16x32_bf16 v[68:71], v[100:103], v[200:203], 0\n\t" \
  "s_add_u32 s60, %[ublo], 688128\n\t" \
  "s_addc_u32 s61, %[ubhi], 0\n\t" \
  "s_add_u32 m0, %[ldo], 57344\n\t" \
  "s_nop 0\n\t" \
  "global_load_lds_dwordx4 %[lo0], s[60:61]\n\t" \
  "s_add_u32 s60, %[ublo], 716800\n\t" \
  "s_addc_u32 s61, %[ubhi], 0\n\t" \
  "s_add_u32 m0, %[ldo], 58368\n\t" \
  "s_nop 0\n\t" \
  "global_load_lds_dwordx4 %[lo1], s[60:61]\n\t" \
  "v_mfma_f32_16x16x32_bf16 v[40:43], v[104:107], v[144:147], 0\n\t" \
  "ds_read_b64_tr_b16 v[136:137], %[ad2] offset:4096\n\t" \
  "ds_read_b64_tr_b16 v[138:139], %[ad2] offset:4608\n\t" \
  "v_mfma_f32_16x16x32_bf16 v[72:75], v[104:107], v[200:203], 0\n\t" \
  "s_add_u32 s62, %[xslo], 4096\n\t" \
  "s_addc_u32 s63, %[xshi], 0\n\t" \
  "global_load_dwordx4 v[168:171], %[voff], s[62:63] offset:2048\n\t" \
  "s_add_u32 s62, %[xslo], 20480\n\t" \
  "s_addc_u32 s63, %[xshi], 0\n\t" \
  "global_load_dwordx4 v[224:227], %[voff], s[62:63] offset:0\n\t" \
  "v_mfma_f32_16x16x32_bf16 v[44:47], v[108:111], v[144:147], 0\n\t" \
  "ds_read_b64_tr_b16 v[140:141], %[ad3] offset:4096\n\t" \
  "ds_read_b64_tr_b16 v[142:143], %[ad3] offset:4608\n\t" \
  "v_mfma_f32_16x16x32_bf16 v[76:79], v[108:111], v[200:203], 0\n\t" \
  "s_add_u32 s62, %[xslo], 4096\n\t" \
  "s_addc_u32 s63, %[xshi], 0\n\t" \
  "global_load_dwordx4 v[172:175], %[voff], s[62:63] offset:3072\n\t" \
  "s_add_u32 s62, %[xslo], 20480\n\t" \
  "s_addc_u32 s63, %[xshi], 0\n\t" \
  "global_load_dwordx4 v[228:231], %[voff], s[62:63] offset:1024\n\t" \
  "s_waitcnt lgkmcnt(6)\n\t" \
  "v_mfma_f32_16x16x32_bf16 v[48:51], v[112:115], v[144:147], 0\n\t" \
  "ds_read_b64_tr_b16 v[96:97], %[ad0] offset:12288\n\t" \
  "ds_read_b64_tr_b16 v[98:99], %[ad0] offset:12800\n\t" \
  "v_mfma_f32_16x16x32_bf16 v[80:83], v[112:115], v[200:203], 0\n\t" \
  "v_mfma_f32_16x16x32_bf16 v[52:55], v[116:119], v[144:147], 0\n\t" \
  "ds_read_b64_tr_b16 v[100:101], %[ad1] offset:12288\n\t" \
  "ds_read_b64_tr_b16 v[102:103], %[ad1] offset:12800\n\t" \
  "v_mfma_f32_16x16x32_bf16 v[84:87], v[116:119], v[200:203], 0\n\t" \
  "v_mfma_f32_16x16x32_bf16 v[56:59], v[120:123], v[144:147], 0\n\t" \
  "ds_read_b64_tr_b16 v[104:105], %[ad2] offset:12288\n\t" \
  "ds_read_b64_tr_b16 v[106:107], %[ad2] offset:12800\n\t" \
  "v_mfma_f32_16x16x32_bf16 v[88:91], v[120:123], v[200:203], 0\n\t" \
  "v_mfma_f32_16x16x32_bf16 v[60:63], v[124:127], v[144:147], 0\n\t" \
  "ds_read_b64_tr_b16 v[108:109], %[ad3] offset:12288\n\t" \
  "ds_read_b64_tr_b16 v[110:111], %[ad3] offset:12800\n\t" \
  "v_mfma_f32_16x16x32_bf16 v[92:95], v[124:127], v[200:203], 0\n\t" \
  "s_waitcnt vmcnt(16)\n\t" \
  "s_waitcnt lgkmcnt(0)\n\t" \
  "s_mov_b64 s[64:65], exec\n\t" \
  "s_mov_b64 exec, 1\n\t" \
  "ds_add_u32 %[cnt], %[one]\n\t" \
  "s_mov_b64 exec, s[64:65]\n\t" \
  "s_mov_b32 s67, 0x400000\n\t" \
  "1:\n\t" \
  "ds_read_b32 %[vt], %[cnt]\n\t" \
  "s_waitcnt lgkmcnt(0)\n\t" \
  "v_readfirstlane_b32 s66, %[vt]\n\t" \
  "s_nop 3\n\t" \
  "s_cmp_ge_u32 s66, 8\n\t" \
  "s_cbranch_scc1 2f\n\t" \
  "s_sub_u32 s67, s67, 1\n\t" \
  "s_cbranch_scc1 2f\n\t" \
  "s_sleep 1\n\t" \
  "s_branch 1b\n\t" \
  "2:\n\t" \
  "s_waitcnt lgkmcnt(6)\n\t" \
  "v_mfma_f32_16x16x32_bf16 v[32:35], v[128:131], v[148:151], v[32:35]\n\t" \
  "ds_read_b64_tr_b16 v[112:113], %[ad0] offset:16384\n\t" \
  "ds_read_b64_tr_b16 v[114:115], %[ad0] offset:16896\n\t" \
  "v_mfma_f32_16x16x32_bf16 v[64:67], v[128:131], v[204:207], v[64:67]\n\t" \
  "v_mfma_f32_16x16x32_bf16 v[36:39], v[132:135], v[148:151], v[36:39]\n\t" \
  "ds_read_b64_tr_b16 v[116:117], %[ad1] offset:16384\n\t" \
  "ds_read_b64_tr_b16 v[118:119], %[ad1] offset:16896\n\t" \
  "v_mfma_f32_16x16x32_bf16 v[68:71], v[132:135], v[204:207], v[68:71]\n\t" \
  "v_mfma_f32_16x16x32_bf16 v[40:43], v[136:139], v[148:151], v[40:43]\n\t" \
  "ds_read_b64_tr_b16 v[120:121], %[ad2] offset:16384\n\t" \
  "ds_read_b64_tr_b16 v[122:123], %[ad2] offset:16896\n\t" \
  "v_mfma_f32_16x16x32_bf16 v[72:75], v[136:139], v[204:207], v[72:75]\n\t" \
  "v_mfma_f32_16x16x32_bf16 v[44:47], v[140:143], v[148:151], v[44:47]\n\t" \
  "ds_read_b64_tr_b16 v[124:125], %[ad3] offset:16384\n\t" \
  "ds_read_b64_tr_b16 v[126:127], %[ad3] offset:16896\n\t" \
  "v_mfma_f32_16x16x32_bf16 v[76:79], v[140:143], v[204:207], v[76:79]\n\t" \
  "s_waitcnt lgkmcnt(6)\n\t" \
  "v_mfma_f32_16x16x32_bf16 v[48:51], v[96:99], v[148:151], v[48:51]\n\t" \
  "ds_read_b64_tr_b16 v[128:129], %[ad0] offset:24576\n\t" \
  "ds_read_b64_tr_b16 v[130:131], %[ad0] offset:25088\n\t" \
  "v_mfma_f32_16x16x32_bf16 v[80:83], v[96:99], v[204:207], v[80:83]\n\t" \
  "v_mfma_f32_16x16x32_bf16 v[52:55], v[100:103], v[148:151], v[52:55]\n\t" \
  "ds_read_b64_tr_b16 v[132:133], %[ad1] offset:24576\n\t" \
  "ds_read_b64_tr_b16 v[134:135], %[ad1] offset:25088\n\t" \
  "v_mfma_f32_16x16x32_bf16 v[84:87], v[100:103], v[204:207], v[84:87]\n\t" \
  "v_mfma_f32_16x16x32_bf16 v[56:59], v[104:107], v[148:151], v[56:59]\n\t" \
  "ds_read_b64_tr_b16 v[136:137], %[ad2] offset:24576\n\t" \
  "ds_read_b64_tr_b16 v[138:139], %[ad2] offset:25088\n\t" \
  "v_mfma_f32_16x16x32_bf16 v[88:91], v[104:107], v[204:207], v[88:91]\n\t" \
  "v_mfma_f32_16x16x32_bf16 v[60:63], v[108:111], v[148:151], v[60:63]\n\t" \
  "ds_read_b64_tr_b16 v[140:141], %[ad3] offset:24576\n\t" \
  "ds_read_b64_tr_b16 v[142:143], %[ad3] offset:25088\n\t" \
  "v_mfma_f32_16x16x32_bf16 v[92:95], v[108:111], v[204:207], v[92:95]\n\t" \
  "s_waitcnt lgkmcnt(6)\n\t" \
  "v_mfma_f32_16x16x32_bf16 v[32:35], v[112:115], v[152:155], v[32:35]\n\t" \
  "ds_read_b64_tr_b16 v[96:97], %[ad0] offset:20480\n\t" \
  "ds_read_b64_tr_b16 v[98:99], %[ad0] offset:20992\n\t" \
  "v_mfma_f32_16x16x32_bf16 v[64:67], v[112:115], v[208:211], v[64:67]\n\t" \
  "s_add_u32 s60, %[gblo], 917504\n\t" \
  "s_addc_u32 s61, %[gbhi], 0\n\t" \
  "s_add_u32 m0, %[ldo], 65536\n\t" \
  "s_nop 0\n\t" \
  "global_load_lds_dwordx4 %[lo0], s[60:61]\n\t" \
  "s_add_u32 s60, %[gblo], 946176\n\t" \
  "s_addc_u32 s61, %[gbhi], 0\n\t" \
  "s_add_u32 m0, %[ldo], 66560\n\t" \
  "s_nop 0\n\t" \
  "global_load_lds_dwordx4 %[lo1], s[60:61]\n\t" \
  "v_mfma_f32_16x16x32_bf16 v[36:39], v[116:119], v[152:155], v[36:39]\n\t" \
  "ds_read_b64_tr_b16 v[100:101], %[ad1] offset:20480\n\t" \
  "ds_read_b64_tr_b16 v[102:103], %[ad1] offset:20992\n\t" \
  "v_mfma_f32_16x16x32_bf16 v[68:71], v[116:119], v[208:211], v[68:71]\n\t" \
  "s_add_u32 s60, %[ublo], 917504\n\t" \
  "s_addc_u32 s61, %[ubhi], 0\n\t" \
  "s_add_u32 m0, %[ldo], 73728\n\t" \
  "s_nop 0\n\t" \
  "global_load_lds_dwordx4 %[lo0], s[60:61]\n\t" \
  "s_add_u32 s60, %[ublo], 946176\n\t" \
  "s_addc_u32 s61, %[ubhi], 0\n\t" \
  "s_add_u32 m0, %[ldo], 74752\n\t" \
  "s_nop 0\n\t" \
  "global_load_lds_dwordx4 %[lo1], s[60:61]\n\t" \
  "v_mfma_f32_16x16x32_bf16 v[40:43], v[120:123], v[152:155], v[40:43]\n\t" \
  "ds_read_b64_tr_b16 v[104:105], %[ad2] offset:20480\n\t" \
  "ds_read_b64_tr_b16 v[106:107], %[ad2] offset:20992\n\t" \
  "v_mfma_f32_16x16x32_bf16 v[72:75], v[120:123], v[208:211], v[72:75]\n\t" \
  "s_add_u32 s62, %[xslo], 8192\n\t" \
  "s_addc_u32 s63, %[xshi], 0\n\t" \
  "global_load_dwordx4 v[176:179], %[voff], s[62:63] offset:0\n\t" \
  "s_add_u32 s62, %[xslo], 20480\n\t" \
  "s_addc_u32 s63, %[xshi], 0\n\t" \
  "global_load_dwordx4 v[232:235], %[voff], s[62:63] offset:2048\n\t" \
  "v_mfma_f32_16x16x32_bf16 v[44:47], v[124:127], v[152:155], v[44:47]\n\t" \
  "ds_read_b64_tr_b16 v[108:109], %[ad3] offset:20480\n\t" \
  "ds_read_b64_tr_b16 v[110:111], %[ad3] offset:20992\n\t" \
  "v_mfma_f32_16x16x32_bf16 v[76:79], v[124:127], v[208:211], v[76:79]\n\t" \
  "s_add_u32 s62, %[xslo], 8192\n\t" \
  "s_addc_u32 s63, %[xshi], 0\n\t" \
  "global_load_dwordx4 v[180:183], %[voff], s[62:63] offset:1024\n\t" \
  "s_add_u32 s62, %[xslo], 20480\n\t" \
  "s_addc_u32 s63, %[xshi], 0\n\t" \
  "global_load_dwordx4 v[236:239], %[voff], s[62:63] offset:3072\n\t" \
  "s_waitcnt lgkmcnt(6)\n\t" \
  "v_mfma_f32_16x16x32_bf16 v[48:51], v[128:131], v[152:155], v[48:51]\n\t" \
  "ds_read_b64_tr_b16 v[112:113], %[ad0] offset:28672\n\t" \
  "ds_read_b64_tr_b16 v[114:115], %[ad0] offset:29184\n\t" \
  "v_mfma_f32_16x16x32_bf16 v[80:83], v[128:131], v[208:211], v[80:83]\n\t" \
  "v_mfma_f32_16x16x32_bf16 v[52:55], v[132:135], v[152:155], v[52:55]\n\t" \
  "ds_read_b64_tr_b16 v[116:117], %[ad1] offset:28672\n\t" \
  "ds_read_b64_tr_b16 v[118:119], %[ad1] offset:29184\n\t" \
  "v_mfma_f32_16x16x32_bf16 v[84:87], v[132:135], v[208:211], v[84:87]\n\t" \
  "v_mfma_f32_16x16x32_bf16 v[56:59], v[136:139], v[152:155], v[56:59]\n\t" \
  "ds_read_b64_tr_b16 v[120:121], %[ad2] offset:28672\n\t" \
  "ds_read_b64_tr_b16 v[122:123], %[ad2] offset:29184\n\t" \
  "v_mfma_f32_16x16x32_bf16 v[88:91], v[136:139], v[208:211], v[88:91]\n\t" \
  "s_add_u32 s62, %[xslo], 114688\n\t" \
  "s_addc_u32 s63, %[xshi], 0\n\t" \
  "global_load_dwordx4 v[144:147], %[voff], s[62:63] offset:0\n\t" \
  "s_add_u32 s62, %[xslo], 126976\n\t" \
  "s_addc_u32 s63, %[xshi], 0\n\t" \
  "global_load_dwordx4 v[200:203], %[voff], s[62:63] offset:2048\n\t" \
  "v_mfma_f32_16x16x32_bf16 v[60:63], v[140:143], v[152:155], v[60:63]\n\t" \
  "ds_read_b64_tr_b16 v[124:125], %[ad3] offset:28672\n\t" \
  "ds_read_b64_tr_b16 v[126:127], %[ad3] offset:29184\n\t" \
  "v_mfma_f32_16x16x32_bf16 v[92:95], v[140:143], v[208:211], v[92:95]\n\t" \
  "s_add_u32 s62, %[xslo], 114688\n\t" \
  "s_addc_u32 s63, %[xshi], 0\n\t" \
  "global_load_dwordx4 v[148:151], %[voff], s[62:63] offset:1024\n\t" \
  "s_add_u32 s62, %[xslo], 126976\n\t" \
  "s_addc_u32 s63, %[xshi], 0\n\t" \
  "global_load_dwordx4 v[204:207], %[voff], s[62:63] offset:3072\n\t" \
  "s_waitcnt vmcnt(20)\n\t" \
  "s_waitcnt lgkmcnt(0)\n\t" \
  "s_mov_b64 s[64:65], exec\n\t" \
  "s_mov_b64 exec, 1\n\t" \
  "ds_add_u32 %[cnt], %[one]\n\t" \
  "s_mov_b64 exec, s[64:65]\n\t" \
  "s_mov_b32 s67, 0x400000\n\t" \
  "1:\n\t" \
  "ds_read_b32 %[vt], %[cnt]\n\t" \
  "s_waitcnt lgkmcnt(0)\n\t" \
  "v_readfirstlane_b32 s66, %[vt]\n\t" \
  "s_nop 3\n\t" \
  "s_cmp_ge_u32 s66, 12\n\t" \
  "s_cbranch_scc1 2f\n\t" \
  "s_sub_u32 s67, s67, 1\n\t" \
  "s_cbranch_scc1 2f\n\t" \
  "s_sleep 1\n\t" \
  "s_branch 1b\n\t" \
  "2:\n\t" \
  "s_waitcnt lgkmcnt(6)\n\t" \
  "v_mfma_f32_16x16x32_bf16 v[32:35], v[96:99], v[156:159], v[32:35]\n\t" \
  "ds_read_b64_tr_b16 v[128:129], %[ad0] offset:32768\n\t" \
  "ds_read_b64_tr_b16 v[130:131], %[ad0] offset:33280\n\t" \
  "v_mfma_f32_16x16x32_bf16 v[64:67], v[96:99], v[212:215], v[64:67]\n\t" \
  "v_mfma_f32_16x16x32_bf16 v[36:39], v[100:103], v[156:159], v[36:39]\n\t" \
  "ds_read_b64_tr_b16 v[132:133], %[ad1] offset:32768\n\t" \
  "ds_read_b64_tr_b16 v[134:135], %[ad1] offset:33280\n\t" \
  "v_mfma_f32_16x16x32_bf16 v[68:71], v[100:103], v[212:215], v[68:71]\n\t" \
  "v_mfma_f32_16x16x32_bf16 v[40:43], v[104:107], v[156:159], v[40:43]\n\t" \
  "ds_read_b64_tr_b16 v[136:137], %[ad2] offset:32768\n\t" \
  "ds_read_b64_tr_b16 v[138:139], %[ad2] offset:33280\n\t" \
  "v_mfma_f32_16x16x32_bf16 v[72:75], v[104:107], v[212:215], v[72:75]\n\t" \
  "v_mfma_f32_16x16x32_bf16 v[44:47], v[108:111], v[156:159], v[44:47]\n\t" \
  "ds_read_b64_tr_b16 v[140:141], %[ad3] offset:32768\n\t" \
  "ds_read_b64_tr_b16 v[142:143], %[ad3] offset:33280\n\t" \
  "v_mfma_f32_16x16x32_bf16 v[76:79], v[108:111], v[212:215], v[76:79]\n\t" \
  "s_waitcnt lgkmcnt(6)\n\t" \
  "v_mfma_f32_16x16x32_bf16 v[48:51], v[112:115], v[156:159], v[48:51]\n\t" \
  "ds_read_b64_tr_b16 v[96:97], %[ad0] offset:40960\n\t" \
  "ds_read_b64_tr_b16 v[98:99], %[ad0] offset:41472\n\t" \
  "v_mfma_f32_16x16x32_bf16 v[80:83], v[112:115], v[212:215], v[80:83]\n\t" \
  "v_mfma_f32_16x16x32_bf16 v[52:55], v[116:119], v[156:159], v[52:55]\n\t" \
  "ds_read_b64_tr_b16 v[100:101], %[ad1] offset:40960\n\t" \
  "ds_read_b64_tr_b16 v[102:103], %[ad1] offset:41472\n\t" \
  "v_mfma_f32_16x16x32_bf16 v[84:87], v[116:119], v[212:215], v[84:87]\n\t" \
  "v_mfma_f32_16x16x32_bf16 v[56:59], v[120:123], v[156:159], v[56:59]\n\t" \
  "ds_read_b64_tr_b16 v[104:105], %[ad2] offset:40960\n\t" \
  "ds_read_b64_tr_b16 v[106:107], %[ad2] offset:41472\n\t" \
  "v_mfma_f32_16x16x32_bf16 v[88:91], v[120:123], v[212:215], v[88:91]\n\t" \
  "v_mfma_f32_16x16x32_bf16 v[60:63], v[124:127], v[156:159], v[60:63]\n\t" \
  "ds_read_b64_tr_b16 v[108:109], %[ad3] offset:40960\n\t" \
  "ds_read_b64_tr_b16 v[110:111], %[ad3] offset:41472\n\t" \
  "v_mfma_f32_16x16x32_bf16 v[92:95], v[124:127], v[212:215], v[92:95]\n\t" \
  "s_waitcnt lgkmcnt(6)\n\t" \
  "v_mfma_f32_16x16x32_bf16 v[32:35], v[128:131], v[160:163], v[32:35]\n\t" \
  "ds_read_b64_tr_b16 v[112:113], %[ad0] offset:36864\n\t" \
  "ds_read_b64_tr_b16 v[114:115], %[ad0] offset:37376\n\t" \
  "v_mfma_f32_16x16x32_bf16 v[64:67], v[128:131], v[216:219], v[64:67]\n\t" \
  "s_add_u32 s60, %[gblo], 1146880\n\t" \
  "s_addc_u32 s61, %[gbhi], 0\n\t" \
  "s_add_u32 m0, %[ldo], 81920\n\t" \
  "s_nop 0\n\t" \
  "global_load_lds_dwordx4 %[lo0], s[60:61]\n\t" \
  "s_add_u32 s60, %[gblo], 1175552\n\t" \
  "s_addc_u32 s61, %[gbhi], 0\n\t" \
  "s_add_u32 m0, %[ldo], 82944\n\t" \
  "s_nop 0\n\t" \
  "global_load_lds_dwordx4 %[lo1], s[60:61]\n\t" \
  "v_mfma_f32_16x16x32_bf16 v[36:39], v[132:135], v[160:163], v[36:39]\n\t" \
  "ds_read_b64_tr_b16 v[116:117], %[ad1] offset:36864\n\t" \
  "ds_read_b64_tr_b16 v[118:119], %[ad1] offset:37376\n\t" \
  "v_mfma_f32_16x16x32_bf16 v[68:71], v[132:135], v[216:219], v[68:71]\n\t" \
  "s_add_u32 s60, %[ublo], 1146880\n\t" \
  "s_addc_u32 s61, %[ubhi], 0\n\t" \
  "s_add_u32 m0, %[ldo], 90112\n\t" \
  "s_nop 0\n\t" \
  "global_load_lds_dwordx4 %[lo0], s[60:61]\n\t" \
  "s_add_u32 s60, %[ublo], 1175552\n\t" \
  "s_addc_u32 s61, %[ubhi], 0\n\t" \
  "s_add_u32 m0, %[ldo], 91136\n\t" \
  "s_nop 0\n\t" \
  "global_load_lds_dwordx4 %[lo1], s[60:61]\n\t" \
  "v_mfma_f32_16x16x32_bf16 v[40:43], v[136:139], v[160:163], v[40:43]\n\t" \
  "ds_read_b64_tr_b16 v[120:121], %[ad2] offset:36864\n\t" \
  "ds_read_b64_tr_b16 v[122:123], %[ad2] offset:37376\n\t" \
  "v_mfma_f32_16x16x32_bf16 v[72:75], v[136:139], v[216:219], v[72:75]\n\t" \
  "s_add_u32 s62, %[xslo], 8192\n\t" \
  "s_addc_u32 s63, %[xshi], 0\n\t" \
  "global_load_dwordx4 v[184:187], %[voff], s[62:63] offset:2048\n\t" \
  "s_add_u32 s62, %[xslo], 24576\n\t" \
  "s_addc_u32 s63, %[xshi], 0\n\t" \
  "global_load_dwordx4 v[240:243], %[voff], s[62:63] offset:0\n\t" \
  "v_mfma_f32_16x16x32_bf16 v[44:47], v[140:143], v[160:163], v[44:47]\n\t" \
  "ds_read_b64_tr_b16 v[124:125], %[ad3] offset:36864\n\t" \
  "ds_read_b64_tr_b16 v[126:127], %[ad3] offset:37376\n\t" \
  "v_mfma_f32_16x16x32_bf16 v[76:79], v[140:143], v[216:219], v[76:79]\n\t" \
  "s_add_u32 s62, %[xslo], 8192\n\t" \
  "s_addc_u32 s63, %[xshi], 0\n\t" \
  "global_load_dwordx4 v[188:191], %[voff], s[62:63] offset:3072\n\t" \
  "s_add_u32 s62, %[xslo], 24576\n\t" \
  "s_addc_u32 s63, %[xshi], 0\n\t" \
  "global_load_dwordx4 v[244:247], %[voff], s[62:63] offset:1024\n\t" \
  "s_waitcnt lgkmcnt(6)\n\t" \
  "v_mfma_f32_16x16x32_bf16 v[48:51], v[96:99], v[160:163], v[48:51]\n\t" \
  "ds_read_b64_tr_b16 v[128:129], %[ad0] offset:45056\n\t" \
  "ds_read_b64_tr_b16 v[130:131], %[ad0] offset:45568\n\t" \
  "v_mfma_f32_16x16x32_bf16 v[80:83], v[96:99], v[216:219], v[80:83]\n\t" \
  "v_mfma_f32_16x16x32_bf16 v[52:55], v[100:103], v[160:163], v[52:55]\n\t" \
  "ds_read_b64_tr_b16 v[132:133], %[ad1] offset:45056\n\t" \
  "ds_read_b64_tr_b16 v[134:135], %[ad1] offset:45568\n\t" \
  "v_mfma_f32_16x16x32_bf16 v[84:87], v[100:103], v[216:219], v[84:87]\n\t" \
  "v_mfma_f32_16x16x32_bf16 v[56:59], v[104:107], v[160:163], v[56:59]\n\t" \
  "ds_read_b64_tr_b16 v[136:137], %[ad2] offset:45056\n\t" \
  "ds_read_b64_tr_b16 v[138:139], %[ad2] offset:45568\n\t" \
  "v_mfma_f32_16x16x32_bf16 v[88:91], v[104:107], v[216:219], v[88:91]\n\t" \
  "s_add_u32 s62, %[xslo], 114688\n\t" \
  "s_addc_u32 s63, %[xshi], 0\n\t" \
  "global_load_dwordx4 v[152:155], %[voff], s[62:63] offset:2048\n\t" \
  "s_add_u32 s62, %[xslo], 131072\n\t" \
  "s_addc_u32 s63, %[xshi], 0\n\t" \
  "global_load_dwordx4 v[208:211], %[voff], s[62:63] offset:0\n\t" \
  "v_mfma_f32_16x16x32_bf16 v[60:63], v[108:111], v[160:163], v[60:63]\n\t" \
  "ds_read_b64_tr_b16 v[140:141], %[ad3] offset:45056\n\t" \
  "ds_read_b64_tr_b16 v[142:143], %[ad3] offset:45568\n\t" \
  "v_mfma_f32_16x16x32_bf16 v[92:95], v[108:111], v[216:219], v[92:95]\n\t" \
  "s_add_u32 s62, %[xslo], 114688\n\t" \
  "s_addc_u32 s63, %[xshi], 0\n\t" \
  "global_load_dwordx4 v[156:159], %[voff], s[62:63] offset:3072\n\t" \
  "s_add_u32 s62, %[xslo], 131072\n\t" \
  "s_addc_u32 s63, %[xshi], 0\n\t" \
  "global_load_dwordx4 v[212:215], %[voff], s[62:63] offset:1024\n\t" \
  "s_waitcnt vmcnt(24)\n\t" \
  "s_waitcnt lgkmcnt(0)\n\t" \
  "s_mov_b64 s[64:65], exec\n\t" \
  "s_mov_b64 exec, 1\n\t" \
  "ds_add_u32 %[cnt], %[one]\n\t" \
  "s_mov_b64 exec, s[64:65]\n\t" \
  "s_mov_b32 s67, 0x400000\n\t" \
  "1:\n\t" \
  "ds_read_b32 %[vt], %[cnt]\n\t" \
  "s_waitcnt lgkmcnt(0)\n\t" \
  "v_readfirstlane_b32 s66, %[vt]\n\t" \
  "s_nop 3\n\t" \
  "s_cmp_ge_u32 s66, 16\n\t" \
  "s_cbranch_scc1 2f\n\t" \
  "s_sub_u32 s67, s67, 1\n\t" \
  "s_cbranch_scc1 2f\n\t" \
  "s_sleep 1\n\t" \
  "s_branch 1b\n\t" \
  "2:\n\t" \
  "s_waitcnt lgkmcnt(6)\n\t" \
  "v_mfma_f32_16x16x32_bf16 v[32:35], v[112:115], v[164:167], v[32:35]\n\t" \
  "ds_read_b64_tr_b16 v[96:97], %[ad0] offset:49152\n\t" \
  "ds_read_b64_tr_b16 v[98:99], %[ad0] offset:49664\n\t" \
  "v_mfma_f32_16x16x32_bf16 v[64:67], v[112:115], v[220:223], v[64:67]\n\t" \
  "v_mfma_f32_16x16x32_bf16 v[36:39], v[116:119], v[164:167], v[36:39]\n\t" \
  "ds_read_b64_tr_b16 v[100:101], %[ad1] offset:49152\n\t" \
  "ds_read_b64_tr_b16 v[102:103], %[ad1] offset:49664\n\t" \
  "v_mfma_f32_16x16x32_bf16 v[68:71], v[116:119], v[220:223], v[68:71]\n\t" \
  "v_mfma_f32_16x16x32_bf16 v[40:43], v[120:123], v[164:167], v[40:43]\n\t" \
  "ds_read_b64_tr_b16 v[104:105], %[ad2] offset:49152\n\t" \
  "ds_read_b64_tr_b16 v[106:107], %[ad2] offset:49664\n\t" \
  "v_mfma_f32_16x16x32_bf16 v[72:75], v[120:123], v[220:223], v[72:75]\n\t" \
  "v_mfma_f32_16x16x32_bf16 v[44:47], v[124:127], v[164:167], v[44:47]\n\t" \
  "ds_read_b64_tr_b16 v[108:109], %[ad3] offset:49152\n\t" \
  "ds_read_b64_tr_b16 v[110:111], %[ad3] offset:49664\n\t" \
  "v_mfma_f32_16x16x32_bf16 v[76:79], v[124:127], v[220:223], v[76:79]\n\t" \
  "s_waitcnt lgkmcnt(6)\n\t" \
  "v_mfma_f32_16x16x32_bf16 v[48:51], v[128:131], v[164:167], v[48:51]\n\t" \
  "ds_read_b64_tr_b16 v[112:113], %[ad0] offset:57344\n\t" \
  "ds_read_b64_tr_b16 v[114:115], %[ad0] offset:57856\n\t" \
  "v_mfma_f32_16x16x32_bf16 v[80:83], v[128:131], v[220:223], v[80:83]\n\t" \
  "v_mfma_f32_16x16x32_bf16 v[52:55], v[132:135], v[164:167], v[52:55]\n\t" \
  "ds_read_b64_tr_b16 v[116:117], %[ad1] offset:57344\n\t" \
  "ds_read_b64_tr_b16 v[118:119], %[ad1] offset:57856\n\t" \
  "v_mfma_f32_16x16x32_bf16 v[84:87], v[132:135], v[220:223], v[84:87]\n\t" \
  "v_mfma_f32_16x16x32_bf16 v[56:59], v[136:139], v[164:167], v[56:59]\n\t" \
  "ds_read_b64_tr_b16 v[120:121], %[ad2] offset:57344\n\t" \
  "ds_read_b64_tr_b16 v[122:123], %[ad2] offset:57856\n\t" \
  "v_mfma_f32_16x16x32_bf16 v[88:91], v[136:139], v[220:223], v[88:91]\n\t" \
  "v_mfma_f32_16x16x32_bf16 v[60:63], v[140:143], v[164:167], v[60:63]\n\t" \
  "ds_read_b64_tr_b16 v[124:125], %[ad3] offset:57344\n\t" \
  "ds_read_b64_tr_b16 v[126:127], %[ad3] offset:57856\n\t" \
  "v_mfma_f32_16x16x32_bf16 v[92:95], v[140:143], v[220:223], v[92:95]\n\t" \
  "s_waitcnt lgkmcnt(6)\n\t" \
  "v_mfma_f32_16x16x32_bf16 v[32:35], v[96:99], v[168:171], v[32:35]\n\t" \
  "ds_read_b64_tr_b16 v[128:129], %[ad0] offset:53248\n\t" \
  "ds_read_b64_tr_b16 v[130:131], %[ad0] offset:53760\n\t" \
  "v_mfma_f32_16x16x32_bf16 v[64:67], v[96:99], v[224:227], v[64:67]\n\t" \
  "s_add_u32 s60, %[gblo], 1376256\n\t" \
  "s_addc_u32 s61, %[gbhi], 0\n\t" \
  "s_add_u32 m0, %[ldo], 98304\n\t" \
  "s_nop 0\n\t" \
  "global_load_lds_dwordx4 %[lo0], s[60:61]\n\t" \
  "s_add_u32 s60, %[gblo], 1404928\n\t" \
  "s_addc_u32 s61, %[gbhi], 0\n\t" \
  "s_add_u32 m0, %[ldo], 99328\n\t" \
  "s_nop 0\n\t" \
  "global_load_lds_dwordx4 %[lo1], s[60:61]\n\t" \
  "v_mfma_f32_16x16x32_bf16 v[36:39], v[100:103], v[168:171], v[36:39]\n\t" \
  "ds_read_b64_tr_b16 v[132:133], %[ad1] offset:53248\n\t" \
  "ds_read_b64_tr_b16 v[134:135], %[ad1] offset:53760\n\t" \
  "v_mfma_f32_16x16x32_bf16 v[68:71], v[100:103], v[224:227], v[68:71]\n\t" \
  "s_add_u32 s60, %[ublo], 1376256\n\t" \
  "s_addc_u32 s61, %[ubhi], 0\n\t" \
  "s_add_u32 m0, %[ldo], 106496\n\t" \
  "s_nop 0\n\t" \
  "global_load_lds_dwordx4 %[lo0], s[60:61]\n\t" \
  "s_add_u32 s60, %[ublo], 1404928\n\t" \
  "s_addc_u32 s61, %[ubhi], 0\n\t" \
  "s_add_u32 m0, %[ldo], 107520\n\t" \
  "s_nop 0\n\t" \
  "global_load_lds_dwordx4 %[lo1], s[60:61]\n\t" \
  "v_mfma_f32_16x16x32_bf16 v[40:43], v[104:107], v[168:171], v[40:43]\n\t" \
  "ds_read_b64_tr_b16 v[136:137], %[ad2] offset:53248\n\t" \
  "ds_read_b64_tr_b16 v[138:139], %[ad2] offset:53760\n\t" \
  "v_mfma_f32_16x16x32_bf16 v[72:75], v[104:107], v[224:227], v[72:75]\n\t" \
  "s_add_u32 s62, %[xslo], 12288\n\t" \
  "s_addc_u32 s63, %[xshi], 0\n\t" \
  "global_load_dwordx4 v[192:195], %[voff], s[62:63] offset:0\n\t" \
  "s_add_u32 s62, %[xslo], 24576\n\t" \
  "s_addc_u32 s63, %[xshi], 0\n\t" \
  "global_load_dwordx4 v[248:251], %[voff], s[62:63] offset:2048\n\t" \
  "v_mfma_f32_16x16x32_bf16 v[44:47], v[108:111], v[168:171], v[44:47]\n\t" \
  "ds_read_b64_tr_b16 v[140:141], %[ad3] offset:53248\n\t" \
  "ds_read_b64_tr_b16 v[142:143], %[ad3] offset:53760\n\t" \
  "v_mfma_f32_16x16x32_bf16 v[76:79], v[108:111], v[224:227], v[76:79]\n\t" \
  "s_add_u32 s62, %[xslo], 12288\n\t" \
  "s_addc_u32 s63, %[xshi], 0\n\t" \
  "global_load_dwordx4 v[196:199], %[voff], s[62:63] offset:1024\n\t" \
  "s_add_u32 s62, %[xslo], 24576\n\t" \
  "s_addc_u32 s63, %[xshi], 0\n\t" \
  "global_load_dwordx4 v[252:255], %[voff], s[62:63] offset:3072\n\t" \
  "s_waitcnt lgkmcnt(6)\n\t" \
  "v_mfma_f32_16x16x32_bf16 v[48:51], v[112:115], v[168:171], v[48:51]\n\t" \
  "ds_read_b64_tr_b16 v[96:97], %[ad0] offset:61440\n\t" \
  "ds_read_b64_tr_b16 v[98:99], %[ad0] offset:61952\n\t" \
  "v_mfma_f32_16x16x32_bf16 v[80:83], v[112:115], v[224:227], v[80:83]\n\t" \
  "v_mfma_f32_16x16x32_bf16 v[52:55], v[116:119], v[168:171], v[52:55]\n\t" \
  "ds_read_b64_tr_b16 v[100:101], %[ad1] offset:61440\n\t" \
  "ds_read_b64_tr_b16 v[102:103], %[ad1] offset:61952\n\t" \
  "v_mfma_f32_16x16x32_bf16 v[84:87], v[116:119], v[224:227], v[84:87]\n\t" \
  "v_mfma_f32_16x16x32_bf16 v[56:59], v[120:123], v[168:171], v[56:59]\n\t" \
  "ds_read_b64_tr_b16 v[104:105], %[ad2] offset:61440\n\t" \
  "ds_read_b64_tr_b16 v[106:107], %[ad2] offset:61952\n\t" \
  "v_mfma_f32_16x16x32_bf16 v[88:91], v[120:123], v[224:227], v[88:91]\n\t" \
  "s_add_u32 s62, %[xslo], 118784\n\t" \
  "s_addc_u32 s63, %[xshi], 0\n\t" \
  "global_load_dwordx4 v[160:163], %[voff], s[62:63] offset:0\n\t" \
  "s_add_u32 s62, %[xslo], 131072\n\t" \
  "s_addc_u32 s63, %[xshi], 0\n\t" \
  "global_load_dwordx4 v[216:219], %[voff], s[62:63] offset:2048\n\t" \
  "v_mfma_f32_16x16x32_bf16 v[60:63], v[124:127], v[168:171], v[60:63]\n\t" \
  "ds_read_b64_tr_b16 v[108:109], %[ad3] offset:61440\n\t" \
  "ds_read_b64_tr_b16 v[110:111], %[ad3] offset:61952\n\t" \
  "v_mfma_f32_16x16x32_bf16 v[92:95], v[124:127], v[224:227], v[92:95]\n\t" \
  "s_add_u32 s62, %[xslo], 118784\n\t" \
  "s_addc_u32 s63, %[xshi], 0\n\t" \
  "global_load_dwordx4 v[164:167], %[voff], s[62:63] offset:1024\n\t" \
  "s_add_u32 s62, %[xslo], 131072\n\t" \
  "s_addc_u32 s63, %[xshi], 0\n\t" \
  "global_load_dwordx4 v[220:223], %[voff], s[62:63] offset:3072\n\t" \
  "s_waitcnt vmcnt(28)\n\t" \
  "s_waitcnt lgkmcnt(0)\n\t" \
  "s_mov_b64 s[64:65], exec\n\t" \
  "s_mov_b64 exec, 1\n\t" \
  "ds_add_u32 %[cnt], %[one]\n\t" \
  "s_mov_b64 exec, s[64:65]\n\t" \
  "s_mov_b32 s67, 0x400000\n\t" \
  "1:\n\t" \
  "ds_read_b32 %[vt], %[cnt]\n\t" \
  "s_waitcnt lgkmcnt(0)\n\t" \
  "v_readfirstlane_b32 s66, %[vt]\n\t" \
  "s_nop 3\n\t" \
  "s_cmp_ge_u32 s66, 20\n\t" \
  "s_cbranch_scc1 2f\n\t" \
  "s_sub_u32 s67, s67, 1\n\t" \
  "s_cbranch_scc1 2f\n\t" \
  "s_sleep 1\n\t" \
  "s_branch 1b\n\t" \
  "2:\n\t" \
  "s_waitcnt lgkmcnt(6)\n\t" \
  "v_add_u32 %[ad0], 0x10000, %[ad0]\n\t" \
  "v_add_u32 %[ad1], 0x10000, %[ad1]\n\t" \
  "v_add_u32 %[ad2], 0x10000, %[ad2]\n\t" \
  "v_add_u32 %[ad3], 0x10000, %[ad3]\n\t" \
  "v_mfma_f32_16x16x32_bf16 v[32:35], v[128:131], v[172:175], v[32:35]\n\t" \
  "ds_read_b64_tr_b16 v[112:113], %[ad0] offset:0\n\t" \
  "ds_read_b64_tr_b16 v[114:115], %[ad0] offset:512\n\t" \
  "v_mfma_f32_16x16x32_bf16 v[64:67], v[128:131], v[228:231], v[64:67]\n\t" \
  "v_mfma_f32_16x16x32_bf16 v[36:39], v[132:135], v[172:175], v[36:39]\n\t" \
  "ds_read_b64_tr_b16 v[116:117], %[ad1] offset:0\n\t" \
  "ds_read_b64_tr_b16 v[118:119], %[ad1] offset:512\n\t" \
  "v_mfma_f32_16x16x32_bf16 v[68:71], v[132:135], v[228:231], v[68:71]\n\t" \
  "v_mfma_f32_16x16x32_bf16 v[40:43], v[136:139], v[172:175], v[40:43]\n\t" \
  "ds_read_b64_tr_b16 v[120:121], %[ad2] offset:0\n\t" \
  "ds_read_b64_tr_b16 v[122:123], %[ad2] offset:512\n\t" \
  "v_mfma_f32_16x16x32_bf16 v[72:75], v[136:139], v[228:231], v[72:75]\n\t" \
  "v_mfma_f32_16x16x32_bf16 v[44:47], v[140:143], v[172:175], v[44:47]\n\t" \
  "ds_read_b64_tr_b16 v[124:125], %[ad3] offset:0\n\t" \
  "ds_read_b64_tr_b16 v[126:127], %[ad3] offset:512\n\t" \
  "v_mfma_f32_16x16x32_bf16 v[76:79], v[140:143], v[228:231], v[76:79]\n\t" \
  "s_waitcnt lgkmcnt(6)\n\t" \
  "v_mfma_f32_16x16x32_bf16 v[48:51], v[96:99], v[172:175], v[48:51]\n\t" \
  "ds_read_b64_tr_b16 v[128:129], %[ad0] offset:8192\n\t" \
  "ds_read_b64_tr_b16 v[130:131], %[ad0] offset:8704\n\t" \
  "v_mfma_f32_16x16x32_bf16 v[80:83], v[96:99], v[228:231], v[80:83]\n\t" \
  "v_mfma_f32_16x16x32_bf16 v[52:55], v[100:103], v[172:175], v[52:55]\n\t" \
  "ds_read_b64_tr_b16 v[132:133], %[ad1] offset:8192\n\t" \
  "ds_read_b64_tr_b16 v[134:135], %[ad1] offset:8704\n\t" \
  "v_mfma_f32_16x16x32_bf16 v[84:87], v[100:103], v[228:231], v[84:87]\n\t" \
  "v_mfma_f32_16x16x32_bf16 v[56:59], v[104:107], v[172:175], v[56:59]\n\t" \
  "ds_read_b64_tr_b16 v[136:137], %[ad2] offset:8192\n\t" \
  "ds_read_b64_tr_b16 v[138:139], %[ad2] offset:8704\n\t" \
  "v_mfma_f32_16x16x32_bf16 v[88:91], v[104:107], v[228:231], v[88:91]\n\t" \
  "v_mfma_f32_16x16x32_bf16 v[60:63], v[108:111], v[172:175], v[60:63]\n\t" \
  "ds_read_b64_tr_b16 v[140:141], %[ad3] offset:8192\n\t" \
  "ds_read_b64_tr_b16 v[142:143], %[ad3] offset:8704\n\t" \
  "v_mfma_f32_16x16x32_bf16 v[92:95], v[108:111], v[228:231], v[92:95]\n\t" \
  "s_waitcnt lgkmcnt(6)\n\t" \
  "v_mfma_f32_16x16x32_bf16 v[32:35], v[112:115], v[176:179], v[32:35]\n\t" \
  "ds_read_b64_tr_b16 v[96:97], %[ad0] offset:4096\n\t" \
  "ds_read_b64_tr_b16 v[98:99], %[ad0] offset:4608\n\t" \
  "v_mfma_f32_16x16x32_bf16 v[64:67], v[112:115], v[232:235], v[64:67]\n\t" \
  "v_mfma_f32_16x16x32_bf16 v[36:39], v[116:119], v[176:179], v[36:39]\n\t" \
  "ds_read_b64_tr_b16 v[100:101], %[ad1] offset:4096\n\t" \
  "ds_read_b64_tr_b16 v[102:103], %[ad1] offset:4608\n\t" \
  "v_mfma_f32_16x16x32_bf16 v[68:71], v[116:119], v[232:235], v[68:71]\n\t" \
  "v_mfma_f32_16x16x32_bf16 v[40:43], v[120:123], v[176:179], v[40:43]\n\t" \
  "ds_read_b64_tr_b16 v[104:105], %[ad2] offset:4096\n\t" \
  "ds_read_b64_tr_b16 v[106:107], %[ad2] offset:4608\n\t" \
  "v_mfma_f32_16x16x32_bf16 v[72:75], v[120:123], v[232:235], v[72:75]\n\t" \
  "v_mfma_f32_16x16x32_bf16 v[44:47], v[124:127], v[176:179], v[44:47]\n\t" \
  "ds_read_b64_tr_b16 v[108:109], %[ad3] offset:4096\n\t" \
  "ds_read_b64_tr_b16 v[110:111], %[ad3] offset:4608\n\t" \
  "v_mfma_f32_16x16x32_bf16 v[76:79], v[124:127], v[232:235], v[76:79]\n\t" \
  "s_waitcnt lgkmcnt(6)\n\t" \
  "v_mfma_f32_16x16x32_bf16 v[48:51], v[128:131], v[176:179], v[48:51]\n\t" \
  "ds_read_b64_tr_b16 v[112:113], %[ad0] offset:12288\n\t" \
  "ds_read_b64_tr_b16 v[114:115], %[ad0] offset:12800\n\t" \
  "v_mfma_f32_16x16x32_bf16 v[80:83], v[128:131], v[232:235], v[80:83]\n\t" \
  "v_mfma_f32_16x16x32_bf16 v[52:55], v[132:135], v[176:179], v[52:55]\n\t" \
  "ds_read_b64_tr_b16 v[116:117], %[ad1] offset:12288\n\t" \
  "ds_read_b64_tr_b16 v[118:119], %[ad1] offset:12800\n\t" \
  "v_mfma_f32_16x16x32_bf16 v[84:87], v[132:135], v[232:235], v[84:87]\n\t" \
  "v_mfma_f32_16x16x32_bf16 v[56:59], v[136:139], v[176:179], v[56:59]\n\t" \
  "ds_read_b64_tr_b16 v[120:121], %[ad2] offset:12288\n\t" \
  "ds_read_b64_tr_b16 v[122:123], %[ad2] offset:12800\n\t" \
  "v_mfma_f32_16x16x32_bf16 v[88:91], v[136:139], v[232:235], v[88:91]\n\t" \
  "s_add_u32 s62, %[xslo], 118784\n\t" \
  "s_addc_u32 s63, %[xshi], 0\n\t" \
  "global_load_dwordx4 v[168:171], %[voff], s[62:63] offset:2048\n\t" \
  "s_add_u32 s62, %[xslo], 135168\n\t" \
  "s_addc_u32 s63, %[xshi], 0\n\t" \
  "global_load_dwordx4 v[224:227], %[voff], s[62:63] offset:0\n\t" \
  "v_mfma_f32_16x16x32_bf16 v[60:63], v[140:143], v[176:179], v[60:63]\n\t" \
  "ds_read_b64_tr_b16 v[124:125], %[ad3] offset:12288\n\t" \
  "ds_read_b64_tr_b16 v[126:127], %[ad3] offset:12800\n\t" \
  "v_mfma_f32_16x16x32_bf16 v[92:95], v[140:143], v[232:235], v[92:95]\n\t" \
  "s_add_u32 s62, %[xslo], 118784\n\t" \
  "s_addc_u32 s63, %[xshi], 0\n\t" \
  "global_load_dwordx4 v[172:175], %[voff], s[62:63] offset:3072\n\t" \
  "s_add_u32 s62, %[xslo], 135168\n\t" \
  "s_addc_u32 s63, %[xshi], 0\n\t" \
  "global_load_dwordx4 v[228:231], %[voff], s[62:63] offset:1024\n\t" \
  "s_waitcnt vmcnt(20)\n\t" \
  "s_waitcnt lgkmcnt(0)\n\t" \
  "s_mov_b64 s[64:65], exec\n\t" \
  "s_mov_b64 exec, 1\n\t" \
  "ds_add_u32 %[cnt], %[one]\n\t" \
  "s_mov_b64 exec, s[64:65]\n\t" \
  "s_mov_b32 s67, 0x400000\n\t" \
  "1:\n\t" \
  "ds_read_b32 %[vt], %[cnt]\n\t" \
  "s_waitcnt lgkmcnt(0)\n\t" \
  "v_readfirstlane_b32 s66, %[vt]\n\t" \
  "s_nop 3\n\t" \
  "s_cmp_ge_u32 s66, 24\n\t" \
  "s_cbranch_scc1 2f\n\t" \
  "s_sub_u32 s67, s67, 1\n\t" \
  "s_cbranch_scc1 2f\n\t" \
  "s_sleep 1\n\t" \
  "s_branch 1b\n\t" \
  "2:\n\t" \
  "s_waitcnt lgkmcnt(6)\n\t" \
  "v_mfma_f32_16x16x32_bf16 v[32:35], v[96:99], v[180:183], v[32:35]\n\t" \
  "ds_read_b64_tr_b16 v[128:129], %[ad0] offset:16384\n\t" \
  "ds_read_b64_tr_b16 v[130:131], %[ad0] offset:16896\n\t" \
  "v_mfma_f32_16x16x32_bf16 v[64:67], v[96:99], v[236:239], v[64:67]\n\t" \
  "v_mfma_f32_16x16x32_bf16 v[36:39], v[100:103], v[180:183], v[36:39]\n\t" \
  "ds_read_b64_tr_b16 v[132:133], %[ad1] offset:16384\n\t" \
  "ds_read_b64_tr_b16 v[134:135], %[ad1] offset:16896\n\t" \
  "v_mfma_f32_16x16x32_bf16 v[68:71], v[100:103], v[236:239], v[68:71]\n\t" \
  "v_mfma_f32_16x16x32_bf16 v[40:43], v[104:107], v[180:183], v[40:43]\n\t" \
  "ds_read_b64_tr_b16 v[136:137], %[ad2] offset:16384\n\t" \
  "ds_read_b64_tr_b16 v[138:139], %[ad2] offset:16896\n\t" \
  "v_mfma_f32_16x16x32_bf16 v[72:75], v[104:107], v[236:239], v[72:75]\n\t" \
  "v_mfma_f32_16x16x32_bf16 v[44:47], v[108:111], v[180:183], v[44:47]\n\t" \
  "ds_read_b64_tr_b16 v[140:141], %[ad3] offset:16384\n\t" \
  "ds_read_b64_tr_b16 v[142:143], %[ad3] offset:16896\n\t" \
  "v_mfma_f32_16x16x32_bf16 v[76:79], v[108:111], v[236:239], v[76:79]\n\t" \
  "s_waitcnt lgkmcnt(6)\n\t" \
  "v_mfma_f32_16x16x32_bf16 v[48:51], v[112:115], v[180:183], v[48:51]\n\t" \
  "ds_read_b64_tr_b16 v[96:97], %[ad0] offset:24576\n\t" \
  "ds_read_b64_tr_b16 v[98:99], %[ad0] offset:25088\n\t" \
  "v_mfma_f32_16x16x32_bf16 v[80:83], v[112:115], v[236:239], v[80:83]\n\t" \
  "v_mfma_f32_16x16x32_bf16 v[52:55], v[116:119], v[180:183], v[52:55]\n\t" \
  "ds_read_b64_tr_b16 v[100:101], %[ad1] offset:24576\n\t" \
  "ds_read_b64_tr_b16 v[102:103], %[ad1] offset:25088\n\t" \
  "v_mfma_f32_16x16x32_bf16 v[84:87], v[116:119], v[236:239], v[84:87]\n\t" \
  "v_mfma_f32_16x16x32_bf16 v[56:59], v[120:123], v[180:183], v[56:59]\n\t" \
  "ds_read_b64_tr_b16 v[104:105], %[ad2] offset:24576\n\t" \
  "ds_read_b64_tr_b16 v[106:107], %[ad2] offset:25088\n\t" \
  "v_mfma_f32_16x16x32_bf16 v[88:91], v[120:123], v[236:239], v[88:91]\n\t" \
  "v_mfma_f32_16x16x32_bf16 v[60:63], v[124:127], v[180:183], v[60:63]\n\t" \
  "ds_read_b64_tr_b16 v[108:109], %[ad3] offset:24576\n\t" \
  "ds_read_b64_tr_b16 v[110:111], %[ad3] offset:25088\n\t" \
  "v_mfma_f32_16x16x32_bf16 v[92:95], v[124:127], v[236:239], v[92:95]\n\t" \
  "s_waitcnt lgkmcnt(6)\n\t" \
  "v_mfma_f32_16x16x32_bf16 v[32:35], v[128:131], v[184:187], v[32:35]\n\t" \
  "ds_read_b64_tr_b16 v[112:113], %[ad0] offset:20480\n\t" \
  "ds_read_b64_tr_b16 v[114:115], %[ad0] offset:20992\n\t" \
  "v_mfma_f32_16x16x32_bf16 v[64:67], v[128:131], v[240:243], v[64:67]\n\t" \
  "v_mfma_f32_16x16x32_bf16 v[36:39], v[132:135], v[184:187], v[36:39]\n\t" \
  "ds_read_b64_tr_b16 v[116:117], %[ad1] offset:20480\n\t" \
  "ds_read_b64_tr_b16 v[118:119], %[ad1] offset:20992\n\t" \
  "v_mfma_f32_16x16x32_bf16 v[68:71], v[132:135], v[240:243], v[68:71]\n\t" \
  "v_mfma_f32_16x16x32_bf16 v[40:43], v[136:139], v[184:187], v[40:43]\n\t" \
  "ds_read_b64_tr_b16 v[120:121], %[ad2] offset:20480\n\t" \
  "ds_read_b64_tr_b16 v[122:123], %[ad2] offset:20992\n\t" \
  "v_mfma_f32_16x16x32_bf16 v[72:75], v[136:139], v[240:243], v[72:75]\n\t" \
  "v_mfma_f32_16x16x32_bf16 v[44:47], v[140:143], v[184:187], v[44:47]\n\t" \
  "ds_read_b64_tr_b16 v[124:125], %[ad3] offset:20480\n\t" \
  "ds_read_b64_tr_b16 v[126:127], %[ad3] offset:20992\n\t" \
  "v_mfma_f32_16x16x32_bf16 v[76:79], v[140:143], v[240:243], v[76:79]\n\t" \
  "s_waitcnt lgkmcnt(6)\n\t" \
  "v_mfma_f32_16x16x32_bf16 v[48:51], v[96:99], v[184:187], v[48:51]\n\t" \
  "ds_read_b64_tr_b16 v[128:129], %[ad0] offset:28672\n\t" \
  "ds_read_b64_tr_b16 v[130:131], %[ad0] offset:29184\n\t" \
  "v_mfma_f32_16x16x32_bf16 v[80:83], v[96:99], v[240:243], v[80:83]\n\t" \
  "v_mfma_f32_16x16x32_bf16 v[52:55], v[100:103], v[184:187], v[52:55]\n\t" \
  "ds_read_b64_tr_b16 v[132:133], %[ad1] offset:28672\n\t" \
  "ds_read_b64_tr_b16 v[134:135], %[ad1] offset:29184\n\t" \
  "v_mfma_f32_16x16x32_bf16 v[84:87], v[100:103], v[240:243], v[84:87]\n\t" \
  "v_mfma_f32_16x16x32_bf16 v[56:59], v[104:107], v[184:187], v[56:59]\n\t" \
  "ds_read_b64_tr_b16 v[136:137], %[ad2] offset:28672\n\t" \
  "ds_read_b64_tr_b16 v[138:139], %[ad2] offset:29184\n\t" \
  "v_mfma_f32_16x16x32_bf16 v[88:91], v[104:107], v[240:243], v[88:91]\n\t" \
  "s_add_u32 s62, %[xslo], 122880\n\t" \
  "s_addc_u32 s63, %[xshi], 0\n\t" \
  "global_load_dwordx4 v[176:179], %[voff], s[62:63] offset:0\n\t" \
  "s_add_u32 s62, %[xslo], 135168\n\t" \
  "s_addc_u32 s63, %[xshi], 0\n\t" \
  "global_load_dwordx4 v[232:235], %[voff], s[62:63] offset:2048\n\t" \
  "v_mfma_f32_16x16x32_bf16 v[60:63], v[108:111], v[184:187], v[60:63]\n\t" \
  "ds_read_b64_tr_b16 v[140:141], %[ad3] offset:28672\n\t" \
  "ds_read_b64_tr_b16 v[142:143], %[ad3] offset:29184\n\t" \
  "v_mfma_f32_16x16x32_bf16 v[92:95], v[108:111], v[240:243], v[92:95]\n\t" \
  "s_add_u32 s62, %[xslo], 122880\n\t" \
  "s_addc_u32 s63, %[xshi], 0\n\t" \
  "global_load_dwordx4 v[180:183], %[voff], s[62:63] offset:1024\n\t" \
  "s_add_u32 s62, %[xslo], 135168\n\t" \
  "s_addc_u32 s63, %[xshi], 0\n\t" \
  "global_load_dwordx4 v[236:239], %[voff], s[62:63] offset:3072\n\t" \
  "s_waitcnt vmcnt(12)\n\t" \
  "s_waitcnt lgkmcnt(0)\n\t" \
  "s_mov_b64 s[64:65], exec\n\t" \
  "s_mov_b64 exec, 1\n\t" \
  "ds_add_u32 %[cnt], %[one]\n\t" \
  "s_mov_b64 exec, s[64:65]\n\t" \
  "s_mov_b32 s67, 0x400000\n\t" \
  "1:\n\t" \
  "ds_read_b32 %[vt], %[cnt]\n\t" \
  "s_waitcnt lgkmcnt(0)\n\t" \
  "v_readfirstlane_b32 s66, %[vt]\n\t" \
  "s_nop 3\n\t" \
  "s_cmp_ge_u32 s66, 28\n\t" \
  "s_cbranch_scc1 2f\n\t" \
  "s_sub_u32 s67, s67, 1\n\t" \
  "s_cbranch_scc1 2f\n\t" \
  "s_sleep 1\n\t" \
  "s_branch 1b\n\t" \
  "2:\n\t" \
  "s_waitcnt lgkmcnt(6)\n\t" \
  "v_mfma_f32_16x16x32_bf16 v[32:35], v[112:115], v[188:191], v[32:35]\n\t" \
  "ds_read_b64_tr_b16 v[96:97], %[ad0] offset:32768\n\t" \
  "ds_read_b64_tr_b16 v[98:99], %[ad0] offset:33280\n\t" \
  "v_mfma_f32_16x16x32_bf16 v[64:67], v[112:115], v[244:247], v[64:67]\n\t" \
  "v_mfma_f32_16x16x32_bf16 v[36:39], v[116:119], v[188:191], v[36:39]\n\t" \
  "ds_read_b64_tr_b16 v[100:101], %[ad1] offset:32768\n\t" \
  "ds_read_b64_tr_b16 v[102:103], %[ad1] offset:33280\n\t" \
  "v_mfma_f32_16x16x32_bf16 v[68:71], v[116:119], v[244:247], v[68:71]\n\t" \
  "v_mfma_f32_16x16x32_bf16 v[40:43], v[120:123], v[188:191], v[40:43]\n\t" \
  "ds_read_b64_tr_b16 v[104:105], %[ad2] offset:32768\n\t" \
  "ds_read_b64_tr_b16 v[106:107], %[ad2] offset:33280\n\t" \
  "v_mfma_f32_16x16x32_bf16 v[72:75], v[120:123], v[244:247], v[72:75]\n\t" \
  "v_mfma_f32_16x16x32_bf16 v[44:47], v[124:127], v[188:191], v[44:47]\n\t" \
  "ds_read_b64_tr_b16 v[108:109], %[ad3] offset:32768\n\t" \
  "ds_read_b64_tr_b16 v[110:111], %[ad3] offset:33280\n\t" \
  "v_mfma_f32_16x16x32_bf16 v[76:79], v[124:127], v[244:247], v[76:79]\n\t" \
  "s_waitcnt lgkmcnt(6)\n\t" \
  "v_mfma_f32_16x16x32_bf16 v[48:51], v[128:131], v[188:191], v[48:51]\n\t" \
  "ds_read_b64_tr_b16 v[112:113], %[ad0] offset:40960\n\t" \
  "ds_read_b64_tr_b16 v[114:115], %[ad0] offset:41472\n\t" \
  "v_mfma_f32_16x16x32_bf16 v[80:83], v[128:131], v[244:247], v[80:83]\n\t" \
  "v_mfma_f32_16x16x32_bf16 v[52:55], v[132:135], v[188:191], v[52:55]\n\t" \
  "ds_read_b64_tr_b16 v[116:117], %[ad1] offset:40960\n\t" \
  "ds_read_b64_tr_b16 v[118:119], %[ad1] offset:41472\n\t" \
  "v_mfma_f32_16x16x32_bf16 v[84:87], v[132:135], v[244:247], v[84:87]\n\t" \
  "v_mfma_f32_16x16x32_bf16 v[56:59], v[136:139], v[188:191], v[56:59]\n\t" \
  "ds_read_b64_tr_b16 v[120:121], %[ad2] offset:40960\n\t" \
  "ds_read_b64_tr_b16 v[122:123], %[ad2] offset:41472\n\t" \
  "v_mfma_f32_16x16x32_bf16 v[88:91], v[136:139], v[244:247], v[88:91]\n\t" \
  "v_mfma_f32_16x16x32_bf16 v[60:63], v[140:143], v[188:191], v[60:63]\n\t" \
  "ds_read_b64_tr_b16 v[124:125], %[ad3] offset:40960\n\t" \
  "ds_read_b64_tr_b16 v[126:127], %[ad3] offset:41472\n\t" \
  "v_mfma_f32_16x16x32_bf16 v[92:95], v[140:143], v[244:247], v[92:95]\n\t" \
  "s_waitcnt lgkmcnt(6)\n\t" \
  "v_mfma_f32_16x16x32_bf16 v[32:35], v[96:99], v[192:195], v[32:35]\n\t" \
  "ds_read_b64_tr_b16 v[128:129], %[ad0] offset:36864\n\t" \
  "ds_read_b64_tr_b16 v[130:131], %[ad0] offset:37376\n\t" \
  "v_mfma_f32_16x16x32_bf16 v[64:67], v[96:99], v[248:251], v[64:67]\n\t" \
  "v_mfma_f32_16x16x32_bf16 v[36:39], v[100:103], v[192:195], v[36:39]\n\t" \
  "ds_read_b64_tr_b16 v[132:133], %[ad1] offset:36864\n\t" \
  "ds_read_b64_tr_b16 v[134:135], %[ad1] offset:37376\n\t" \
  "v_mfma_f32_16x16x32_bf16 v[68:71], v[100:103], v[248:251], v[68:71]\n\t" \
  "v_mfma_f32_16x16x32_bf16 v[40:43], v[104:107], v[192:195], v[40:43]\n\t" \
  "ds_read_b64_tr_b16 v[136:137], %[ad2] offset:36864\n\t" \
  "ds_read_b64_tr_b16 v[138:139], %[ad2] offset:37376\n\t" \
  "v_mfma_f32_16x16x32_bf16 v[72:75], v[104:107], v[248:251], v[72:75]\n\t" \
  "v_mfma_f32_16x16x32_bf16 v[44:47], v[108:111], v[192:195], v[44:47]\n\t" \
  "ds_read_b64_tr_b16 v[140:141], %[ad3] offset:36864\n\t" \
  "ds_read_b64_tr_b16 v[142:143], %[ad3] offset:37376\n\t" \
  "v_mfma_f32_16x16x32_bf16 v[76:79], v[108:111], v[248:251], v[76:79]\n\t" \
  "s_waitcnt lgkmcnt(6)\n\t" \
  "v_mfma_f32_16x16x32_bf16 v[48:51], v[112:115], v[192:195], v[48:51]\n\t" \
  "ds_read_b64_tr_b16 v[96:97], %[ad0] offset:45056\n\t" \
  "ds_read_b64_tr_b16 v[98:99], %[ad0] offset:45568\n\t" \
  "v_mfma_f32_16x16x32_bf16 v[80:83], v[112:115], v[248:251], v[80:83]\n\t" \
  "v_mfma_f32_16x16x32_bf16 v[52:55], v[116:119], v[192:195], v[52:55]\n\t" \
  "ds_read_b64_tr_b16 v[100:101], %[ad1] offset:45056\n\t" \
  "ds_read_b64_tr_b16 v[102:103], %[ad1] offset:45568\n\t" \
  "v_mfma_f32_16x16x32_bf16 v[84:87], v[116:119], v[248:251], v[84:87]\n\t" \
  "v_mfma_f32_16x16x32_bf16 v[56:59], v[120:123], v[192:195], v[56:59]\n\t" \
  "ds_read_b64_tr_b16 v[104:105], %[ad2] offset:45056\n\t" \
  "ds_read_b64_tr_b16 v[106:107], %[ad2] offset:45568\n\t" \
  "v_mfma_f32_16x16x32_bf16 v[88:91], v[120:123], v[248:251], v[88:91]\n\t" \
  "s_add_u32 s62, %[xslo], 122880\n\t" \
  "s_addc_u32 s63, %[xshi], 0\n\t" \
  "global_load_dwordx4 v[184:187], %[voff], s[62:63] offset:2048\n\t" \
  "s_add_u32 s62, %[xslo], 139264\n\t" \
  "s_addc_u32 s63, %[xshi], 0\n\t" \
  "global_load_dwordx4 v[240:243], %[voff], s[62:63] offset:0\n\t" \
  "v_mfma_f32_16x16x32_bf16 v[60:63], v[124:127], v[192:195], v[60:63]\n\t" \
  "ds_read_b64_tr_b16 v[108:109], %[ad3] offset:45056\n\t" \
  "ds_read_b64_tr_b16 v[110:111], %[ad3] offset:45568\n\t" \
  "v_mfma_f32_16x16x32_bf16 v[92:95], v[124:127], v[248:251], v[92:95]\n\t" \
  "s_add_u32 s62, %[xslo], 122880\n\t" \
  "s_addc_u32 s63, %[xshi], 0\n\t" \
  "global_load_dwordx4 v[188:191], %[voff], s[62:63] offset:3072\n\t" \
  "s_add_u32 s62, %[xslo], 139264\n\t" \
  "s_addc_u32 s63, %[xshi], 0\n\t" \
  "global_load_dwordx4 v[244:247], %[voff], s[62:63] offset:1024\n\t" \
  "s_waitcnt lgkmcnt(8)\n\t" \
  "v_mfma_f32_16x16x32_bf16 v[32:35], v[128:131], v[196:199], v[32:35]\n\t" \
  "v_mfma_f32_16x16x32_bf16 v[64:67], v[128:131], v[252:255], v[64:67]\n\t" \
  "v_mfma_f32_16x16x32_bf16 v[36:39], v[132:135], v[196:199], v[36:39]\n\t" \
  "v_mfma_f32_16x16x32_bf16 v[68:71], v[132:135], v[252:255], v[68:71]\n\t" \
  "v_mfma_f32_16x16x32_bf16 v[40:43], v[136:139], v[196:199], v[40:43]\n\t" \
  "v_mfma_f32_16x16x32_bf16 v[72:75], v[136:139], v[252:255], v[72:75]\n\t" \
  "v_mfma_f32_16x16x32_bf16 v[44:47], v[140:143], v[196:199], v[44:47]\n\t" \
  "v_mfma_f32_16x16x32_bf16 v[76:79], v[140:143], v[252:255], v[76:79]\n\t" \
  "s_waitcnt lgkmcnt(0)\n\t" \
  "v_mfma_f32_16x16x32_bf16 v[48:51], v[96:99], v[196:199], v[48:51]\n\t" \
  "v_mfma_f32_16x16x32_bf16 v[80:83], v[96:99], v[252:255], v[80:83]\n\t" \
  "v_mfma_f32_16x16x32_bf16 v[52:55], v[100:103], v[196:199], v[52:55]\n\t" \
  "v_mfma_f32_16x16x32_bf16 v[84:87], v[100:103], v[252:255], v[84:87]\n\t" \
  "v_mfma_f32_16x16x32_bf16 v[56:59], v[104:107], v[196:199], v[56:59]\n\t" \
  "v_mfma_f32_16x16x32_bf16 v[88:91], v[104:107], v[252:255], v[88:91]\n\t" \
  "v_mfma_f32_16x16x32_bf16 v[60:63], v[108:111], v[196:199], v[60:63]\n\t" \
  "v_mfma_f32_16x16x32_bf16 v[92:95], v[108:111], v[252:255], v[92:95]\n\t" \
  "v_subrev_u32 %[ad0], 0x10000, %[ad0]\n\t" \
  "v_subrev_u32 %[ad1], 0x10000, %[ad1]\n\t" \
  "v_subrev_u32 %[ad2], 0x10000, %[ad2]\n\t" \
  "v_subrev_u32 %[ad3], 0x10000, %[ad3]\n\t" \
  "s_nop 7\n\t" \
  "s_nop 7\n\t" \
  "s_add_u32 s62, %[xslo], 126976\n\t" \
  "s_addc_u32 s63, %[xshi], 0\n\t" \
  "global_load_dwordx4 v[192:195], %[voff], s[62:63] offset:0\n\t" \
  "s_add_u32 s62, %[xslo], 139264\n\t" \
  "s_addc_u32 s63, %[xshi], 0\n\t" \
  "global_load_dwordx4 v[248:251], %[voff], s[62:63] offset:2048\n\t" \
  "s_add_u32 s62, %[xslo], 126976\n\t" \
  "s_addc_u32 s63, %[xshi], 0\n\t" \
  "global_load_dwordx4 v[196:199], %[voff], s[62:63] offset:1024\n\t" \
  "s_add_u32 s62, %[xslo], 139264\n\t" \
  "s_addc_u32 s63, %[xshi], 0\n\t" \
  "global_load_dwordx4 v[252:255], %[voff], s[62:63] offset:3072\n\t"

#define K2_ROUND1_ASM \
  "ds_read_b64_tr_b16 v[96:97], %[ad0] offset:0\n\t" \
  "ds_read_b64_tr_b16 v[98:99], %[ad0] offset:512\n\t" \
  "ds_read_b64_tr_b16 v[100:101], %[ad1] offset:0\n\t" \
  "ds_read_b64_tr_b16 v[102:103], %[ad1] offset:512\n\t" \
  "ds_read_b64_tr_b16 v[104:105], %[ad2] offset:0\n\t" \
  "ds_read_b64_tr_b16 v[106:107], %[ad2] offset:512\n\t" \
  "ds_read_b64_tr_b16 v[108:109], %[ad3] offset:0\n\t" \
  "ds_read_b64_tr_b16 v[110:111], %[ad3] offset:512\n\t" \
  "s_waitcnt lgkmcnt(4)\n\t" \
  "ds_read_b64_tr_b16 v[112:113], %[ad0] offset:8192\n\t" \
  "ds_read_b64_tr_b16 v[114:115], %[ad0] offset:8704\n\t" \
  "ds_read_b64_tr_b16 v[116:117], %[ad1] offset:8192\n\t" \
  "ds_read_b64_tr_b16 v[118:119], %[ad1] offset:8704\n\t" \
  "ds_read_b64_tr_b16 v[120:121], %[ad2] offset:8192\n\t" \
  "ds_read_b64_tr_b16 v[122:123], %[ad2] offset:8704\n\t" \
  "ds_read_b64_tr_b16 v[124:125], %[ad3] offset:8192\n\t" \
  "ds_read_b64_tr_b16 v[126:127], %[ad3] offset:8704\n\t" \
  "s_waitcnt lgkmcnt(6)\n\t" \
  "v_mfma_f32_16x16x32_bf16 v[32:35], v[96:99], v[144:147], 0\n\t" \
  "ds_read_b64_tr_b16 v[128:129], %[ad0] offset:4096\n\t" \
  "ds_read_b64_tr_b16 v[130:131], %[ad0] offset:4608\n\t" \
  "v_mfma_f32_16x16x32_bf16 v[64:67], v[96:99], v[200:203], 0\n\t" \
  "v_mfma_f32_16x16x32_bf16 v[36:39], v[100:103], v[144:147], 0\n\t" \
  "ds_read_b64_tr_b16 v[132:133], %[ad1] offset:4096\n\t" \
  "ds_read_b64_tr_b16 v[134:135], %[ad1] offset:4608\n\t" \
  "v_mfma_f32_16x16x32_bf16 v[68:71], v[100:103], v[200:203], 0\n\t" \
  "v_mfma_f32_16x16x32_bf16 v[40:43], v[104:107], v[144:147], 0\n\t" \
  "ds_read_b64_tr_b16 v[136:137], %[ad2] offset:4096\n\t" \
  "ds_read_b64_tr_b16 v[138:139], %[ad2] offset:4608\n\t" \
  "v_mfma_f32_16x16x32_bf16 v[72:75], v[104:107], v[200:203], 0\n\t" \
  "v_mfma_f32_16x16x32_bf16 v[44:47], v[108:111], v[144:147], 0\n\t" \
  "ds_read_b64_tr_b16 v[140:141], %[ad3] offset:4096\n\t" \
  "ds_read_b64_tr_b16 v[142:143], %[ad3] offset:4608\n\t" \
  "v_mfma_f32_16x16x32_bf16 v[76:79], v[108:111], v[200:203], 0\n\t" \
  "s_waitcnt lgkmcnt(6)\n\t" \
  "v_mfma_f32_16x16x32_bf16 v[48:51], v[112:115], v[144:147], 0\n\t" \
  "ds_read_b64_tr_b16 v[96:97], %[ad0] offset:12288\n\t" \
  "ds_read_b64_tr_b16 v[98:99], %[ad0] offset:12800\n\t" \
  "v_mfma_f32_16x16x32_bf16 v[80:83], v[112:115], v[200:203], 0\n\t" \
  "v_mfma_f32_16x16x32_bf16 v[52:55], v[116:119], v[144:147], 0\n\t" \
  "ds_read_b64_tr_b16 v[100:101], %[ad1] offset:12288\n\t" \
  "ds_read_b64_tr_b16 v[102:103], %[ad1] offset:12800\n\t" \
  "v_mfma_f32_16x16x32_bf16 v[84:87], v[116:119], v[200:203], 0\n\t" \
  "v_mfma_f32_16x16x32_bf16 v[56:59], v[120:123], v[144:147], 0\n\t" \
  "ds_read_b64_tr_b16 v[104:105], %[ad2] offset:12288\n\t" \
  "ds_read_b64_tr_b16 v[106:107], %[ad2] offset:12800\n\t" \
  "v_mfma_f32_16x16x32_bf16 v[88:91], v[120:123], v[200:203], 0\n\t" \
  "v_mfma_f32_16x16x32_bf16 v[60:63], v[124:127], v[144:147], 0\n\t" \
  "ds_read_b64_tr_b16 v[108:109], %[ad3] offset:12288\n\t" \
  "ds_read_b64_tr_b16 v[110:111], %[ad3] offset:12800\n\t" \
  "v_mfma_f32_16x16x32_bf16 v[92:95], v[124:127], v[200:203], 0\n\t" \
  "s_waitcnt lgkmcnt(6)\n\t" \
  "v_mfma_f32_16x16x32_bf16 v[32:35], v[128:131], v[148:151], v[32:35]\n\t" \
  "ds_read_b64_tr_b16 v[112:113], %[ad0] offset:16384\n\t" \
  "ds_read_b64_tr_b16 v[114:115], %[ad0] offset:16896\n\t" \
  "v_mfma_f32_16x16x32_bf16 v[64:67], v[128:131], v[204:207], v[64:67]\n\t" \
  "v_mfma_f32_16x16x32_bf16 v[36:39], v[132:135], v[148:151], v[36:39]\n\t" \
  "ds_read_b64_tr_b16 v[116:117], %[ad1] offset:16384\n\t" \
  "ds_read_b64_tr_b16 v[118:119], %[ad1] offset:16896\n\t" \
  "v_mfma_f32_16x16x32_bf16 v[68:71], v[132:135], v[204:207], v[68:71]\n\t" \
  "v_mfma_f32_16x16x32_bf16 v[40:43], v[136:139], v[148:151], v[40:43]\n\t" \
  "ds_read_b64_tr_b16 v[120:121], %[ad2] offset:16384\n\t" \
  "ds_read_b64_tr_b16 v[122:123], %[ad2] offset:16896\n\t" \
  "v_mfma_f32_16x16x32_bf16 v[72:75], v[136:139], v[204:207], v[72:75]\n\t" \
  "v_mfma_f32_16x16x32_bf16 v[44:47], v[140:143], v[148:151], v[44:47]\n\t" \
  "ds_read_b64_tr_b16 v[124:125], %[ad3] offset:16384\n\t" \
  "ds_read_b64_tr_b16 v[126:127], %[ad3] offset:16896\n\t" \
  "v_mfma_f32_16x16x32_bf16 v[76:79], v[140:143], v[204:207], v[76:79]\n\t" \
  "s_waitcnt lgkmcnt(6)\n\t" \
  "v_mfma_f32_16x16x32_bf16 v[48:51], v[96:99], v[148:151], v[48:51]\n\t" \
  "ds_read_b64_tr_b16 v[128:129], %[ad0] offset:24576\n\t" \
  "ds_read_b64_tr_b16 v[130:131], %[ad0] offset:25088\n\t" \
  "v_mfma_f32_16x16x32_bf16 v[80:83], v[96:99], v[204:207], v[80:83]\n\t" \
  "v_mfma_f32_16x16x32_bf16 v[52:55], v[100:103], v[148:151], v[52:55]\n\t" \
  "ds_read_b64_tr_b16 v[132:133], %[ad1] offset:24576\n\t" \
  "ds_read_b64_tr_b16 v[134:135], %[ad1] offset:25088\n\t" \
  "v_mfma_f32_16x16x32_bf16 v[84:87], v[100:103], v[204:207], v[84:87]\n\t" \
  "v_mfma_f32_16x16x32_bf16 v[56:59], v[104:107], v[148:151], v[56:59]\n\t" \
  "ds_read_b64_tr_b16 v[136:137], %[ad2] offset:24576\n\t" \
  "ds_read_b64_tr_b16 v[138:139], %[ad2] offset:25088\n\t" \
  "v_mfma_f32_16x16x32_bf16 v[88:91], v[104:107], v[204:207], v[88:91]\n\t" \
  "v_mfma_f32_16x16x32_bf16 v[60:63], v[108:111], v[148:151], v[60:63]\n\t" \
  "ds_read_b64_tr_b16 v[140:141], %[ad3] offset:24576\n\t" \
  "ds_read_b64_tr_b16 v[142:143], %[ad3] offset:25088\n\t" \
  "v_mfma_f32_16x16x32_bf16 v[92:95], v[108:111], v[204:207], v[92:95]\n\t" \
  "s_waitcnt lgkmcnt(6)\n\t" \
  "v_mfma_f32_16x16x32_bf16 v[32:35], v[112:115], v[152:155], v[32:35]\n\t" \
  "ds_read_b64_tr_b16 v[96:97], %[ad0] offset:20480\n\t" \
  "ds_read_b64_tr_b16 v[98:99], %[ad0] offset:20992\n\t" \
  "v_mfma_f32_16x16x32_bf16 v[64:67], v[112:115], v[208:211], v[64:67]\n\t" \
  "v_mfma_f32_16x16x32_bf16 v[36:39], v[116:119], v[152:155], v[36:39]\n\t" \
  "ds_read_b64_tr_b16 v[100:101], %[ad1] offset:20480\n\t" \
  "ds_read_b64_tr_b16 v[102:103], %[ad1] offset:20992\n\t" \
  "v_mfma_f32_16x16x32_bf16 v[68:71], v[116:119], v[208:211], v[68:71]\n\t" \
  "v_mfma_f32_16x16x32_bf16 v[40:43], v[120:123], v[152:155], v[40:43]\n\t" \
  "ds_read_b64_tr_b16 v[104:105], %[ad2] offset:20480\n\t" \
  "ds_read_b64_tr_b16 v[106:107], %[ad2] offset:20992\n\t" \
  "v_mfma_f32_16x16x32_bf16 v[72:75], v[120:123], v[208:211], v[72:75]\n\t" \
  "v_mfma_f32_16x16x32_bf16 v[44:47], v[124:127], v[152:155], v[44:47]\n\t" \
  "ds_read_b64_tr_b16 v[108:109], %[ad3] offset:20480\n\t" \
  "ds_read_b64_tr_b16 v[110:111], %[ad3] offset:20992\n\t" \
  "v_mfma_f32_16x16x32_bf16 v[76:79], v[124:127], v[208:211], v[76:79]\n\t" \
  "s_waitcnt lgkmcnt(6)\n\t" \
  "v_mfma_f32_16x16x32_bf16 v[48:51], v[128:131], v[152:155], v[48:51]\n\t" \
  "ds_read_b64_tr_b16 v[112:113], %[ad0] offset:28672\n\t" \
  "ds_read_b64_tr_b16 v[114:115], %[ad0] offset:29184\n\t" \
  "v_mfma_f32_16x16x32_bf16 v[80:83], v[128:131], v[208:211], v[80:83]\n\t" \
  "v_mfma_f32_16x16x32_bf16 v[52:55], v[132:135], v[152:155], v[52:55]\n\t" \
  "ds_read_b64_tr_b16 v[116:117], %[ad1] offset:28672\n\t" \
  "ds_read_b64_tr_b16 v[118:119], %[ad1] offset:29184\n\t" \
  "v_mfma_f32_16x16x32_bf16 v[84:87], v[132:135], v[208:211], v[84:87]\n\t" \
  "v_mfma_f32_16x16x32_bf16 v[56:59], v[136:139], v[152:155], v[56:59]\n\t" \
  "ds_read_b64_tr_b16 v[120:121], %[ad2] offset:28672\n\t" \
  "ds_read_b64_tr_b16 v[122:123], %[ad2] offset:29184\n\t" \
  "v_mfma_f32_16x16x32_bf16 v[88:91], v[136:139], v[208:211], v[88:91]\n\t" \
  "v_mfma_f32_16x16x32_bf16 v[60:63], v[140:143], v[152:155], v[60:63]\n\t" \
  "ds_read_b64_tr_b16 v[124:125], %[ad3] offset:28672\n\t" \
  "ds_read_b64_tr_b16 v[126:127], %[ad3] offset:29184\n\t" \
  "v_mfma_f32_16x16x32_bf16 v[92:95], v[140:143], v[208:211], v[92:95]\n\t" \
  "s_waitcnt lgkmcnt(6)\n\t" \
  "v_mfma_f32_16x16x32_bf16 v[32:35], v[96:99], v[156:159], v[32:35]\n\t" \
  "ds_read_b64_tr_b16 v[128:129], %[ad0] offset:32768\n\t" \
  "ds_read_b64_tr_b16 v[130:131], %[ad0] offset:33280\n\t" \
  "v_mfma_f32_16x16x32_bf16 v[64:67], v[96:99], v[212:215], v[64:67]\n\t" \
  "v_mfma_f32_16x16x32_bf16 v[36:39], v[100:103], v[156:159], v[36:39]\n\t" \
  "ds_read_b64_tr_b16 v[132:133], %[ad1] offset:32768\n\t" \
  "ds_read_b64_tr_b16 v[134:135], %[ad1] offset:33280\n\t" \
  "v_mfma_f32_16x16x32_bf16 v[68:71], v[100:103], v[212:215], v[68:71]\n\t" \
  "v_mfma_f32_16x16x32_bf16 v[40:43], v[104:107], v[156:159], v[40:43]\n\t" \
  "ds_read_b64_tr_b16 v[136:137], %[ad2] offset:32768\n\t" \
  "ds_read_b64_tr_b16 v[138:139], %[ad2] offset:33280\n\t" \
  "v_mfma_f32_16x16x32_bf16 v[72:75], v[104:107], v[212:215], v[72:75]\n\t" \
  "v_mfma_f32_16x16x32_bf16 v[44:47], v[108:111], v[156:159], v[44:47]\n\t" \
  "ds_read_b64_tr_b16 v[140:141], %[ad3] offset:32768\n\t" \
  "ds_read_b64_tr_b16 v[142:143], %[ad3] offset:33280\n\t" \
  "v_mfma_f32_16x16x32_bf16 v[76:79], v[108:111], v[212:215], v[76:79]\n\t" \
  "s_waitcnt lgkmcnt(6)\n\t" \
  "v_mfma_f32_16x16x32_bf16 v[48:51], v[112:115], v[156:159], v[48:51]\n\t" \
  "ds_read_b64_tr_b16 v[96:97], %[ad0] offset:40960\n\t" \
  "ds_read_b64_tr_b16 v[98:99], %[ad0] offset:41472\n\t" \
  "v_mfma_f32_16x16x32_bf16 v[80:83], v[112:115], v[212:215], v[80:83]\n\t" \
  "v_mfma_f32_16x16x32_bf16 v[52:55], v[116:119], v[156:159], v[52:55]\n\t" \
  "ds_read_b64_tr_b16 v[100:101], %[ad1] offset:40960\n\t" \
  "ds_read_b64_tr_b16 v[102:103], %[ad1] offset:41472\n\t" \
  "v_mfma_f32_16x16x32_bf16 v[84:87], v[116:119], v[212:215], v[84:87]\n\t" \
  "v_mfma_f32_16x16x32_bf16 v[56:59], v[120:123], v[156:159], v[56:59]\n\t" \
  "ds_read_b64_tr_b16 v[104:105], %[ad2] offset:40960\n\t" \
  "ds_read_b64_tr_b16 v[106:107], %[ad2] offset:41472\n\t" \
  "v_mfma_f32_16x16x32_bf16 v[88:91], v[120:123], v[212:215], v[88:91]\n\t" \
  "v_mfma_f32_16x16x32_bf16 v[60:63], v[124:127], v[156:159], v[60:63]\n\t" \
  "ds_read_b64_tr_b16 v[108:109], %[ad3] offset:40960\n\t" \
  "ds_read_b64_tr_b16 v[110:111], %[ad3] offset:41472\n\t" \
  "v_mfma_f32_16x16x32_bf16 v[92:95], v[124:127], v[212:215], v[92:95]\n\t" \
  "s_waitcnt lgkmcnt(6)\n\t" \
  "v_mfma_f32_16x16x32_bf16 v[32:35], v[128:131], v[160:163], v[32:35]\n\t" \
  "ds_read_b64_tr_b16 v[112:113], %[ad0] offset:36864\n\t" \
  "ds_read_b64_tr_b16 v[114:115], %[ad0] offset:37376\n\t" \
  "v_mfma_f32_16x16x32_bf16 v[64:67], v[128:131], v[216:219], v[64:67]\n\t" \
  "v_mfma_f32_16x16x32_bf16 v[36:39], v[132:135], v[160:163], v[36:39]\n\t" \
  "ds_read_b64_tr_b16 v[116:117], %[ad1] offset:36864\n\t" \
  "ds_read_b64_tr_b16 v[118:119], %[ad1] offset:37376\n\t" \
  "v_mfma_f32_16x16x32_bf16 v[68:71], v[132:135], v[216:219], v[68:71]\n\t" \
  "v_mfma_f32_16x16x32_bf16 v[40:43], v[136:139], v[160:163], v[40:43]\n\t" \
  "ds_read_b64_tr_b16 v[120:121], %[ad2] offset:36864\n\t" \
  "ds_read_b64_tr_b16 v[122:123], %[ad2] offset:37376\n\t" \
  "v_mfma_f32_16x16x32_bf16 v[72:75], v[136:139], v[216:219], v[72:75]\n\t" \
  "v_mfma_f32_16x16x32_bf16 v[44:47], v[140:143], v[160:163], v[44:47]\n\t" \
  "ds_read_b64_tr_b16 v[124:125], %[ad3] offset:36864\n\t" \
  "ds_read_b64_tr_b16 v[126:127], %[ad3] offset:37376\n\t" \
  "v_mfma_f32_16x16x32_bf16 v[76:79], v[140:143], v[216:219], v[76:79]\n\t" \
  "s_waitcnt lgkmcnt(6)\n\t" \
  "v_mfma_f32_16x16x32_bf16 v[48:51], v[96:99], v[160:163], v[48:51]\n\t" \
  "ds_read_b64_tr_b16 v[128:129], %[ad0] offset:45056\n\t" \
  "ds_read_b64_tr_b16 v[130:131], %[ad0] offset:45568\n\t" \
  "v_mfma_f32_16x16x32_bf16 v[80:83], v[96:99], v[216:219], v[80:83]\n\t" \
  "v_mfma_f32_16x16x32_bf16 v[52:55], v[100:103], v[160:163], v[52:55]\n\t" \
  "ds_read_b64_tr_b16 v[132:133], %[ad1] offset:45056\n\t" \
  "ds_read_b64_tr_b16 v[134:135], %[ad1] offset:45568\n\t" \
  "v_mfma_f32_16x16x32_bf16 v[84:87], v[100:103], v[216:219], v[84:87]\n\t" \
  "v_mfma_f32_16x16x32_bf16 v[56:59], v[104:107], v[160:163], v[56:59]\n\t" \
  "ds_read_b64_tr_b16 v[136:137], %[ad2] offset:45056\n\t" \
  "ds_read_b64_tr_b16 v[138:139], %[ad2] offset:45568\n\t" \
  "v_mfma_f32_16x16x32_bf16 v[88:91], v[104:107], v[216:219], v[88:91]\n\t" \
  "v_mfma_f32_16x16x32_bf16 v[60:63], v[108:111], v[160:163], v[60:63]\n\t" \
  "ds_read_b64_tr_b16 v[140:141], %[ad3] offset:45056\n\t" \
  "ds_read_b64_tr_b16 v[142:143], %[ad3] offset:45568\n\t" \
  "v_mfma_f32_16x16x32_bf16 v[92:95], v[108:111], v[216:219], v[92:95]\n\t" \
  "s_waitcnt lgkmcnt(6)\n\t" \
  "v_mfma_f32_16x16x32_bf16 v[32:35], v[112:115], v[164:167], v[32:35]\n\t" \
  "ds_read_b64_tr_b16 v[96:97], %[ad0] offset:49152\n\t" \
  "ds_read_b64_tr_b16 v[98:99], %[ad0] offset:49664\n\t" \
  "v_mfma_f32_16x16x32_bf16 v[64:67], v[112:115], v[220:223], v[64:67]\n\t" \
  "v_mfma_f32_16x16x32_bf16 v[36:39], v[116:119], v[164:167], v[36:39]\n\t" \
  "ds_read_b64_tr_b16 v[100:101], %[ad1] offset:49152\n\t" \
  "ds_read_b64_tr_b16 v[102:103], %[ad1] offset:49664\n\t" \
  "v_mfma_f32_16x16x32_bf16 v[68:71], v[116:119], v[220:223], v[68:71]\n\t" \
  "v_mfma_f32_16x16x32_bf16 v[40:43], v[120:123], v[164:167], v[40:43]\n\t" \
  "ds_read_b64_tr_b16 v[104:105], %[ad2] offset:49152\n\t" \
  "ds_read_b64_tr_b16 v[106:107], %[ad2] offset:49664\n\t" \
  "v_mfma_f32_16x16x32_bf16 v[72:75], v[120:123], v[220:223], v[72:75]\n\t" \
  "v_mfma_f32_16x16x32_bf16 v[44:47], v[124:127], v[164:167], v[44:47]\n\t" \
  "ds_read_b64_tr_b16 v[108:109], %[ad3] offset:49152\n\t" \
  "ds_read_b64_tr_b16 v[110:111], %[ad3] offset:49664\n\t" \
  "v_mfma_f32_16x16x32_bf16 v[76:79], v[124:127], v[220:223], v[76:79]\n\t" \
  "s_waitcnt lgkmcnt(6)\n\t" \
  "v_mfma_f32_16x16x32_bf16 v[48:51], v[128:131], v[164:167], v[48:51]\n\t" \
  "ds_read_b64_tr_b16 v[112:113], %[ad0] offset:57344\n\t" \
  "ds_read_b64_tr_b16 v[114:115], %[ad0] offset:57856\n\t" \
  "v_mfma_f32_16x16x32_bf16 v[80:83], v[128:131], v[220:223], v[80:83]\n\t" \
  "v_mfma_f32_16x16x32_bf16 v[52:55], v[132:135], v[164:167], v[52:55]\n\t" \
  "ds_read_b64_tr_b16 v[116:117], %[ad1] offset:57344\n\t" \
  "ds_read_b64_tr_b16 v[118:119], %[ad1] offset:57856\n\t" \
  "v_mfma_f32_16x16x32_bf16 v[84:87], v[132:135], v[220:223], v[84:87]\n\t" \
  "v_mfma_f32_16x16x32_bf16 v[56:59], v[136:139], v[164:167], v[56:59]\n\t" \
  "ds_read_b64_tr_b16 v[120:121], %[ad2] offset:57344\n\t" \
  "ds_read_b64_tr_b16 v[122:123], %[ad2] offset:57856\n\t" \
  "v_mfma_f32_16x16x32_bf16 v[88:91], v[136:139], v[220:223], v[88:91]\n\t" \
  "v_mfma_f32_16x16x32_bf16 v[60:63], v[140:143], v[164:167], v[60:63]\n\t" \
  "ds_read_b64_tr_b16 v[124:125], %[ad3] offset:57344\n\t" \
  "ds_read_b64_tr_b16 v[126:127], %[ad3] offset:57856\n\t" \
  "v_mfma_f32_16x16x32_bf16 v[92:95], v[140:143], v[220:223], v[92:95]\n\t" \
  "s_waitcnt lgkmcnt(6)\n\t" \
  "v_mfma_f32_16x16x32_bf16 v[32:35], v[96:99], v[168:171], v[32:35]\n\t" \
  "ds_read_b64_tr_b16 v[128:129], %[ad0] offset:53248\n\t" \
  "ds_read_b64_tr_b16 v[130:131], %[ad0] offset:53760\n\t" \
  "v_mfma_f32_16x16x32_bf16 v[64:67], v[96:99], v[224:227], v[64:67]\n\t" \
  "v_mfma_f32_16x16x32_bf16 v[36:39], v[100:103], v[168:171], v[36:39]\n\t" \
  "ds_read_b64_tr_b16 v[132:133], %[ad1] offset:53248\n\t" \
  "ds_read_b64_tr_b16 v[134:135], %[ad1] offset:53760\n\t" \
  "v_mfma_f32_16x16x32_bf16 v[68:71], v[100:103], v[224:227], v[68:71]\n\t" \
  "v_mfma_f32_16x16x32_bf16 v[40:43], v[104:107], v[168:171], v[40:43]\n\t" \
  "ds_read_b64_tr_b16 v[136:137], %[ad2] offset:53248\n\t" \
  "ds_read_b64_tr_b16 v[138:139], %[ad2] offset:53760\n\t" \
  "v_mfma_f32_16x16x32_bf16 v[72:75], v[104:107], v[224:227], v[72:75]\n\t" \
  "v_mfma_f32_16x16x32_bf16 v[44:47], v[108:111], v[168:171], v[44:47]\n\t" \
  "ds_read_b64_tr_b16 v[140:141], %[ad3] offset:53248\n\t" \
  "ds_read_b64_tr_b16 v[142:143], %[ad3] offset:53760\n\t" \
  "v_mfma_f32_16x16x32_bf16 v[76:79], v[108:111], v[224:227], v[76:79]\n\t" \
  "s_waitcnt lgkmcnt(6)\n\t" \
  "v_mfma_f32_16x16x32_bf16 v[48:51], v[112:115], v[168:171], v[48:51]\n\t" \
  "ds_read_b64_tr_b16 v[96:97], %[ad0] offset:61440\n\t" \
  "ds_read_b64_tr_b16 v[98:99], %[ad0] offset:61952\n\t" \
  "v_mfma_f32_16x16x32_bf16 v[80:83], v[112:115], v[224:227], v[80:83]\n\t" \
  "v_mfma_f32_16x16x32_bf16 v[52:55], v[116:119], v[168:171], v[52:55]\n\t" \
  "ds_read_b64_tr_b16 v[100:101], %[ad1] offset:61440\n\t" \
  "ds_read_b64_tr_b16 v[102:103], %[ad1] offset:61952\n\t" \
  "v_mfma_f32_16x16x32_bf16 v[84:87], v[116:119], v[224:227], v[84:87]\n\t" \
  "v_mfma_f32_16x16x32_bf16 v[56:59], v[120:123], v[168:171], v[56:59]\n\t" \
  "ds_read_b64_tr_b16 v[104:105], %[ad2] offset:61440\n\t" \
  "ds_read_b64_tr_b16 v[106:107], %[ad2] offset:61952\n\t" \
  "v_mfma_f32_16x16x32_bf16 v[88:91], v[120:123], v[224:227], v[88:91]\n\t" \
  "v_mfma_f32_16x16x32_bf16 v[60:63], v[124:127], v[168:171], v[60:63]\n\t" \
  "ds_read_b64_tr_b16 v[108:109], %[ad3] offset:61440\n\t" \
  "ds_read_b64_tr_b16 v[110:111], %[ad3] offset:61952\n\t" \
  "v_mfma_f32_16x16x32_bf16 v[92:95], v[124:127], v[224:227], v[92:95]\n\t" \
  "s_waitcnt lgkmcnt(6)\n\t" \
  "v_add_u32 %[ad0], 0x10000, %[ad0]\n\t" \
  "v_add_u32 %[ad1], 0x10000, %[ad1]\n\t" \
  "v_add_u32 %[ad2], 0x10000, %[ad2]\n\t" \
  "v_add_u32 %[ad3], 0x10000, %[ad3]\n\t" \
  "v_mfma_f32_16x16x32_bf16 v[32:35], v[128:131], v[172:175], v[32:35]\n\t" \
  "ds_read_b64_tr_b16 v[112:113], %[ad0] offset:0\n\t" \
  "ds_read_b64_tr_b16 v[114:115], %[ad0] offset:512\n\t" \
  "v_mfma_f32_16x16x32_bf16 v[64:67], v[128:131], v[228:231], v[64:67]\n\t" \
  "v_mfma_f32_16x16x32_bf16 v[36:39], v[132:135], v[172:175], v[36:39]\n\t" \
  "ds_read_b64_tr_b16 v[116:117], %[ad1] offset:0\n\t" \
  "ds_read_b64_tr_b16 v[118:119], %[ad1] offset:512\n\t" \
  "v_mfma_f32_16x16x32_bf16 v[68:71], v[132:135], v[228:231], v[68:71]\n\t" \
  "v_mfma_f32_16x16x32_bf16 v[40:43], v[136:139], v[172:175], v[40:43]\n\t" \
  "ds_read_b64_tr_b16 v[120:121], %[ad2] offset:0\n\t" \
  "ds_read_b64_tr_b16 v[122:123], %[ad2] offset:512\n\t" \
  "v_mfma_f32_16x16x32_bf16 v[72:75], v[136:139], v[228:231], v[72:75]\n\t" \
  "v_mfma_f32_16x16x32_bf16 v[44:47], v[140:143], v[172:175], v[44:47]\n\t" \
  "ds_read_b64_tr_b16 v[124:125], %[ad3] offset:0\n\t" \
  "ds_read_b64_tr_b16 v[126:127], %[ad3] offset:512\n\t" \
  "v_mfma_f32_16x16x32_bf16 v[76:79], v[140:143], v[228:231], v[76:79]\n\t" \
  "s_waitcnt lgkmcnt(6)\n\t" \
  "v_mfma_f32_16x16x32_bf16 v[48:51], v[96:99], v[172:175], v[48:51]\n\t" \
  "ds_read_b64_tr_b16 v[128:129], %[ad0] offset:8192\n\t" \
  "ds_read_b64_tr_b16 v[130:131], %[ad0] offset:8704\n\t" \
  "v_mfma_f32_16x16x32_bf16 v[80:83], v[96:99], v[228:231], v[80:83]\n\t" \
  "v_mfma_f32_16x16x32_bf16 v[52:55], v[100:103], v[172:175], v[52:55]\n\t" \
  "ds_read_b64_tr_b16 v[132:133], %[ad1] offset:8192\n\t" \
  "ds_read_b64_tr_b16 v[134:135], %[ad1] offset:8704\n\t" \
  "v_mfma_f32_16x16x32_bf16 v[84:87], v[100:103], v[228:231], v[84:87]\n\t" \
  "v_mfma_f32_16x16x32_bf16 v[56:59], v[104:107], v[172:175], v[56:59]\n\t" \
  "ds_read_b64_tr_b16 v[136:137], %[ad2] offset:8192\n\t" \
  "ds_read_b64_tr_b16 v[138:139], %[ad2] offset:8704\n\t" \
  "v_mfma_f32_16x16x32_bf16 v[88:91], v[104:107], v[228:231], v[88:91]\n\t" \
  "v_mfma_f32_16x16x32_bf16 v[60:63], v[108:111], v[172:175], v[60:63]\n\t" \
  "ds_read_b64_tr_b16 v[140:141], %[ad3] offset:8192\n\t" \
  "ds_read_b64_tr_b16 v[142:143], %[ad3] offset:8704\n\t" \
  "v_mfma_f32_16x16x32_bf16 v[92:95], v[108:111], v[228:231], v[92:95]\n\t" \
  "s_waitcnt lgkmcnt(6)\n\t" \
  "v_mfma_f32_16x16x32_bf16 v[32:35], v[112:115], v[176:179], v[32:35]\n\t" \
  "ds_read_b64_tr_b16 v[96:97], %[ad0] offset:4096\n\t" \
  "ds_read_b64_tr_b16 v[98:99], %[ad0] offset:4608\n\t" \
  "v_mfma_f32_16x16x32_bf16 v[64:67], v[112:115], v[232:235], v[64:67]\n\t" \
  "v_mfma_f32_16x16x32_bf16 v[36:39], v[116:119], v[176:179], v[36:39]\n\t" \
  "ds_read_b64_tr_b16 v[100:101], %[ad1] offset:4096\n\t" \
  "ds_read_b64_tr_b16 v[102:103], %[ad1] offset:4608\n\t" \
  "v_mfma_f32_16x16x32_bf16 v[68:71], v[116:119], v[232:235], v[68:71]\n\t" \
  "v_mfma_f32_16x16x32_bf16 v[40:43], v[120:123], v[176:179], v[40:43]\n\t" \
  "ds_read_b64_tr_b16 v[104:105], %[ad2] offset:4096\n\t" \
  "ds_read_b64_tr_b16 v[106:107], %[ad2] offset:4608\n\t" \
  "v_mfma_f32_16x16x32_bf16 v[72:75], v[120:123], v[232:235], v[72:75]\n\t" \
  "v_mfma_f32_16x16x32_bf16 v[44:47], v[124:127], v[176:179], v[44:47]\n\t" \
  "ds_read_b64_tr_b16 v[108:109], %[ad3] offset:4096\n\t" \
  "ds_read_b64_tr_b16 v[110:111], %[ad3] offset:4608\n\t" \
  "v_mfma_f32_16x16x32_bf16 v[76:79], v[124:127], v[232:235], v[76:79]\n\t" \
  "s_waitcnt lgkmcnt(6)\n\t" \
  "v_mfma_f32_16x16x32_bf16 v[48:51], v[128:131], v[176:179], v[48:51]\n\t" \
  "ds_read_b64_tr_b16 v[112:113], %[ad0] offset:12288\n\t" \
  "ds_read_b64_tr_b16 v[114:115], %[ad0] offset:12800\n\t" \
  "v_mfma_f32_16x16x32_bf16 v[80:83], v[128:131], v[232:235], v[80:83]\n\t" \
  "v_mfma_f32_16x16x32_bf16 v[52:55], v[132:135], v[176:179], v[52:55]\n\t" \
  "ds_read_b64_tr_b16 v[116:117], %[ad1] offset:12288\n\t" \
  "ds_read_b64_tr_b16 v[118:119], %[ad1] offset:12800\n\t" \
  "v_mfma_f32_16x16x32_bf16 v[84:87], v[132:135], v[232:235], v[84:87]\n\t" \
  "v_mfma_f32_16x16x32_bf16 v[56:59], v[136:139], v[176:179], v[56:59]\n\t" \
  "ds_read_b64_tr_b16 v[120:121], %[ad2] offset:12288\n\t" \
  "ds_read_b64_tr_b16 v[122:123], %[ad2] offset:12800\n\t" \
  "v_mfma_f32_16x16x32_bf16 v[88:91], v[136:139], v[232:235], v[88:91]\n\t" \
  "v_mfma_f32_16x16x32_bf16 v[60:63], v[140:143], v[176:179], v[60:63]\n\t" \
  "ds_read_b64_tr_b16 v[124:125], %[ad3] offset:12288\n\t" \
  "ds_read_b64_tr_b16 v[126:127], %[ad3] offset:12800\n\t" \
  "v_mfma_f32_16x16x32_bf16 v[92:95], v[140:143], v[232:235], v[92:95]\n\t" \
  "s_waitcnt lgkmcnt(6)\n\t" \
  "v_mfma_f32_16x16x32_bf16 v[32:35], v[96:99], v[180:183], v[32:35]\n\t" \
  "ds_read_b64_tr_b16 v[128:129], %[ad0] offset:16384\n\t" \
  "ds_read_b64_tr_b16 v[130:131], %[ad0] offset:16896\n\t" \
  "v_mfma_f32_16x16x32_bf16 v[64:67], v[96:99], v[236:239], v[64:67]\n\t" \
  "v_mfma_f32_16x16x32_bf16 v[36:39], v[100:103], v[180:183], v[36:39]\n\t" \
  "ds_read_b64_tr_b16 v[132:133], %[ad1] offset:16384\n\t" \
  "ds_read_b64_tr_b16 v[134:135], %[ad1] offset:16896\n\t" \
  "v_mfma_f32_16x16x32_bf16 v[68:71], v[100:103], v[236:239], v[68:71]\n\t" \
  "v_mfma_f32_16x16x32_bf16 v[40:43], v[104:107], v[180:183], v[40:43]\n\t" \
  "ds_read_b64_tr_b16 v[136:137], %[ad2] offset:16384\n\t" \
  "ds_read_b64_tr_b16 v[138:139], %[ad2] offset:16896\n\t" \
  "v_mfma_f32_16x16x32_bf16 v[72:75], v[104:107], v[236:239], v[72:75]\n\t" \
  "v_mfma_f32_16x16x32_bf16 v[44:47], v[108:111], v[180:183], v[44:47]\n\t" \
  "ds_read_b64_tr_b16 v[140:141], %[ad3] offset:16384\n\t" \
  "ds_read_b64_tr_b16 v[142:143], %[ad3] offset:16896\n\t" \
  "v_mfma_f32_16x16x32_bf16 v[76:79], v[108:111], v[236:239], v[76:79]\n\t" \
  "s_waitcnt lgkmcnt(6)\n\t" \
  "v_mfma_f32_16x16x32_bf16 v[48:51], v[112:115], v[180:183], v[48:51]\n\t" \
  "ds_read_b64_tr_b16 v[96:97], %[ad0] offset:24576\n\t" \
  "ds_read_b64_tr_b16 v[98:99], %[ad0] offset:25088\n\t" \
  "v_mfma_f32_16x16x32_bf16 v[80:83], v[112:115], v[236:239], v[80:83]\n\t" \
  "v_mfma_f32_16x16x32_bf16 v[52:55], v[116:119], v[180:183], v[52:55]\n\t" \
  "ds_read_b64_tr_b16 v[100:101], %[ad1] offset:24576\n\t" \
  "ds_read_b64_tr_b16 v[102:103], %[ad1] offset:25088\n\t" \
  "v_mfma_f32_16x16x32_bf16 v[84:87], v[116:119], v[236:239], v[84:87]\n\t" \
  "v_mfma_f32_16x16x32_bf16 v[56:59], v[120:123], v[180:183], v[56:59]\n\t" \
  "ds_read_b64_tr_b16 v[104:105], %[ad2] offset:24576\n\t" \
  "ds_read_b64_tr_b16 v[106:107], %[ad2] offset:25088\n\t" \
  "v_mfma_f32_16x16x32_bf16 v[88:91], v[120:123], v[236:239], v[88:91]\n\t" \
  "v_mfma_f32_16x16x32_bf16 v[60:63], v[124:127], v[180:183], v[60:63]\n\t" \
  "ds_read_b64_tr_b16 v[108:109], %[ad3] offset:24576\n\t" \
  "ds_read_b64_tr_b16 v[110:111], %[ad3] offset:25088\n\t" \
  "v_mfma_f32_16x16x32_bf16 v[92:95], v[124:127], v[236:239], v[92:95]\n\t" \
  "s_waitcnt lgkmcnt(6)\n\t" \
  "v_mfma_f32_16x16x32_bf16 v[32:35], v[128:131], v[184:187], v[32:35]\n\t" \
  "ds_read_b64_tr_b16 v[112:113], %[ad0] offset:20480\n\t" \
  "ds_read_b64_tr_b16 v[114:115], %[ad0] offset:20992\n\t" \
  "v_mfma_f32_16x16x32_bf16 v[64:67], v[128:131], v[240:243], v[64:67]\n\t" \
  "v_mfma_f32_16x16x32_bf16 v[36:39], v[132:135], v[184:187], v[36:39]\n\t" \
  "ds_read_b64_tr_b16 v[116:117], %[ad1] offset:20480\n\t" \
  "ds_read_b64_tr_b16 v[118:119], %[ad1] offset:20992\n\t" \
  "v_mfma_f32_16x16x32_bf16 v[68:71], v[132:135], v[240:243], v[68:71]\n\t" \
  "v_mfma_f32_16x16x32_bf16 v[40:43], v[136:139], v[184:187], v[40:43]\n\t" \
  "ds_read_b64_tr_b16 v[120:121], %[ad2] offset:20480\n\t" \
  "ds_read_b64_tr_b16 v[122:123], %[ad2] offset:20992\n\t" \
  "v_mfma_f32_16x16x32_bf16 v[72:75], v[136:139], v[240:243], v[72:75]\n\t" \
  "v_mfma_f32_16x16x32_bf16 v[44:47], v[140:143], v[184:187], v[44:47]\n\t" \
  "ds_read_b64_tr_b16 v[124:125], %[ad3] offset:20480\n\t" \
  "ds_read_b64_tr_b16 v[126:127], %[ad3] offset:20992\n\t" \
  "v_mfma_f32_16x16x32_bf16 v[76:79], v[140:143], v[240:243], v[76:79]\n\t" \
  "s_waitcnt lgkmcnt(6)\n\t" \
  "v_mfma_f32_16x16x32_bf16 v[48:51], v[96:99], v[184:187], v[48:51]\n\t" \
  "ds_read_b64_tr_b16 v[128:129], %[ad0] offset:28672\n\t" \
  "ds_read_b64_tr_b16 v[130:131], %[ad0] offset:29184\n\t" \
  "v_mfma_f32_16x16x32_bf16 v[80:83], v[96:99], v[240:243], v[80:83]\n\t" \
  "v_mfma_f32_16x16x32_bf16 v[52:55], v[100:103], v[184:187], v[52:55]\n\t" \
  "ds_read_b64_tr_b16 v[132:133], %[ad1] offset:28672\n\t" \
  "ds_read_b64_tr_b16 v[134:135], %[ad1] offset:29184\n\t" \
  "v_mfma_f32_16x16x32_bf16 v[84:87], v[100:103], v[240:243], v[84:87]\n\t" \
  "v_mfma_f32_16x16x32_bf16 v[56:59], v[104:107], v[184:187], v[56:59]\n\t" \
  "ds_read_b64_tr_b16 v[136:137], %[ad2] offset:28672\n\t" \
  "ds_read_b64_tr_b16 v[138:139], %[ad2] offset:29184\n\t" \
  "v_mfma_f32_16x16x32_bf16 v[88:91], v[104:107], v[240:243], v[88:91]\n\t" \
  "v_mfma_f32_16x16x32_bf16 v[60:63], v[108:111], v[184:187], v[60:63]\n\t" \
  "ds_read_b64_tr_b16 v[140:141], %[ad3] offset:28672\n\t" \
  "ds_read_b64_tr_b16 v[142:143], %[ad3] offset:29184\n\t" \
  "v_mfma_f32_16x16x32_bf16 v[92:95], v[108:111], v[240:243], v[92:95]\n\t" \
  "s_waitcnt lgkmcnt(6)\n\t" \
  "v_mfma_f32_16x16x32_bf16 v[32:35], v[112:115], v[188:191], v[32:35]\n\t" \
  "ds_read_b64_tr_b16 v[96:97], %[ad0] offset:32768\n\t" \
  "ds_read_b64_tr_b16 v[98:99], %[ad0] offset:33280\n\t" \
  "v_mfma_f32_16x16x32_bf16 v[64:67], v[112:115], v[244:247], v[64:67]\n\t" \
  "v_mfma_f32_16x16x32_bf16 v[36:39], v[116:119], v[188:191], v[36:39]\n\t" \
  "ds_read_b64_tr_b16 v[100:101], %[ad1] offset:32768\n\t" \
  "ds_read_b64_tr_b16 v[102:103], %[ad1] offset:33280\n\t" \
  "v_mfma_f32_16x16x32_bf16 v[68:71], v[116:119], v[244:247], v[68:71]\n\t" \
  "v_mfma_f32_16x16x32_bf16 v[40:43], v[120:123], v[188:191], v[40:43]\n\t" \
  "ds_read_b64_tr_b16 v[104:105], %[ad2] offset:32768\n\t" \
  "ds_read_b64_tr_b16 v[106:107], %[ad2] offset:33280\n\t" \
  "v_mfma_f32_16x16x32_bf16 v[72:75], v[120:123], v[244:247], v[72:75]\n\t" \
  "v_mfma_f32_16x16x32_bf16 v[44:47], v[124:127], v[188:191], v[44:47]\n\t" \
  "ds_read_b64_tr_b16 v[108:109], %[ad3] offset:32768\n\t" \
  "ds_read_b64_tr_b16 v[110:111], %[ad3] offset:33280\n\t" \
  "v_mfma_f32_16x16x32_bf16 v[76:79], v[124:127], v[244:247], v[76:79]\n\t" \
  "s_waitcnt lgkmcnt(6)\n\t" \
  "v_mfma_f32_16x16x32_bf16 v[48:51], v[128:131], v[188:191], v[48:51]\n\t" \
  "ds_read_b64_tr_b16 v[112:113], %[ad0] offset:40960\n\t" \
  "ds_read_b64_tr_b16 v[114:115], %[ad0] offset:41472\n\t" \
  "v_mfma_f32_16x16x32_bf16 v[80:83], v[128:131], v[244:247], v[80:83]\n\t" \
  "v_mfma_f32_16x16x32_bf16 v[52:55], v[132:135], v[188:191], v[52:55]\n\t" \
  "ds_read_b64_tr_b16 v[116:117], %[ad1] offset:40960\n\t" \
  "ds_read_b64_tr_b16 v[118:119], %[ad1] offset:41472\n\t" \
  "v_mfma_f32_16x16x32_bf16 v[84:87], v[132:135], v[244:247], v[84:87]\n\t" \
  "v_mfma_f32_16x16x32_bf16 v[56:59], v[136:139], v[188:191], v[56:59]\n\t" \
  "ds_read_b64_tr_b16 v[120:121], %[ad2] offset:40960\n\t" \
  "ds_read_b64_tr_b16 v[122:123], %[ad2] offset:41472\n\t" \
  "v_mfma_f32_16x16x32_bf16 v[88:91], v[136:139], v[244:247], v[88:91]\n\t" \
  "v_mfma_f32_16x16x32_bf16 v[60:63], v[140:143], v[188:191], v[60:63]\n\t" \
  "ds_read_b64_tr_b16 v[124:125], %[ad3] offset:40960\n\t" \
  "ds_read_b64_tr_b16 v[126:127], %[ad3] offset:41472\n\t" \
  "v_mfma_f32_16x16x32_bf16 v[92:95], v[140:143], v[244:247], v[92:95]\n\t" \
  "s_waitcnt lgkmcnt(6)\n\t" \
  "v_mfma_f32_16x16x32_bf16 v[32:35], v[96:99], v[192:195], v[32:35]\n\t" \
  "ds_read_b64_tr_b16 v[128:129], %[ad0] offset:36864\n\t" \
  "ds_read_b64_tr_b16 v[130:131], %[ad0] offset:37376\n\t" \
  "v_mfma_f32_16x16x32_bf16 v[64:67], v[96:99], v[248:251], v[64:67]\n\t" \
  "v_mfma_f32_16x16x32_bf16 v[36:39], v[100:103], v[192:195], v[36:39]\n\t" \
  "ds_read_b64_tr_b16 v[132:133], %[ad1] offset:36864\n\t" \
  "ds_read_b64_tr_b16 v[134:135], %[ad1] offset:37376\n\t" \
  "v_mfma_f32_16x16x32_bf16 v[68:71], v[100:103], v[248:251], v[68:71]\n\t" \
  "v_mfma_f32_16x16x32_bf16 v[40:43], v[104:107], v[192:195], v[40:43]\n\t" \
  "ds_read_b64_tr_b16 v[136:137], %[ad2] offset:36864\n\t" \
  "ds_read_b64_tr_b16 v[138:139], %[ad2] offset:37376\n\t" \
  "v_mfma_f32_16x16x32_bf16 v[72:75], v[104:107], v[248:251], v[72:75]\n\t" \
  "v_mfma_f32_16x16x32_bf16 v[44:47], v[108:111], v[192:195], v[44:47]\n\t" \
  "ds_read_b64_tr_b16 v[140:141], %[ad3] offset:36864\n\t" \
  "ds_read_b64_tr_b16 v[142:143], %[ad3] offset:37376\n\t" \
  "v_mfma_f32_16x16x32_bf16 v[76:79], v[108:111], v[248:251], v[76:79]\n\t" \
  "s_waitcnt lgkmcnt(6)\n\t" \
  "v_mfma_f32_16x16x32_bf16 v[48:51], v[112:115], v[192:195], v[48:51]\n\t" \
  "ds_read_b64_tr_b16 v[96:97], %[ad0] offset:45056\n\t" \
  "ds_read_b64_tr_b16 v[98:99], %[ad0] offset:45568\n\t" \
  "v_mfma_f32_16x16x32_bf16 v[80:83], v[112:115], v[248:251], v[80:83]\n\t" \
  "v_mfma_f32_16x16x32_bf16 v[52:55], v[116:119], v[192:195], v[52:55]\n\t" \
  "ds_read_b64_tr_b16 v[100:101], %[ad1] offset:45056\n\t" \
  "ds_read_b64_tr_b16 v[102:103], %[ad1] offset:45568\n\t" \
  "v_mfma_f32_16x16x32_bf16 v[84:87], v[116:119], v[248:251], v[84:87]\n\t" \
  "v_mfma_f32_16x16x32_bf16 v[56:59], v[120:123], v[192:195], v[56:59]\n\t" \
  "ds_read_b64_tr_b16 v[104:105], %[ad2] offset:45056\n\t" \
  "ds_read_b64_tr_b16 v[106:107], %[ad2] offset:45568\n\t" \
  "v_mfma_f32_16x16x32_bf16 v[88:91], v[120:123], v[248:251], v[88:91]\n\t" \
  "v_mfma_f32_16x16x32_bf16 v[60:63], v[124:127], v[192:195], v[60:63]\n\t" \
  "ds_read_b64_tr_b16 v[108:109], %[ad3] offset:45056\n\t" \
  "ds_read_b64_tr_b16 v[110:111], %[ad3] offset:45568\n\t" \
  "v_mfma_f32_16x16x32_bf16 v[92:95], v[124:127], v[248:251], v[92:95]\n\t" \
  "s_waitcnt lgkmcnt(8)\n\t" \
  "v_mfma_f32_16x16x32_bf16 v[32:35], v[128:131], v[196:199], v[32:35]\n\t" \
  "v_mfma_f32_16x16x32_bf16 v[64:67], v[128:131], v[252:255], v[64:67]\n\t" \
  "v_mfma_f32_16x16x32_bf16 v[36:39], v[132:135], v[196:199], v[36:39]\n\t" \
  "v_mfma_f32_16x16x32_bf16 v[68:71], v[132:135], v[252:255], v[68:71]\n\t" \
  "v_mfma_f32_16x16x32_bf16 v[40:43], v[136:139], v[196:199], v[40:43]\n\t" \
  "v_mfma_f32_16x16x32_bf16 v[72:75], v[136:139], v[252:255], v[72:75]\n\t" \
  "v_mfma_f32_16x16x32_bf16 v[44:47], v[140:143], v[196:199], v[44:47]\n\t" \
  "v_mfma_f32_16x16x32_bf16 v[76:79], v[140:143], v[252:255], v[76:79]\n\t" \
  "s_waitcnt lgkmcnt(0)\n\t" \
  "v_mfma_f32_16x16x32_bf16 v[48:51], v[96:99], v[196:199], v[48:51]\n\t" \
  "v_mfma_f32_16x16x32_bf16 v[80:83], v[96:99], v[252:255], v[80:83]\n\t" \
  "v_mfma_f32_16x16x32_bf16 v[52:55], v[100:103], v[196:199], v[52:55]\n\t" \
  "v_mfma_f32_16x16x32_bf16 v[84:87], v[100:103], v[252:255], v[84:87]\n\t" \
  "v_mfma_f32_16x16x32_bf16 v[56:59], v[104:107], v[196:199], v[56:59]\n\t" \
  "v_mfma_f32_16x16x32_bf16 v[88:91], v[104:107], v[252:255], v[88:91]\n\t" \
  "v_mfma_f32_16x16x32_bf16 v[60:63], v[108:111], v[196:199], v[60:63]\n\t" \
  "v_mfma_f32_16x16x32_bf16 v[92:95], v[108:111], v[252:255], v[92:95]\n\t" \
  "v_subrev_u32 %[ad0], 0x10000, %[ad0]\n\t" \
  "v_subrev_u32 %[ad1], 0x10000, %[ad1]\n\t" \
  "v_subrev_u32 %[ad2], 0x10000, %[ad2]\n\t" \
  "v_subrev_u32 %[ad3], 0x10000, %[ad3]\n\t" \
  "s_nop 7\n\t" \
  "s_nop 7\n\t"

#define K2_ROUND_STMT(ASM) \
  asm volatile(ASM \
    : "=&{v[32:47]}"(A0), "=&{v[48:63]}"(A1), "=&{v[64:79]}"(A2), "=&{v[80:95]}"(A3), \
      "+{v[144:159]}"(X0), "+{v[160:175]}"(X1), "+{v[176:191]}"(X2), "+{v[192:207]}"(X3), "+{v[208:223]}"(X4), "+{v[224:239]}"(X5), "+{v[240:255]}"(X6), \
      [ad0] "+v"(ad0), [ad1] "+v"(ad1), [ad2] "+v"(ad2), [ad3] "+v"(ad3), [vt] "=&v"(vt) \
    : [voff] "v"(voff), [lo0] "v"(lo0), [lo1] "v"(lo1), [cnt] "v"(cnt), [one] "v"(one), \
      [gblo] "s"((unsigned)(size_t)gb), [gbhi] "s"((unsigned)((size_t)gb >> 32)), [ublo] "s"((unsigned)(size_t)ub), [ubhi] "s"((unsigned)((size_t)ub >> 32)), \
      [xslo] "s"((unsigned)(size_t)xs), [xshi] "s"((unsigned)((size_t)xs >> 32)), [ldo] "s"(ldo) \
    : "v96", "v97", "v98", "v99", "v100", "v101", "v102", "v103", "v104", "v105", "v106", "v107", "v108", "v109", "v110", "v111", "v112", "v113", "v114", "v115", "v116", "v117", "v118", "v119", "v120", "v121", "v122", "v123", "v124", "v125", "v126", "v127", "v128", "v129", "v130", "v131", "v132", "v133", "v134", "v135", "v136", "v137", "v138", "v139", "v140", "v141", "v142", "v143", "s60", "s61", "s62", "s63", "s64", "s65", "s66", "s67", "scc", "memory")

DI void k2_epilogue(const f32x16& G0, const f32x16& U0, const f32x16& G1, const f32x16& U1, char* ws, unsigned hoff) {
#pragma unroll
  for (int rbi = 0; rbi < 2; ++rbi)
#pragma unroll
    for (int p = 0; p < 2; ++p) {
      u32x4 hv;
#pragma unroll
      for (int hlf = 0; hlf < 2; ++hlf) {
        const int o = (2 * p + hlf) * 4;
        const f32x16& gg = rbi ? G1 : G0; const f32x16& uu = rbi ? U1 : U0;
        unsigned g01 = pack_bf16(gg[o], gg[o + 1]), g23 = pack_bf16(gg[o + 2], gg[o + 3]);
        unsigned u01 = pack_bf16(uu[o], uu[o + 1]), u23 = pack_bf16(uu[o + 2], uu[o + 3]);
        unsigned e01 = pack_bf16(gelu_tanh(bf_lo(g01)), gelu_tanh(bf_hi(g01)));
        unsigned e23 = pack_bf16(gelu_tanh(bf_lo(g23)), gelu_tanh(bf_hi(g23)));
        hv[2 * hlf] = pack_bf16(bf_lo(e01) * bf_lo(u01), bf_hi(e01) * bf_hi(u01));
        hv[2 * hlf + 1] = pack_bf16(bf_lo(e23) * bf_lo(u23), bf_hi(e23) * bf_hi(u23));
      }
      st16_sc1(ws + OFF_H, hoff + (unsigned)((p * 128 + rbi) * 1024), hv);
    }
}

#define K2_SETUP() \
  const int l = lane_id(); \
  const int g = l >> 4, q = (l & 15) >> 2, pp = l & 3; \
  const int cx = 2 * ((q >> 1) + 2 * (g & 1)); \
  const unsigned lbase = (unsigned)(size_t)smem + (8 * g + q) * 128 + 8 * (pp & 1); \
  unsigned ad0 = lbase + ((0 ^ cx) + (pp >> 1)) * 16, ad1 = lbase + ((2 ^ cx) + (pp >> 1)) * 16; \
  unsigned ad2 = lbase + ((4 ^ cx) + (pp >> 1)) * 16, ad3 = lbase + ((6 ^ cx) + (pp >> 1)) * 16; \
  const int lr = l >> 3, lp = l & 7; \
  const unsigned lo0 = lr * (FF_ * 2) + ((lp ^ (2 * ((lr >> 1) & 1))) * 16), lo1 = lo0 ^ 64u; \
  const char* gb = (const char*)(wg + 64 * nt) + (size_t)(16 * w4) * (FF_ * 2); \
  const char* ub = (const char*)(wu + 64 * nt) + (size_t)(16 * w4) * (FF_ * 2); \
  const char* xs = ws + OFF_XNF + (size_t)rb16 * 14 * 1024; \
  const unsigned ldo = (unsigned)(size_t)smem + 16 * w4 * 128, voff = l * 16, cnt = cntG, one = 1u; \
  const unsigned hoff = (unsigned)(((2 * nt) * 128 + rb16) * 1024) + voff; \
  unsigned vt; \
  f32x16 A0, A1, A2, A3;

DI void k2_gemm_first(const unsigned short* wg, const unsigned short* wu, char* smem, char* ws, int rb16, int nt, int w4, unsigned cntG) {
  K2_SETUP();
  f32x16 X0, X1, X2, X3, X4, X5, X6;
  asm volatile("" : "={v[144:159]}"(X0), "={v[160:175]}"(X1), "={v[176:191]}"(X2), "={v[192:207]}"(X3), "={v[208:223]}"(X4), "={v[224:239]}"(X5), "={v[240:255]}"(X6));
  K2_ROUND_STMT(K2_ROUND0S_ASM);
  STAMP(ws, BIDX * 8 + 4 + w4, 20);
  k2_epilogue(A0, A1, A2, A3, ws, hoff);
  STAMP(ws, BIDX * 8 + 4 + w4, 21);
  asm volatile("s_waitcnt vmcnt(4)" ::: "memory");
  K2_ROUND_STMT(K2_ROUND1_ASM);
  STAMP(ws, BIDX * 8 + 4 + w4, 22);
  k2_epilogue(A0, A1, A2, A3, ws, hoff + 8 * 1024);
  STAMP(ws, BIDX * 8 + 4 + w4, 23);
}

__global__ __launch_bounds__(512, 2) void k2_gemm1_colfft(const unsigned short* __restrict__ wg, const unsigned short* __restrict__ wu,
                                                          const unsigned short* __restrict__ wd, char* __restrict__ ws) {
  extern __shared__ __attribute__((aligned(16))) char smem[];
  const int b = BIDX, tid = TIDX, l = tid & 63;
  const int w = __builtin_amdgcn_readfirstlane(tid >> 6);
  const bool has_gemm = b < 224;
  const int xcd = b & 7, jj = b >> 3;
  const int mt = 4 * (xcd >> 2) + (jj & 3);
  const int nt = has_gemm ? 7 * (xcd & 3) + (jj >> 2) : 0;
  const int rb16 = 16 * mt + 2 * (w & 3);
  STAMP(ws, BIDX * 8 + (TIDX >> 6), 0);
  const unsigned cntG = (unsigned)(size_t)smem + K2_WLDS + 2 * K2_FBUF, cntF = cntG + 16;
  if (tid == 0) { *(lds_u32*)(size_t)cntG = 0u; *(lds_u32*)(size_t)cntF = 0u; }
  WG_SYNC();
  if (w >= 4) {
    if (has_gemm) {
      k2_gemm_first(wg, wu, smem, ws, rb16, nt, w - 4, cntG);
    }
  } else {
    if (DBG_FFTDELAY > 0) { for (int i = 0; i < DBG_FFTDELAY; ++i) __builtin_amdgcn_s_sleep(16); }
    __builtin_amdgcn_s_setprio(DBG_PRIO);
    const int t = tid, k2 = has_gemm ? xcd * 28 + jj : 224;
    cf* A = (cf*)(smem + K2_WLDS);
    cf* B = (cf*)(smem + K2_WLDS + K2_FBUF);
    cf a[8];
    const cf* s1 = (const cf*)(ws + OFF_S1) + ((size_t)(t >> 3) * DF_ + k2) * 8 + (t & 7);
#pragma unroll
    for (int j = 0; j < 8; ++j) a[j] = s1[(size_t)j * 32 * DF_ * 8];
    const int q1 = t >> 5, u = t & 31, q2 = (t >> 2) & 7, v = t & 3;
    cf T1[7], T2[7], T3[7];
#pragma unroll
    for (int q = 1; q < 8; ++q) { T1[q - 1] = T2A[(q - 1) * 256 + t]; T2[q - 1] = T2B[(q - 1) * 32 + u]; T3[q - 1] = T2C[(q - 1) * 4 + v]; }
    const u32x4* wp = (const u32x4*)(ws + OFF_WP + ((size_t)k2 * 256 + t) * 32);
    const u32x4 w0 = wp[0], w1 = wp[1];
    dft8<false>(a);
#pragma unroll
    for (int q = 1; q < 8; ++q) a[q] = cmul(a[q], T1[q - 1]);
#pragma unroll
    for (int q = 0; q < 8; ++q) A[padi(q * 256 + t)] = a[q];
    STAMP(ws, BIDX * 8 + (TIDX >> 6), 1);
    sub_sync(cntF, 4, true);
    STAMP(ws, BIDX * 8 + (TIDX >> 6), 2);
#pragma unroll
    for (int j = 0; j < 8; ++j) a[j] = A[padi(q1 * 256 + u + 32 * j)];
    dft8<false>(a);
#pragma unroll
    for (int q = 1; q < 8; ++q) a[q] = cmul(a[q], T2[q - 1]);
#pragma unroll
    for (int q = 0; q < 8; ++q) B[padi(q1 * 256 + q * 32 + u)] = a[q];
    STAMP(ws, BIDX * 8 + (TIDX >> 6), 3);
    sub_sync(cntF, 8, true);
    STAMP(ws, BIDX * 8 + (TIDX >> 6), 4);
#pragma unroll
    for (int j = 0; j < 8; ++j) a[j] = B[padi(q1 * 256 + q2 * 32 + v + 4 * j)];
    dft8<false>(a);
#pragma unroll
    for (int q = 1; q < 8; ++q) a[q] = cmul(a[q], T3[q - 1]);
#pragma unroll
    for (int q = 0; q < 8; ++q) A[padi(q1 * 256 + q2 * 32 + q * 4 + v)] = a[q];
    STAMP(ws, BIDX * 8 + (TIDX >> 6), 5);
    sub_sync(cntF, 12, true);
    STAMP(ws, BIDX * 8 + (TIDX >> 6), 6);
#pragma unroll
    for (int e = 0; e < 8; ++e) a[e] = A[padi(8 * t + e)];
    dft4<false>(a[0], a[1], a[2], a[3]);
    dft4<false>(a[4], a[5], a[6], a[7]);
    {
      const float SC = 1.0f / (2.0f * 2048.0f * 448.0f);
#pragma unroll
      for (int e = 0; e < 8; ++e) {
        const unsigned pk = e < 4 ? w0[e] : w1[e - 4];
        cf ww = mkc(bf_lo(pk) * SC, bf_hi(pk) * SC);
        a[e] = cmul(a[e], ww);
      }
    }
    dft4<true>(a[0], a[1], a[2], a[3]);
    dft4<true>(a[4], a[5], a[6], a[7]);
#pragma unroll
    for (int e = 0; e < 8; ++e) B[padi(8 * t + e)] = a[e];
    STAMP(ws, BIDX * 8 + (TIDX >> 6), 7);
    sub_sync(cntF, 16, true);
    STAMP(ws, BIDX * 8 + (TIDX >> 6), 8);
#pragma unroll
    for (int q = 0; q < 8; ++q) a[q] = B[padi(q1 * 256 + q2 * 32 + q * 4 + v)];
#pragma unroll
    for (int q = 1; q < 8; ++q) a[q] = cmulc(a[q], T3[q - 1]);
    dft8<true>(a);
#pragma unroll
    for (int j = 0; j < 8; ++j) A[padi(q1 * 256 + q2 * 32 + v + 4 * j)] = a[j];
    STAMP(ws, BIDX * 8 + (TIDX >> 6), 9);
    sub_sync(cntF, 20, true);
    STAMP(ws, BIDX * 8 + (TIDX >> 6), 10);
#pragma unroll
    for (int q = 0; q < 8; ++q) a[q] = A[padi(q1 * 256 + q * 32 + u)];
#pragma unroll
    for (int q = 1; q < 8; ++q) a[q] = cmulc(a[q], T2[q - 1]);
    dft8<true>(a);
#pragma unroll
    for (int j = 0; j < 8; ++j) B[padi(q1 * 256 + u + 32 * j)] = a[j];
    STAMP(ws, BIDX * 8 + (TIDX >> 6), 11);
    sub_sync(cntF, 24, true);
    STAMP(ws, BIDX * 8 + (TIDX >> 6), 12);
#pragma unroll
    for (int q = 0; q < 8; ++q) a[q] = B[padi(q * 256 + t)];
#pragma unroll
    for (int q = 1; q < 8; ++q) a[q] = cmulc(a[q], T1[q - 1]);
    dft8<true>(a);
    cf* s2 = (cf*)(ws + OFF_S2) + ((size_t)(t >> 3) * DF_ + k2) * 8 + (t & 7);
#pragma unroll
    for (int j = 0; j < 8; ++j) s2[(size_t)j * 32 * DF_ * 8] = a[j];
    STAMP(ws, BIDX * 8 + (TIDX >> 6), 13);
    if (has_gemm)
      {
      const int l = lane_id();
      const int g = l >> 4, r = l & 15;
      const int fw = (xcd * 28 + jj) * 4 + w;
      unsigned short v[2][8];
#pragma unroll
      for (int i = 0; i < 2; ++i) {
        const int fi = fw + 896 * i;
        const int fc = fi < 28 * 56 ? fi : 0;
        const int db = fc % 28, kp = fc / 28;
        const unsigned short* src = wd + (size_t)(32 * kp + 4 * g) * D_ + 16 * db + r;
#pragma unroll
        for (int j = 0; j < 8; ++j) v[i][j] = src[((j & 3) + 16 * (j >> 2)) * D_];
      }
#pragma unroll
      for (int i = 0; i < 2; ++i) {
        const int fi = fw + 896 * i;
        if (fi < 28 * 56)
          *(u32x4*)(ws + OFF_WDF + (size_t)fi * 1024 + l * 16) =
              u32x4{v[i][0] | ((unsigned)v[i][1] << 16), v[i][2] | ((unsigned)v[i][3] << 16), v[i][4] | ((unsigned)v[i][5] << 16), v[i][6] | ((unsigned)v[i][7] << 16)};
      }
      }
    return;
  }
}

constexpr int K3_LDS = 48 * 1024;
__global__ __launch_bounds__(256, 2) void k3_gemm2_rowifft(const unsigned short* __restrict__ x, char* __restrict__ ws,
                                                           unsigned short* __restrict__ out) {
  __shared__ __attribute__((aligned(16))) char smem[K3_LDS];
  const int b = BIDX, tid = TIDX, l = tid & 63;
  const int w = __builtin_amdgcn_readfirstlane(tid >> 6);
  unsigned* cnt = (unsigned*)(ws + OFF_CNT);
  STAMP(ws, 2048 + BIDX * 8 + (TIDX >> 6), 0);
  if (b < 256) {
    __builtin_amdgcn_s_setprio(DBG_PRIO);
    const int rb8 = (b & 7) * 32 + (b >> 3);
    const int rowa = rb8 * 8 + 2 * w;
    char* buf = smem + w * 4608;
    const float* s2 = (const float*)(ws + OFF_S2) + ((size_t)rb8 * DF_ * 8 + 2 * w) * 2;
    f32x4 in[4];
#pragma unroll
    for (int i = 0; i < 4; ++i) {
      const int k = l + 64 * i;
      in[i] = f32x4{0.f, 0.f, 0.f, 0.f};
      if (k <= 224) in[i] = *(const f32x4*)(s2 + (size_t)k * 16);
    }
    u32x4 xa = {0, 0, 0, 0}, xb = {0, 0, 0, 0};
    if (l < 56) {
      xa = *(const u32x4*)(x + (size_t)rowa * D_ + 8 * l);
      xb = *(const u32x4*)(x + (size_t)(rowa + 1) * D_ + 8 * l);
    }
    cf t1[6], t2[7];
    load_tw448(t1, t2, l);
    cf* zb = (cf*)buf;
#pragma unroll
    for (int i = 0; i < 4; ++i) {
      const int k = l + 64 * i;
      if (k <= 224) {
        const f32x4 v = in[i];
        if (k == 0 || k == 224) {
          zb[k] = mkc(v[0], v[2]);
        } else {
          zb[k] = mkc(v[0] - v[3], v[1] + v[2]);
          zb[448 - k] = mkc(v[0] + v[3], v[2] - v[1]);
        }
      }
    }
    WAVE_FENCE();
    cf z[7], o[8];
#pragma unroll
    for (int j = 0; j < 7; ++j) z[j] = zb[l + 64 * j];
    WAVE_FENCE();
    fft448<true>(z, o, t1, t2, buf, l);
    if (l < 56) {
      const int q = l >> 3, bb = l & 7;
#pragma unroll
      for (int a = 0; a < 8; ++a) zb[7 * (bb + 8 * a) + q] = o[a];
    }
    WAVE_FENCE();
    if (l < 56) {
      u32x4 ya, yb;
#pragma unroll
      for (int j = 0; j < 4; ++j) {
        f32x4 v = *(const f32x4*)(buf + l * 64 + j * 16);
        unsigned pa = pack_bf16(v[0], v[2]), pb = pack_bf16(v[1], v[3]);
        ya[j] = pack_bf16(bf_lo(xa[j]) + bf_lo(pa), bf_hi(xa[j]) + bf_hi(pa));
        yb[j] = pack_bf16(bf_lo(xb[j]) + bf_lo(pb), bf_hi(xb[j]) + bf_hi(pb));
      }
      char* ybase = ws + OFF_Y;
      const unsigned o0 = (unsigned)(rowa * D_ * 2 + l * 16);
      asm volatile("global_store_dwordx4 %0, %1, %2 sc1\n\ts_nop 1" :: "v"(o0), "v"(ya), "s"(ybase) : "memory");
      asm volatile("global_store_dwordx4 %0, %1, %2 sc1\n\ts_nop 1" :: "v"(o0 + D_ * 2), "v"(yb), "s"(ybase) : "memory");
    }
    STAMP(ws, 2048 + BIDX * 8 + (TIDX >> 6), 1);
    asm volatile("s_waitcnt vmcnt(0)" ::: "memory");
    WG_SYNC();
    STAMP(ws, 2048 + BIDX * 8 + (TIDX >> 6), 2);
    if (tid == 0) __hip_atomic_fetch_add(&cnt[(rb8 >> 3) * 32], 1u, __ATOMIC_RELAXED, __HIP_MEMORY_SCOPE_AGENT);
    return;
  }
  const int gi = b - 256;
  const int xcd = gi & 7, jj = gi >> 3;
  const int mt = 4 * xcd + (jj & 3), nt = jj >> 2;
  const char* hsrc = ws + OFF_H + ((size_t)(14 * w) * 128 + 4 * mt) * 1024;
  const char* wsrc = ws + OFF_WDF + ((size_t)(14 * w) * 28 + 4 * nt) * 1024;
  const unsigned loff = l * 16;
  f32x4 acc[4][4];
#pragma unroll
  for (int i = 0; i < 4; ++i)
#pragma unroll
    for (int j = 0; j < 4; ++j) acc[i][j] = f32x4{0.f, 0.f, 0.f, 0.f};
  constexpr int PD = 3;
  bf16x8 hf[PD + 1][4], wf[PD + 1][4];
#pragma unroll
  for (int kp = 0; kp < PD; ++kp)
#pragma unroll
    for (int i = 0; i < 4; ++i) {
      wf[kp][i] = *(const bf16x8*)((wsrc + (kp * 28 + i) * 1024) + loff);
      hf[kp][i] = *(const bf16x8*)((hsrc + (kp * 128 + i) * 1024) + loff);
    }
  unsigned dummy[6];
  if (DBG_K3WARM) {
    const int sl = jj * 4 + w;
    const char* hw = ws + OFF_H + ((size_t)(sl >> 1) * 128 + 16 * xcd) * 1024 + (sl & 1) * 8192;
    const char* ww = ws + OFF_WDF + (size_t)sl * 14336;
    const unsigned lo = l * 64;
    __builtin_amdgcn_sched_barrier(0);
    asm volatile("global_load_dword %0, %1, %2" : "=v"(dummy[0]) : "v"(lo), "s"(hw) : "memory");
    asm volatile("global_load_dword %0, %1, %2" : "=v"(dummy[1]) : "v"(lo + 4096), "s"(hw) : "memory");
    asm volatile("global_load_dword %0, %1, %2" : "=v"(dummy[2]) : "v"(lo), "s"(ww) : "memory");
    asm volatile("global_load_dword %0, %1, %2" : "=v"(dummy[3]) : "v"(lo + 4096), "s"(ww) : "memory");
    asm volatile("global_load_dword %0, %1, %2" : "=v"(dummy[4]) : "v"(lo + 8192), "s"(ww) : "memory");
    asm volatile("global_load_dword %0, %1, %2" : "=v"(dummy[5]) : "v"(lo < 2048 ? lo + 12288 : lo), "s"(ww) : "memory");
  }
  bool ready = false;
#pragma unroll
  for (int kp = 0; kp < 14; ++kp) {
    __builtin_amdgcn_sched_barrier(0);
    if (kp + PD < 14) {
#pragma unroll
      for (int i = 0; i < 4; ++i) {
        wf[(kp + PD) % (PD + 1)][i] = *(const bf16x8*)((wsrc + ((kp + PD) * 28 + i) * 1024) + loff);
        hf[(kp + PD) % (PD + 1)][i] = *(const bf16x8*)((hsrc + ((kp + PD) * 128 + i) * 1024) + loff);
      }
    }
    if (kp == 10 && w == 0) {
      if (__hip_atomic_load(&cnt[mt * 32], __ATOMIC_RELAXED, __HIP_MEMORY_SCOPE_AGENT) >= 8u) {
        ready = true;
        __builtin_amdgcn_fence(__ATOMIC_ACQUIRE, "agent");
      }
    }
    __builtin_amdgcn_sched_barrier(0);
#pragma unroll
    for (int di = 0; di < 4; ++di)
#pragma unroll
      for (int ri = 0; ri < 4; ++ri)
        acc[di][ri] = __builtin_amdgcn_mfma_f32_16x16x32_bf16(wf[kp % (PD + 1)][di], hf[kp % (PD + 1)][ri], acc[di][ri], 0, 0, 0);
  }
  if (DBG_K3WARM) { asm volatile("s_waitcnt vmcnt(0)" ::: "memory"); asm volatile("" :: "v"(dummy[0]), "v"(dummy[1]), "v"(dummy[2]), "v"(dummy[3]), "v"(dummy[4]), "v"(dummy[5])); }
  STAMP(ws, 2048 + BIDX * 8 + (TIDX >> 6), 1);
#pragma unroll
  for (int ri = 0; ri < 4; ++ri) {
    if (ri != w) {
      const int slot = (w - ri - 1) & 3;
#pragma unroll
      for (int di = 0; di < 4; ++di) *(f32x4*)(smem + ((ri * 3 + slot) * 4 + di) * 1024 + l * 16) = acc[di][ri];
    }
  }
  STAMP(ws, 2048 + BIDX * 8 + (TIDX >> 6), 2);
  if (w == 0) {
    if (!ready) {
      for (unsigned it = 0; it < (1u << 22) && __hip_atomic_load(&cnt[mt * 32], __ATOMIC_RELAXED, __HIP_MEMORY_SCOPE_AGENT) < 8u; ++it) __builtin_amdgcn_s_sleep(2);
      __builtin_amdgcn_fence(__ATOMIC_ACQUIRE, "agent");
    }
    asm volatile("s_waitcnt vmcnt(0)" ::: "memory");
  }
  WG_SYNC();
  STAMP(ws, 2048 + BIDX * 8 + (TIDX >> 6), 3);
  const int row = 64 * mt + 16 * w + (l & 15);
  const int d0 = 64 * nt + 4 * (l >> 4);
  u32x2 yv[4];
#pragma unroll
  for (int di = 0; di < 4; ++di) yv[di] = *(const u32x2*)(ws + OFF_Y + ((size_t)row * D_ + d0 + 16 * di) * 2);
  f32x4 fin[4];
#pragma unroll
  for (int di = 0; di < 4; ++di) {
    f32x4 v = w == 0 ? acc[di][0] : w == 1 ? acc[di][1] : w == 2 ? acc[di][2] : acc[di][3];
#pragma unroll
    for (int slot = 0; slot < 3; ++slot) v += *(const f32x4*)(smem + ((w * 3 + slot) * 4 + di) * 1024 + l * 16);
    fin[di] = v;
  }
#pragma unroll
  for (int di = 0; di < 4; ++di) {
    const size_t off = (size_t)row * D_ + d0 + 16 * di;
    const unsigned b01 = pack_bf16(fin[di][0], fin[di][1]), b23 = pack_bf16(fin[di][2], fin[di][3]);
    u32x2 ov;
    ov[0] = pack_bf16(bf_lo(yv[di][0]) + bf_lo(b01), bf_hi(yv[di][0]) + bf_hi(b01));
    ov[1] = pack_bf16(bf_lo(yv[di][1]) + bf_lo(b23), bf_hi(yv[di][1]) + bf_hi(b23));
    *(u32x2*)(out + off) = ov;
  }
  STAMP(ws, 2048 + BIDX * 8 + (TIDX >> 6), 4);
}

#if defined(__HIP_DEVICE_COMPILE__)
#pragma clang attribute pop
#endif
extern "C" void kernel_launch(void* const* d_in, const int* in_sizes, int n_in, void* d_out, int out_size, void* d_ws,
                              size_t ws_size, hipStream_t stream) {
  const unsigned short* x = (const unsigned short*)d_in[0];
  const unsigned short* wr = (const unsigned short*)d_in[1];
  const unsigned short* wi = (const unsigned short*)d_in[2];
  const unsigned short* wg = (const unsigned short*)d_in[4];
  const unsigned short* wu = (const unsigned short*)d_in[5];
  const unsigned short* wd = (const unsigned short*)d_in[6];
  char* ws = (char*)d_ws;
  (void)hipFuncSetAttribute((const void*)k2_gemm1_colfft, hipFuncAttributeMaxDynamicSharedMemorySize, K2_LDS);
  k1_ln_rowfft<<<256, 512, 0, stream>>>(x, wr, wi, wg, wu, wd, ws);
  k2_gemm1_colfft<<<225, 512, K2_LDS, stream>>>(wg, wu, wd, ws);
  k3_gemm2_rowifft<<<480, 256, 0, stream>>>(x, ws, (unsigned short*)d_out);
}
```
